```python
import torch

def _softmax(x, axis=-1):
    return torch.softmax(x, dim=axis)
def _gelu(x):
    return torch.nn.functional.gelu(x, approximate="tanh")
def _silu(x):
    return torch.nn.functional.silu(x)
def _sigmoid(x):
    return torch.sigmoid(x)
def _layernorm(x, axis=-1, eps=1e-5):
    m = x.mean(dim=axis, keepdim=True)
    v = x.var(dim=axis, keepdim=True, unbiased=False)
    return (x - m) / torch.sqrt(v + eps)
def _scatter_add(tgt, idx, src, axis=0):
    return tgt.clone().index_add_(axis, idx, src)
def _segment_sum(src, seg, n):
    out = torch.zeros((n,) + src.shape[1:], dtype=src.dtype)
    return out.index_add_(0, seg, src)
def _pad(x, pw):
    flat = tuple(v for pair in reversed(pw) for v in pair)
    return torch.nn.functional.pad(x, flat)

kernel_name = 'standalone__gqa_softmax__near_ridge'

def setup_inputs():

    _w = (lambda a: (lambda t: t.to(torch.bfloat16) if t.is_floating_point() else t)(torch.as_tensor(a)))
    B, Hq, Hkv, Sq, Sk, D = 16, 32, 4, 384, 384, 128
    return {
        "q": _w((lambda *s: torch.randn(*s, dtype=torch.bfloat16) * 0.8)(B, Hq, Sq, D)),
        "k": _w((lambda *s: torch.randn(*s, dtype=torch.bfloat16) * 0.8)(B, Hkv, Sk, D)),
        "v": _w((lambda *s: torch.randn(*s, dtype=torch.bfloat16) * 0.05)(B, Hkv, Sk, D)),
    }

def reference(q, k, v):
    B, Hq, Sq, D = q.shape
    Hkv = k.shape[1]
    rep = Hq // Hkv
    k = (lambda x, n, axis: torch.repeat_interleave(x, n, dim=axis))(k, rep, axis=1)
    v = (lambda x, n, axis: torch.repeat_interleave(x, n, dim=axis))(v, rep, axis=1)
    scale = D ** -0.5
    scores = torch.einsum("bhqd,bhkd->bhqk", q, k) * scale
    p = _softmax(scores, axis=-1)
    return torch.einsum("bhqk,bhkd->bhqd", p, v)
```

```cpp
#include <hip/hip_runtime.h>
#include <hip/hip_bf16.h>
#include <cstdio>
using bf16 = __hip_bfloat16;
constexpr int   D = 128, NW = 8, QBLK = 32, KVBLK = 64;
constexpr float SCALE = 0.088388347648318440f;
constexpr float THR = 8.f;
constexpr int SDEPTH = 2;
constexpr int B = 16, HQ = 32, HKV = 4, S = 384, SKV = 384;
constexpr int LDQ = D, LDK = D, LDO = D;
constexpr size_t SHM_V = KVBLK * D * 2, SHM_K = KVBLK * D * 2, SHM_ATTN = 2 * SHM_V + 2 * SHM_K + NW * 64 * 4;
using bf16x8 = __attribute__((ext_vector_type(8))) short;
using s16x4  = __attribute__((ext_vector_type(4))) short;
using f32x16 = __attribute__((ext_vector_type(16))) float;
using f32x8  = __attribute__((ext_vector_type(8))) float;
using u32x4  = __attribute__((ext_vector_type(4))) unsigned;
#define KSWZ(row, colB) ((row) * 256 + ((colB) ^ (((row) & 7) << 4)))
#define SBAR() __builtin_amdgcn_sched_barrier(0)
__device__ __forceinline__ int crow(int r, int hi) { return (r & 3) + 8 * (r >> 2) + 4 * hi; }
__device__ __forceinline__ unsigned cvtpk(float lo, float hi) {
  unsigned r; asm volatile("v_cvt_pk_bf16_f32 %0, %1, %2" : "=v"(r) : "v"(lo), "v"(hi)); return r;
}
__device__ __forceinline__ bf16x8 ld8(const bf16* p) { return *reinterpret_cast<const bf16x8*>(p); }
__device__ __forceinline__ void partialSM(f32x16& p0, f32x16& p1, float& m_reg, float& mn, float& alpha) {
  constexpr float C = SCALE * 1.4426950408889634f;
  float pmax = p0[0]; for (int r = 1; r < 16; ++r) pmax = fmaxf(pmax, p0[r]); for (int r = 0; r < 16; ++r) pmax = fmaxf(pmax, p1[r]);
  { auto rr = __builtin_amdgcn_permlane32_swap(__float_as_uint(pmax), __float_as_uint(pmax), false, false);
    pmax = fmaxf(__uint_as_float(rr[0]), __uint_as_float(rr[1])); }
  if (__builtin_expect(__all(pmax - m_reg <= THR / SCALE), 1)) { mn = m_reg; alpha = 1.f; }
  else { mn = fmaxf(m_reg, pmax); alpha = __builtin_amdgcn_exp2f((m_reg - mn) * C); m_reg = mn; }
  float mnC = -mn * C;
  for (int r = 0; r < 16; ++r) p0[r] = fmaf(p0[r], C, mnC); for (int r = 0; r < 16; ++r) p1[r] = fmaf(p1[r], C, mnC);
  for (int r = 0; r < 16; ++r) p0[r] = __builtin_amdgcn_exp2f(p0[r]);
}
__device__ __forceinline__ void finishSM(f32x16& p0, f32x16& p1, float alpha, float& l_reg, bf16x8& pa0, bf16x8& pa1, bf16x8& pa2, bf16x8& pa3) {
  for (int r = 0; r < 16; ++r) p1[r] = __builtin_amdgcn_exp2f(p1[r]);
  float ps = 0; for (int r = 0; r < 16; ++r) ps += p0[r]; for (int r = 0; r < 16; ++r) ps += p1[r];
  { auto rr = __builtin_amdgcn_permlane32_swap(__float_as_uint(ps), __float_as_uint(ps), false, false);
    ps = __uint_as_float(rr[0]) + __uint_as_float(rr[1]); }
  l_reg = l_reg * alpha + ps;
#define PK4(P, BASE, OUT) do { unsigned a0 = cvtpk(P[BASE + 0], P[BASE + 1]), a1 = cvtpk(P[BASE + 2], P[BASE + 3]);   \
    unsigned b0 = cvtpk(P[BASE + 4], P[BASE + 5]), b1 = cvtpk(P[BASE + 6], P[BASE + 7]);                              \
    auto r0 = __builtin_amdgcn_permlane32_swap(a0, b0, false, false); auto r1 = __builtin_amdgcn_permlane32_swap(a1, b1, false, false); \
    u32x4 w = {r0[0], r1[0], r0[1], r1[1]}; OUT = *reinterpret_cast<bf16x8*>(&w); } while (0)
  PK4(p0, 0, pa0); PK4(p0, 8, pa1); PK4(p1, 0, pa2); PK4(p1, 8, pa3);
#undef PK4
}
__device__ __forceinline__ void qkt(f32x16& p0, f32x16& p1, const bf16* Ks, const bf16x8* qr, int r32, int hi) {
  p0 = f32x16{}; p1 = f32x16{};
  for (int d0 = 0; d0 < 8; ++d0) { int cb = (d0 * 16 + hi * 8) * 2;
    bf16x8 b0 = *reinterpret_cast<const bf16x8*>((const char*)Ks + KSWZ(r32, cb));
    bf16x8 b1 = *reinterpret_cast<const bf16x8*>((const char*)Ks + KSWZ(32 + r32, cb));
    p0 = __builtin_amdgcn_mfma_f32_32x32x16_bf16(b0, qr[d0], p0, 0, 0, 0);
    p1 = __builtin_amdgcn_mfma_f32_32x32x16_bf16(b1, qr[d0], p1, 0, 0, 0); }
}
__device__ __forceinline__ int v_st(int k, int c) { const int kk = (k & ~0xC) | ((k & 4) << 1) | ((k & 8) >> 1); return ((kk >> 3) * 4 + (c >> 5)) * 512 + ((kk & 7) * 32 + (c & 31)) * 2; }
__device__ __forceinline__ int v_rd_base(int lane) { return ((lane & 3) << 3) | (((lane >> 2) & 3) << 6) | (((lane >> 4) & 1) << 5) | (((lane >> 5) & 1) << 8); }
constexpr int v_rd_off(int d0, int ks, int half) { return d0 * 512 + ks * 4096 + half * 2048; }
template <int OFF> __device__ __forceinline__ s16x4 tr_read(int vb) {
  s16x4 r; asm volatile("ds_read_b64_tr_b16 %0, %1 offset:%2" : "=&v"(r) : "v"(vb), "i"(OFF) : "memory"); return r;
}
template <int D0> __device__ __forceinline__ void pv_one(f32x16& od, int vb, bf16x8 pa0, bf16x8 pa1, bf16x8 pa2, bf16x8 pa3) {
  const s16x4 l0 = tr_read<v_rd_off(D0, 0, 0)>(vb), h0 = tr_read<v_rd_off(D0, 0, 1)>(vb), l1 = tr_read<v_rd_off(D0, 1, 0)>(vb), h1 = tr_read<v_rd_off(D0, 1, 1)>(vb);
  const s16x4 l2 = tr_read<v_rd_off(D0, 2, 0)>(vb), h2 = tr_read<v_rd_off(D0, 2, 1)>(vb), l3 = tr_read<v_rd_off(D0, 3, 0)>(vb), h3 = tr_read<v_rd_off(D0, 3, 1)>(vb);
  asm volatile("s_waitcnt lgkmcnt(0)" ::: "memory"); SBAR();
#define PK(L, H) (bf16x8){L[0], L[1], L[2], L[3], H[0], H[1], H[2], H[3]}
  od = __builtin_amdgcn_mfma_f32_32x32x16_bf16(pa0, PK(l0, h0), od, 0, 0, 0);
  od = __builtin_amdgcn_mfma_f32_32x32x16_bf16(pa1, PK(l1, h1), od, 0, 0, 0);
  od = __builtin_amdgcn_mfma_f32_32x32x16_bf16(pa2, PK(l2, h2), od, 0, 0, 0);
  od = __builtin_amdgcn_mfma_f32_32x32x16_bf16(pa3, PK(l3, h3), od, 0, 0, 0);
#undef PK
}
__device__ __forceinline__ void pv_d0(f32x16* o, int vb, bf16x8 pa0, bf16x8 pa1, bf16x8 pa2, bf16x8 pa3) {
  pv_one<0>(o[0], vb, pa0, pa1, pa2, pa3); pv_one<1>(o[1], vb, pa0, pa1, pa2, pa3); pv_one<2>(o[2], vb, pa0, pa1, pa2, pa3); pv_one<3>(o[3], vb, pa0, pa1, pa2, pa3);
}
__device__ __forceinline__ void attn_dense_body(const bf16* __restrict__ Qb, const bf16* __restrict__ Kh, const bf16* __restrict__ Vh,
                                                bf16* __restrict__ Ob, int seq, char* lds) {
  const int tid = threadIdx.x, wid = tid >> 6, lane = tid & 63, r32 = lane & 31, hi = lane >> 5;
  bf16* V_lds = (bf16*)lds; bf16* K_lds = (bf16*)(lds + 2 * SHM_V);
  float* ws = (float*)(lds + 2 * SHM_V + 2 * SHM_K) + wid * 64; float* li_l = ws; float* al_l = ws + 32;
  float m_reg = -1e30f, l_reg = 0; f32x16 o[4] = {}; bf16x8 qr[8];
  const bf16* Qw = Qb + (long)(wid * QBLK + r32) * LDQ + hi * 8;
#pragma unroll
  for (int d0 = 0; d0 < 8; ++d0) qr[d0] = ld8(Qw + d0 * 16);
  const int sr = tid >> 4, sc = (tid & 15) * 8, vst0 = v_st(sr, sc), vst1 = v_st(32 + sr, sc);
  const int vb0 = (int)(uintptr_t)V_lds + v_rd_base(lane);
  struct { bf16x8 vs0, vs1, ks0, ks1; } sr_[SDEPTH];
#define SLOAD(i, k0) do { sr_[i].vs0 = ld8(&Vh[(long)((k0) + sr) * LDK + sc]); sr_[i].vs1 = ld8(&Vh[(long)((k0) + 32 + sr) * LDK + sc]); \
    sr_[i].ks0 = ld8(&Kh[(long)((k0) + sr) * LDK + sc]); sr_[i].ks1 = ld8(&Kh[(long)((k0) + 32 + sr) * LDK + sc]); } while (0)
#define SWRITE(b, i) do { *(bf16x8*)((char*)V_lds + (b) * SHM_V + vst0) = sr_[i].vs0;          \
    *(bf16x8*)((char*)V_lds + (b) * SHM_V + vst1) = sr_[i].vs1; int kc = sc * 2;               \
    *(bf16x8*)((char*)K_lds + (b) * SHM_K + KSWZ(sr, kc)) = sr_[i].ks0;                       \
    *(bf16x8*)((char*)K_lds + (b) * SHM_K + KSWZ(32 + sr, kc)) = sr_[i].ks1; } while (0)
#define SWAIT() do { if constexpr (SDEPTH == 2) asm volatile("s_waitcnt vmcnt(4)" ::: "memory"); else asm volatile("s_waitcnt vmcnt(0)" ::: "memory"); } while (0)
#define RESC(a) do { if (__any((a) < 1.f)) { if (hi == 0) al_l[r32] = (a); asm volatile("s_waitcnt lgkmcnt(0)" ::: "memory"); \
    for (int d = 0; d < 4; ++d) for (int r = 0; r < 16; ++r) o[d][r] *= al_l[crow(r, hi)]; } } while (0)
  f32x16 pA0, pA1, pB0, pB1; float mnA, mnB, alA, alB; bf16x8 pa0, pa1, pa2, pa3; const int NT = seq / KVBLK;
  constexpr int SE = 0, SO = SDEPTH - 1;
  SLOAD(SE, 0); asm volatile("s_waitcnt vmcnt(0)" ::: "memory"); SWRITE(0, SE); __syncthreads();
  qkt(pA0, pA1, K_lds, qr, r32, hi); partialSM(pA0, pA1, m_reg, mnA, alA);
  SLOAD(SO, KVBLK); if constexpr (SDEPTH == 2) { if (2 < NT) SLOAD(SE, 2 * KVBLK); }
  SWAIT(); SWRITE(1, SO); __syncthreads();
  for (int j = 1; j + 1 < NT; j += 2) {
    SBAR(); qkt(pB0, pB1, (bf16*)((char*)K_lds + SHM_K), qr, r32, hi);
    finishSM(pA0, pA1, alA, l_reg, pa0, pa1, pa2, pa3); SBAR();
    SLOAD(SO, (j + SDEPTH) * KVBLK); SBAR();
    pv_d0(o, vb0, pa0, pa1, pa2, pa3); partialSM(pB0, pB1, m_reg, mnB, alB);
    __syncthreads(); SWAIT(); SWRITE(0, SE);
    RESC(alB); __syncthreads();
    SBAR(); qkt(pA0, pA1, K_lds, qr, r32, hi);
    finishSM(pB0, pB1, alB, l_reg, pa0, pa1, pa2, pa3); SBAR();
    if (SDEPTH == 1 || j + 3 < NT) SLOAD(SE, (j + 1 + SDEPTH) * KVBLK); SBAR();
    pv_d0(o, vb0 + (int)SHM_V, pa0, pa1, pa2, pa3); partialSM(pA0, pA1, m_reg, mnA, alA);
    __syncthreads(); SWAIT(); SWRITE(1, SO);
    RESC(alA); __syncthreads();
  }
  SBAR(); qkt(pB0, pB1, (bf16*)((char*)K_lds + SHM_K), qr, r32, hi);
  finishSM(pA0, pA1, alA, l_reg, pa0, pa1, pa2, pa3); SBAR();
  pv_d0(o, vb0, pa0, pa1, pa2, pa3); partialSM(pB0, pB1, m_reg, mnB, alB);
  __syncthreads(); RESC(alB);
  finishSM(pB0, pB1, alB, l_reg, pa0, pa1, pa2, pa3); SBAR();
  pv_d0(o, vb0 + (int)SHM_V, pa0, pa1, pa2, pa3);
  if (hi == 0) li_l[r32] = l_reg; asm volatile("s_waitcnt lgkmcnt(0)" ::: "memory");
  float rli[16];
#pragma unroll
  for (int r = 0; r < 16; ++r) rli[r] = __builtin_amdgcn_rcpf(li_l[crow(r, hi)]);
  bf16* Ow = Ob + (long)(wid * QBLK) * LDO;
#pragma unroll
  for (int r = 0; r < 16; ++r) { int orow = crow(r, hi);
    for (int d0 = 0; d0 < 4; ++d0) Ow[(long)orow * LDO + d0 * 32 + r32] = __float2bfloat16(o[d0][r] * rli[r]); }
}
__global__ __launch_bounds__(64 * NW, 1)
void attn_fwd_dense(const bf16* __restrict__ Q, const bf16* __restrict__ K, const bf16* __restrict__ V, bf16* __restrict__ O) {
  extern __shared__ __attribute__((aligned(16))) char shm[];
  const long g = blockIdx.y; const long i0 = (long)blockIdx.x * 256;
  const long q0 = (g * 3072 + i0) * D, k0 = g * SKV * (long)D;
  attn_dense_body(Q + q0, K + k0, V + k0, O + q0, SKV, shm);
}
extern "C" void kernel_launch(void* const* d_in, const int* in_sizes, int n_in, void* d_out, int out_size, void* d_ws, size_t ws_size, hipStream_t stream) {
  hipFuncSetAttribute((const void*)attn_fwd_dense, hipFuncAttributeMaxDynamicSharedMemorySize, (int)SHM_ATTN);
  hipLaunchKernelGGL(attn_fwd_dense, dim3(12, 64), dim3(64 * NW), SHM_ATTN, stream,
                     (const bf16*)d_in[0], (const bf16*)d_in[1], (const bf16*)d_in[2], (bf16*)d_out);
}
```

```cpp
#include <hip/hip_runtime.h>
#include <hip/hip_bf16.h>

typedef unsigned u32x4 __attribute__((ext_vector_type(4)));

__device__ __forceinline__ float bf2f(unsigned short b) { return __uint_as_float((unsigned)b << 16); }

__device__ __forceinline__ void safe_rows(const unsigned short* __restrict__ q, const unsigned short* __restrict__ k,
                                       const unsigned short* __restrict__ v, unsigned short* __restrict__ o, int lane) {
  const int row = lane & 31, hf = lane >> 5;
  const float c = 0.12751743082f;
  u32x4 qp[8];
#pragma unroll
  for (int i = 0; i < 8; ++i) qp[i] = *(const u32x4*)(q + row * 128 + hf * 64 + i * 8);
  float m = -__builtin_inff();
  for (int pass = 0; pass < 2; ++pass) {
    float acc[64]; float l = 0.f;
#pragma unroll
    for (int i = 0; i < 64; ++i) acc[i] = 0.f;
    for (int key = 0; key < 384; ++key) {
      float dot = 0.f;
#pragma unroll
      for (int i = 0; i < 8; ++i) {
        const u32x4 kk = *(const u32x4*)(k + key * 128 + hf * 64 + i * 8);
#pragma unroll
        for (int e = 0; e < 4; ++e) {
          dot = fmaf(__uint_as_float(qp[i][e] << 16), __uint_as_float(kk[e] << 16), dot);
          dot = fmaf(__uint_as_float(qp[i][e] & 0xffff0000u), __uint_as_float(kk[e] & 0xffff0000u), dot);
        }
      }
      dot += __shfl_xor(dot, 32);
      if (pass == 0) { m = fmaxf(m, dot); }
      else {
        const float p = __builtin_amdgcn_exp2f((dot - m) * c);
        l += p;
#pragma unroll
        for (int i = 0; i < 8; ++i) {
          const u32x4 vv = *(const u32x4*)(v + key * 128 + hf * 64 + i * 8);
#pragma unroll
          for (int e = 0; e < 4; ++e) {
            acc[i * 8 + 2 * e] = fmaf(p, __uint_as_float(vv[e] << 16), acc[i * 8 + 2 * e]);
            acc[i * 8 + 2 * e + 1] = fmaf(p, __uint_as_float(vv[e] & 0xffff0000u), acc[i * 8 + 2 * e + 1]);
          }
        }
      }
    }
    if (pass == 1) {
      const float r = 1.f / l;
#pragma unroll
      for (int i = 0; i < 8; ++i) {
        u32x4 w;
#pragma unroll
        for (int e = 0; e < 4; ++e) {
          typedef float f2 __attribute__((ext_vector_type(2))); typedef __bf16 b2 __attribute__((ext_vector_type(2)));
          f2 x = {acc[i * 8 + 2 * e] * r, acc[i * 8 + 2 * e + 1] * r};
          w[e] = __builtin_bit_cast(unsigned, __builtin_convertvector(x, b2));
        }
        *(u32x4*)(o + row * 128 + hf * 64 + i * 8) = w;
      }
    }
  }
}

__global__ __launch_bounds__(256, 1) void gqa_attn(const char* __restrict__ q, const char* __restrict__ k, const char* __restrict__ v, char* __restrict__ o) {
  extern __shared__ __attribute__((aligned(1024))) char lds[];
  const unsigned tid = threadIdx.x;
  const unsigned wid = __builtin_amdgcn_readfirstlane(tid >> 6);
  const unsigned bid = blockIdx.x;
  const unsigned g = (bid & 7) * 8 + (bid >> 5), quarter = (bid >> 3) & 3;
  const size_t qbase = ((size_t)g * 3072 + quarter * 768) * 256, kbase = (size_t)g * 98304;
  __amdgpu_buffer_rsrc_t srdQ = __builtin_amdgcn_make_buffer_rsrc((void*)(q + qbase), (short)0, 768 * 256, 0x00020000);
  __amdgpu_buffer_rsrc_t srdO = __builtin_amdgcn_make_buffer_rsrc((void*)(o + qbase), (short)0, 768 * 256, 0x00020000);
  __amdgpu_buffer_rsrc_t srdK = __builtin_amdgcn_make_buffer_rsrc((void*)(k + kbase), (short)0, 98304, 0x00020000);
  __amdgpu_buffer_rsrc_t srdV = __builtin_amdgcn_make_buffer_rsrc((void*)(v + kbase - 896), (short)0, 98304 + 896, 0x00020000);
  const unsigned sLds = __builtin_amdgcn_readfirstlane((unsigned)(size_t)lds);
  const unsigned sKl = sLds + wid * 8192 - 16384, sKg = wid * 8192;
  const unsigned sVl = sLds + 81920 + wid * 2048, sVg = wid * 2048;
  const unsigned sSt = sLds + 114688 + wid * 12288, sQg = wid * 16384;
  const unsigned sK0a = wid < 2 ? sLds + 114688 + (2 * wid) * 12288 + 8192 : sKl;
  const unsigned sK0b = wid < 2 ? sLds + 114688 + (2 * wid + 1) * 12288 + 8192 : sKl + 4096;
  const unsigned sPf = (((bid >> 3) & 3) * 4 + wid) * 4096;
  const unsigned sPf0 = (wid & 1) * 4096;
  unsigned flags;
  asm volatile(
    "s_getpc_b64 s[46:47]\n\t"
    "L_pc_%=:\n\t"
    "v_mbcnt_lo_u32_b32 v30, -1, 0\n\t"
    "v_mbcnt_hi_u32_b32 v30, -1, v30\n\t"
    "v_lshlrev_b32 v30, 6, v30\n\t"
    "v_add_u32 v29, %15, v30\n\t"
    "global_load_dword v25, v29, s[46:47]\n\t"
    "s_add_u32 s46, s46, L_main_%=-L_pc_%=\n\t"
    "s_addc_u32 s47, s47, 0\n\t"
    "v_add_u32 v30, %14, v30\n\t"
    "v_min_u32 v30, L_end_%=-L_main_%=-4, v30\n\t"
    "global_load_dword v24, v30, s[46:47]\n\t"
    "v_mbcnt_lo_u32_b32 v245, -1, 0\n\t"
    "v_mbcnt_hi_u32_b32 v245, -1, v245\n\t"
    "s_mov_b32 s41, 0\n\t"
    "s_mov_b32 s44, 0x5f800000\n\t"
    "s_mov_b32 s42, 0x3e0293ee\n\t"
    "s_mov_b32 s43, 0x3e0293ee\n\t"
    "v_lshrrev_b32 v0, 4, v245\n\t"
    "v_and_b32 v1, 15, v245\n\t"
    "v_xor_b32 v1, v1, v0\n\t"
    "v_lshlrev_b32 v1, 4, v1\n\t"
    "v_lshl_add_u32 v248, v0, 8, v1\n\t"
    "v_xor_b32 v249, 64, v248\n\t"
    "v_xor_b32 v250, 128, v248\n\t"
    "v_xor_b32 v251, 192, v248\n\t"
    "s_mov_b32 m0, %12\n\t"
    "s_add_u32 s40, %7, 0\n\t"
    "buffer_load_dwordx4 v248, %2, s40 offen offset:0  lds\n\t"
    "buffer_load_dwordx4 v249, %2, s40 offen offset:1024  lds\n\t"
    "buffer_load_dwordx4 v250, %2, s40 offen offset:2048  lds\n\t"
    "buffer_load_dwordx4 v251, %2, s40 offen offset:3072  lds\n\t"
    "s_mov_b32 m0, %13\n\t"
    "s_add_u32 s40, %7, 4096\n\t"
    "buffer_load_dwordx4 v248, %2, s40 offen offset:0  lds\n\t"
    "buffer_load_dwordx4 v249, %2, s40 offen offset:1024  lds\n\t"
    "buffer_load_dwordx4 v250, %2, s40 offen offset:2048  lds\n\t"
    "buffer_load_dwordx4 v251, %2, s40 offen offset:3072  lds\n\t"
    "v_bfe_u32 v0, v245, 2, 3\n\t"
    "v_lshrrev_b32 v1, 5, v245\n\t"
    "v_and_b32 v2, 3, v245\n\t"
    "v_bfe_u32 v3, v245, 4, 1\n\t"
    "v_lshlrev_b32 v3, 1, v3\n\t"
    "v_xor_b32 v2, v2, v3\n\t"
    "v_lshlrev_b32 v0, 8, v0\n\t"
    "v_lshl_add_u32 v0, v1, 6, v0\n\t"
    "v_lshl_add_u32 v227, v2, 4, v0\n\t"
    "v_lshrrev_b32 v0, 3, v245\n\t"
    "v_and_b32 v1, 7, v245\n\t"
    "v_lshrrev_b32 v2, 4, v245\n\t"
    "v_xor_b32 v1, v1, v2\n\t"
    "v_lshlrev_b32 v1, 4, v1\n\t"
    "v_lshl_add_u32 v228, v0, 8, v1\n\t"
    "v_xor_b32 v229, 64, v228\n\t"
    "s_add_u32 m0, %6, 32768\n\t"
    "s_add_u32 s40, %7, 32768\n\t"
    "buffer_load_dwordx4 v248, %2, s40 offen offset:0  lds\n\t"
    "buffer_load_dwordx4 v249, %2, s40 offen offset:1024  lds\n\t"
    "buffer_load_dwordx4 v250, %2, s40 offen offset:2048  lds\n\t"
    "buffer_load_dwordx4 v251, %2, s40 offen offset:3072  lds\n\t"
    "s_add_u32 m0, %6, 36864\n\t"
    "s_add_u32 s40, %7, 36864\n\t"
    "buffer_load_dwordx4 v248, %2, s40 offen offset:0  lds\n\t"
    "buffer_load_dwordx4 v249, %2, s40 offen offset:1024  lds\n\t"
    "buffer_load_dwordx4 v250, %2, s40 offen offset:2048  lds\n\t"
    "buffer_load_dwordx4 v251, %2, s40 offen offset:3072  lds\n\t"
    "v_lshlrev_b32 v0, 4, v245\n\t"
    "v_add_u32 v224, %5, v0\n\t"
    "v_add_u32 v244, 0x10000, v224\n\t"
    "v_lshrrev_b32 v1, 4, v245\n\t"
    "v_and_b32 v2, 15, v245\n\t"
    "v_lshrrev_b32 v0, 5, v245\n\t"
    "v_lshlrev_b32 v0, 11, v0\n\t"
    "v_bfe_u32 v3, v245, 2, 3\n\t"
    "v_lshl_add_u32 v0, v3, 6, v0\n\t"
    "v_and_b32 v3, 3, v245\n\t"
    "v_lshl_add_u32 v0, v3, 3, v0\n\t"
    "v_add_u32 v0, %5, v0\n\t"
    "v_add_u32 v0, 81920, v0\n\t"
    "v_bfe_u32 v3, v245, 4, 1\n\t"
    "v_lshl_add_u32 v226, v3, 5, v0\n\t"
    "v_xor_b32 v225, 32, v226\n\t"
    "v_lshrrev_b32 v3, 1, v2\n\t"
    "v_xor_b32 v3, v3, v1\n\t"
    "v_lshlrev_b32 v3, 4, v3\n\t"
    "v_lshl_add_u32 v3, v2, 7, v3\n\t"
    "v_add_u32 v230, %10, v3\n\t"
    "v_xor_b32 v231, 64, v230\n\t"
    "v_xor_b32 v3, v2, v1\n\t"
    "v_lshlrev_b32 v3, 4, v3\n\t"
    "v_lshl_add_u32 v3, v2, 8, v3\n\t"
    "v_lshrrev_b32 v5, 5, v245\n\t"
    "v_and_b32 v7, 7, v245\n\t"
    "v_xor_b32 v5, v5, v7\n\t"
    "v_lshlrev_b32 v5, 4, v5\n\t"
    "v_lshl_add_u32 v5, v2, 7, v5\n\t"
    "v_bfe_u32 v7, v245, 3, 1\n\t"
    "v_bfe_u32 v4, v245, 4, 1\n\t"
    "v_xor_b32 v7, v7, v4\n\t"
    "v_lshl_add_u32 v5, v7, 3, v5\n\t"
    "v_add_u32 v5, %10, v5\n\t"
    "v_add_u32 v234, 8192, v5\n\t"
    "v_lshrrev_b32 v5, 3, v245\n\t"
    "v_and_b32 v7, 7, v245\n\t"
    "v_xor_b32 v16, v7, v5\n\t"
    "v_lshlrev_b32 v16, 4, v16\n\t"
    "v_lshl_add_u32 v16, v5, 7, v16\n\t"
    "v_add_u32 v16, %10, v16\n\t"
    "v_add_u32 v235, 8192, v16\n\t"
    "v_lshlrev_b32 v7, 4, v7\n\t"
    "v_lshl_add_u32 v236, v5, 8, v7\n\t"
    "v_lshlrev_b32 v17, 4, v245\n\t"
    "v_add_u32 v192, %12, v17\n\t"
    "v_add_u32 v193, %13, v17\n\t"
    "v_add_u32 v20, %12, v3\n\t"
    "v_add_u32 v21, %13, v3\n\t"
    "v_xor_b32 v8, 0, v20\n\t"
    "v_xor_b32 v9, 64, v20\n\t"
    "v_xor_b32 v10, 128, v20\n\t"
    "v_xor_b32 v11, 192, v20\n\t"
    "v_xor_b32 v12, 0, v21\n\t"
    "v_xor_b32 v13, 64, v21\n\t"
    "v_xor_b32 v14, 128, v21\n\t"
    "v_xor_b32 v15, 192, v21\n\t"
    "s_waitcnt vmcnt(8)\n\t"
    "ds_read_b128 v[32:35], v8\n\t"
    "ds_read_b128 v[36:39], v9\n\t"
    "ds_read_b128 v[40:43], v10\n\t"
    "ds_read_b128 v[44:47], v11\n\t"
    "ds_read_b128 v[48:51], v12\n\t"
    "ds_read_b128 v[52:55], v13\n\t"
    "ds_read_b128 v[56:59], v14\n\t"
    "ds_read_b128 v[60:63], v15\n\t"
    "s_waitcnt lgkmcnt(7)\n\t"
    "s_add_u32 m0, %6, 65536\n\t"
    "s_add_u32 s40, %7, 65536\n\t"
    "buffer_load_dwordx4 v248, %2, s40 offen offset:0  lds\n\t"
    "buffer_load_dwordx4 v249, %2, s40 offen offset:1024  lds\n\t"
    "buffer_load_dwordx4 v250, %2, s40 offen offset:2048  lds\n\t"
    "buffer_load_dwordx4 v251, %2, s40 offen offset:3072  lds\n\t"
    "s_add_u32 m0, %6, 69632\n\t"
    "s_add_u32 s40, %7, 69632\n\t"
    "buffer_load_dwordx4 v248, %2, s40 offen offset:0  lds\n\t"
    "buffer_load_dwordx4 v249, %2, s40 offen offset:1024  lds\n\t"
    "buffer_load_dwordx4 v250, %2, s40 offen offset:2048  lds\n\t"
    "buffer_load_dwordx4 v251, %2, s40 offen offset:3072  lds\n\t"
    "v_lshlrev_b32 v64, 16, v32\n\t"
    "v_and_b32 v65, 0xffff0000, v32\n\t"
    "v_pk_mul_f32 v[64:65], v[64:65], s[42:43]\n\t"
    "v_cvt_pk_bf16_f32 v32, v64, v65\n\t"
    "v_lshlrev_b32 v66, 16, v33\n\t"
    "v_and_b32 v67, 0xffff0000, v33\n\t"
    "v_pk_mul_f32 v[66:67], v[66:67], s[42:43]\n\t"
    "v_cvt_pk_bf16_f32 v33, v66, v67\n\t"
    "v_lshlrev_b32 v68, 16, v34\n\t"
    "v_and_b32 v69, 0xffff0000, v34\n\t"
    "v_pk_mul_f32 v[68:69], v[68:69], s[42:43]\n\t"
    "v_cvt_pk_bf16_f32 v34, v68, v69\n\t"
    "v_lshlrev_b32 v70, 16, v35\n\t"
    "v_and_b32 v71, 0xffff0000, v35\n\t"
    "v_pk_mul_f32 v[70:71], v[70:71], s[42:43]\n\t"
    "v_cvt_pk_bf16_f32 v35, v70, v71\n\t"
    "s_waitcnt lgkmcnt(6)\n\t"
    "s_add_u32 m0, %10, 0\n\t"
    "s_add_u32 s40, %11, 0\n\t"
    "buffer_load_dwordx4 v228, %1, s40 offen nt lds\n\t"
    "s_add_u32 m0, %10, 1024\n\t"
    "s_add_u32 s40, %11, 2048\n\t"
    "buffer_load_dwordx4 v229, %1, s40 offen nt lds\n\t"
    "v_lshlrev_b32 v72, 16, v36\n\t"
    "v_and_b32 v73, 0xffff0000, v36\n\t"
    "v_pk_mul_f32 v[72:73], v[72:73], s[42:43]\n\t"
    "v_cvt_pk_bf16_f32 v36, v72, v73\n\t"
    "v_lshlrev_b32 v74, 16, v37\n\t"
    "v_and_b32 v75, 0xffff0000, v37\n\t"
    "v_pk_mul_f32 v[74:75], v[74:75], s[42:43]\n\t"
    "v_cvt_pk_bf16_f32 v37, v74, v75\n\t"
    "v_lshlrev_b32 v76, 16, v38\n\t"
    "v_and_b32 v77, 0xffff0000, v38\n\t"
    "v_pk_mul_f32 v[76:77], v[76:77], s[42:43]\n\t"
    "v_cvt_pk_bf16_f32 v38, v76, v77\n\t"
    "v_lshlrev_b32 v78, 16, v39\n\t"
    "v_and_b32 v79, 0xffff0000, v39\n\t"
    "v_pk_mul_f32 v[78:79], v[78:79], s[42:43]\n\t"
    "v_cvt_pk_bf16_f32 v39, v78, v79\n\t"
    "s_waitcnt lgkmcnt(5)\n\t"
    "s_add_u32 m0, %10, 2048\n\t"
    "s_add_u32 s40, %11, 4096\n\t"
    "buffer_load_dwordx4 v228, %1, s40 offen nt lds\n\t"
    "v_lshlrev_b32 v64, 16, v40\n\t"
    "v_and_b32 v65, 0xffff0000, v40\n\t"
    "v_pk_mul_f32 v[64:65], v[64:65], s[42:43]\n\t"
    "v_cvt_pk_bf16_f32 v40, v64, v65\n\t"
    "v_lshlrev_b32 v66, 16, v41\n\t"
    "v_and_b32 v67, 0xffff0000, v41\n\t"
    "v_pk_mul_f32 v[66:67], v[66:67], s[42:43]\n\t"
    "v_cvt_pk_bf16_f32 v41, v66, v67\n\t"
    "v_lshlrev_b32 v68, 16, v42\n\t"
    "v_and_b32 v69, 0xffff0000, v42\n\t"
    "v_pk_mul_f32 v[68:69], v[68:69], s[42:43]\n\t"
    "v_cvt_pk_bf16_f32 v42, v68, v69\n\t"
    "v_lshlrev_b32 v70, 16, v43\n\t"
    "v_and_b32 v71, 0xffff0000, v43\n\t"
    "v_pk_mul_f32 v[70:71], v[70:71], s[42:43]\n\t"
    "v_cvt_pk_bf16_f32 v43, v70, v71\n\t"
    "s_waitcnt lgkmcnt(4)\n\t"
    "s_add_u32 m0, %10, 3072\n\t"
    "s_add_u32 s40, %11, 6144\n\t"
    "buffer_load_dwordx4 v229, %1, s40 offen nt lds\n\t"
    "v_lshlrev_b32 v72, 16, v44\n\t"
    "v_and_b32 v73, 0xffff0000, v44\n\t"
    "v_pk_mul_f32 v[72:73], v[72:73], s[42:43]\n\t"
    "v_cvt_pk_bf16_f32 v44, v72, v73\n\t"
    "v_lshlrev_b32 v74, 16, v45\n\t"
    "v_and_b32 v75, 0xffff0000, v45\n\t"
    "v_pk_mul_f32 v[74:75], v[74:75], s[42:43]\n\t"
    "v_cvt_pk_bf16_f32 v45, v74, v75\n\t"
    "v_lshlrev_b32 v76, 16, v46\n\t"
    "v_and_b32 v77, 0xffff0000, v46\n\t"
    "v_pk_mul_f32 v[76:77], v[76:77], s[42:43]\n\t"
    "v_cvt_pk_bf16_f32 v46, v76, v77\n\t"
    "v_lshlrev_b32 v78, 16, v47\n\t"
    "v_and_b32 v79, 0xffff0000, v47\n\t"
    "v_pk_mul_f32 v[78:79], v[78:79], s[42:43]\n\t"
    "v_cvt_pk_bf16_f32 v47, v78, v79\n\t"
    "s_waitcnt lgkmcnt(3)\n\t"
    "s_add_u32 m0, %10, 4096\n\t"
    "s_add_u32 s40, %11, 128\n\t"
    "buffer_load_dwordx4 v228, %1, s40 offen nt lds\n\t"
    "v_lshlrev_b32 v64, 16, v48\n\t"
    "v_and_b32 v65, 0xffff0000, v48\n\t"
    "v_pk_mul_f32 v[64:65], v[64:65], s[42:43]\n\t"
    "v_cvt_pk_bf16_f32 v48, v64, v65\n\t"
    "v_lshlrev_b32 v66, 16, v49\n\t"
    "v_and_b32 v67, 0xffff0000, v49\n\t"
    "v_pk_mul_f32 v[66:67], v[66:67], s[42:43]\n\t"
    "v_cvt_pk_bf16_f32 v49, v66, v67\n\t"
    "v_lshlrev_b32 v68, 16, v50\n\t"
    "v_and_b32 v69, 0xffff0000, v50\n\t"
    "v_pk_mul_f32 v[68:69], v[68:69], s[42:43]\n\t"
    "v_cvt_pk_bf16_f32 v50, v68, v69\n\t"
    "v_lshlrev_b32 v70, 16, v51\n\t"
    "v_and_b32 v71, 0xffff0000, v51\n\t"
    "v_pk_mul_f32 v[70:71], v[70:71], s[42:43]\n\t"
    "v_cvt_pk_bf16_f32 v51, v70, v71\n\t"
    "s_waitcnt lgkmcnt(2)\n\t"
    "s_add_u32 m0, %10, 5120\n\t"
    "s_add_u32 s40, %11, 2176\n\t"
    "buffer_load_dwordx4 v229, %1, s40 offen nt lds\n\t"
    "v_lshlrev_b32 v72, 16, v52\n\t"
    "v_and_b32 v73, 0xffff0000, v52\n\t"
    "v_pk_mul_f32 v[72:73], v[72:73], s[42:43]\n\t"
    "v_cvt_pk_bf16_f32 v52, v72, v73\n\t"
    "v_lshlrev_b32 v74, 16, v53\n\t"
    "v_and_b32 v75, 0xffff0000, v53\n\t"
    "v_pk_mul_f32 v[74:75], v[74:75], s[42:43]\n\t"
    "v_cvt_pk_bf16_f32 v53, v74, v75\n\t"
    "v_lshlrev_b32 v76, 16, v54\n\t"
    "v_and_b32 v77, 0xffff0000, v54\n\t"
    "v_pk_mul_f32 v[76:77], v[76:77], s[42:43]\n\t"
    "v_cvt_pk_bf16_f32 v54, v76, v77\n\t"
    "v_lshlrev_b32 v78, 16, v55\n\t"
    "v_and_b32 v79, 0xffff0000, v55\n\t"
    "v_pk_mul_f32 v[78:79], v[78:79], s[42:43]\n\t"
    "v_cvt_pk_bf16_f32 v55, v78, v79\n\t"
    "v_add_u32 v194, %6, v17\n\t"
    "v_add_u32 v194, 32768, v194\n\t"
    "v_add_u32 v195, 4096, v194\n\t"
    "v_add_u32 v20, %6, v3\n\t"
    "v_add_u32 v20, 32768, v20\n\t"
    "v_add_u32 v21, 4096, v20\n\t"
    "v_xor_b32 v8, 0, v20\n\t"
    "v_xor_b32 v9, 64, v20\n\t"
    "v_xor_b32 v10, 128, v20\n\t"
    "v_xor_b32 v11, 192, v20\n\t"
    "v_xor_b32 v12, 0, v21\n\t"
    "v_xor_b32 v13, 64, v21\n\t"
    "v_xor_b32 v14, 128, v21\n\t"
    "v_xor_b32 v15, 192, v21\n\t"
    "s_waitcnt lgkmcnt(1)\n\t"
    "s_add_u32 m0, %10, 6144\n\t"
    "s_add_u32 s40, %11, 4224\n\t"
    "buffer_load_dwordx4 v228, %1, s40 offen nt lds\n\t"
    "v_lshlrev_b32 v64, 16, v56\n\t"
    "v_and_b32 v65, 0xffff0000, v56\n\t"
    "v_pk_mul_f32 v[64:65], v[64:65], s[42:43]\n\t"
    "v_cvt_pk_bf16_f32 v56, v64, v65\n\t"
    "v_lshlrev_b32 v66, 16, v57\n\t"
    "v_and_b32 v67, 0xffff0000, v57\n\t"
    "v_pk_mul_f32 v[66:67], v[66:67], s[42:43]\n\t"
    "v_cvt_pk_bf16_f32 v57, v66, v67\n\t"
    "v_lshlrev_b32 v68, 16, v58\n\t"
    "v_and_b32 v69, 0xffff0000, v58\n\t"
    "v_pk_mul_f32 v[68:69], v[68:69], s[42:43]\n\t"
    "v_cvt_pk_bf16_f32 v58, v68, v69\n\t"
    "v_lshlrev_b32 v70, 16, v59\n\t"
    "v_and_b32 v71, 0xffff0000, v59\n\t"
    "v_pk_mul_f32 v[70:71], v[70:71], s[42:43]\n\t"
    "v_cvt_pk_bf16_f32 v59, v70, v71\n\t"
    "s_waitcnt lgkmcnt(0)\n\t"
    "s_add_u32 m0, %10, 7168\n\t"
    "s_add_u32 s40, %11, 6272\n\t"
    "buffer_load_dwordx4 v229, %1, s40 offen nt lds\n\t"
    "v_lshlrev_b32 v72, 16, v60\n\t"
    "v_and_b32 v73, 0xffff0000, v60\n\t"
    "v_pk_mul_f32 v[72:73], v[72:73], s[42:43]\n\t"
    "v_cvt_pk_bf16_f32 v60, v72, v73\n\t"
    "v_lshlrev_b32 v74, 16, v61\n\t"
    "v_and_b32 v75, 0xffff0000, v61\n\t"
    "v_pk_mul_f32 v[74:75], v[74:75], s[42:43]\n\t"
    "v_cvt_pk_bf16_f32 v61, v74, v75\n\t"
    "v_lshlrev_b32 v76, 16, v62\n\t"
    "v_and_b32 v77, 0xffff0000, v62\n\t"
    "v_pk_mul_f32 v[76:77], v[76:77], s[42:43]\n\t"
    "v_cvt_pk_bf16_f32 v62, v76, v77\n\t"
    "v_lshlrev_b32 v78, 16, v63\n\t"
    "v_and_b32 v79, 0xffff0000, v63\n\t"
    "v_pk_mul_f32 v[78:79], v[78:79], s[42:43]\n\t"
    "v_cvt_pk_bf16_f32 v63, v78, v79\n\t"
    "s_waitcnt vmcnt(16)\n\t"
    "ds_read_b128 v[96:99], v8\n\t"
    "ds_read_b128 v[100:103], v9\n\t"
    "ds_read_b128 v[104:107], v10\n\t"
    "ds_read_b128 v[108:111], v11\n\t"
    "ds_read_b128 v[112:115], v12\n\t"
    "ds_read_b128 v[116:119], v13\n\t"
    "ds_read_b128 v[120:123], v14\n\t"
    "ds_read_b128 v[124:127], v15\n\t"
    "ds_write_b128 v192, v[32:35] offset:0\n\t"
    "ds_write_b128 v192, v[36:39] offset:1024\n\t"
    "ds_write_b128 v192, v[40:43] offset:2048\n\t"
    "ds_write_b128 v192, v[44:47] offset:3072\n\t"
    "ds_write_b128 v193, v[48:51] offset:0\n\t"
    "ds_write_b128 v193, v[52:55] offset:1024\n\t"
    "ds_write_b128 v193, v[56:59] offset:2048\n\t"
    "ds_write_b128 v193, v[60:63] offset:3072\n\t"
    "s_waitcnt lgkmcnt(15)\n\t"
    "s_add_u32 m0, %8, 0\n\t"
    "s_add_u32 s40, %9, 896\n\t"
    "buffer_load_dwordx4 v227, %3, s40 offen  lds\n\t"
    "v_lshlrev_b32 v64, 16, v96\n\t"
    "v_and_b32 v65, 0xffff0000, v96\n\t"
    "v_pk_mul_f32 v[64:65], v[64:65], s[42:43]\n\t"
    "v_cvt_pk_bf16_f32 v96, v64, v65\n\t"
    "v_lshlrev_b32 v66, 16, v97\n\t"
    "v_and_b32 v67, 0xffff0000, v97\n\t"
    "v_pk_mul_f32 v[66:67], v[66:67], s[42:43]\n\t"
    "v_cvt_pk_bf16_f32 v97, v66, v67\n\t"
    "v_lshlrev_b32 v68, 16, v98\n\t"
    "v_and_b32 v69, 0xffff0000, v98\n\t"
    "v_pk_mul_f32 v[68:69], v[68:69], s[42:43]\n\t"
    "v_cvt_pk_bf16_f32 v98, v68, v69\n\t"
    "v_lshlrev_b32 v70, 16, v99\n\t"
    "v_and_b32 v71, 0xffff0000, v99\n\t"
    "v_pk_mul_f32 v[70:71], v[70:71], s[42:43]\n\t"
    "v_cvt_pk_bf16_f32 v99, v70, v71\n\t"
    "s_waitcnt lgkmcnt(14)\n\t"
    "s_add_u32 m0, %8, 1024\n\t"
    "s_add_u32 s40, %9, 1024\n\t"
    "buffer_load_dwordx4 v227, %3, s40 offen  lds\n\t"
    "v_lshlrev_b32 v72, 16, v100\n\t"
    "v_and_b32 v73, 0xffff0000, v100\n\t"
    "v_pk_mul_f32 v[72:73], v[72:73], s[42:43]\n\t"
    "v_cvt_pk_bf16_f32 v100, v72, v73\n\t"
    "v_lshlrev_b32 v74, 16, v101\n\t"
    "v_and_b32 v75, 0xffff0000, v101\n\t"
    "v_pk_mul_f32 v[74:75], v[74:75], s[42:43]\n\t"
    "v_cvt_pk_bf16_f32 v101, v74, v75\n\t"
    "v_lshlrev_b32 v76, 16, v102\n\t"
    "v_and_b32 v77, 0xffff0000, v102\n\t"
    "v_pk_mul_f32 v[76:77], v[76:77], s[42:43]\n\t"
    "v_cvt_pk_bf16_f32 v102, v76, v77\n\t"
    "v_lshlrev_b32 v78, 16, v103\n\t"
    "v_and_b32 v79, 0xffff0000, v103\n\t"
    "v_pk_mul_f32 v[78:79], v[78:79], s[42:43]\n\t"
    "v_cvt_pk_bf16_f32 v103, v78, v79\n\t"
    "s_waitcnt lgkmcnt(13)\n\t"
    "s_add_u32 m0, %8, 8192\n\t"
    "s_add_u32 s40, %9, 9088\n\t"
    "buffer_load_dwordx4 v227, %3, s40 offen  lds\n\t"
    "v_lshlrev_b32 v64, 16, v104\n\t"
    "v_and_b32 v65, 0xffff0000, v104\n\t"
    "v_pk_mul_f32 v[64:65], v[64:65], s[42:43]\n\t"
    "v_cvt_pk_bf16_f32 v104, v64, v65\n\t"
    "v_lshlrev_b32 v66, 16, v105\n\t"
    "v_and_b32 v67, 0xffff0000, v105\n\t"
    "v_pk_mul_f32 v[66:67], v[66:67], s[42:43]\n\t"
    "v_cvt_pk_bf16_f32 v105, v66, v67\n\t"
    "v_lshlrev_b32 v68, 16, v106\n\t"
    "v_and_b32 v69, 0xffff0000, v106\n\t"
    "v_pk_mul_f32 v[68:69], v[68:69], s[42:43]\n\t"
    "v_cvt_pk_bf16_f32 v106, v68, v69\n\t"
    "v_lshlrev_b32 v70, 16, v107\n\t"
    "v_and_b32 v71, 0xffff0000, v107\n\t"
    "v_pk_mul_f32 v[70:71], v[70:71], s[42:43]\n\t"
    "v_cvt_pk_bf16_f32 v107, v70, v71\n\t"
    "s_waitcnt lgkmcnt(12)\n\t"
    "s_add_u32 m0, %8, 9216\n\t"
    "s_add_u32 s40, %9, 9216\n\t"
    "buffer_load_dwordx4 v227, %3, s40 offen  lds\n\t"
    "v_lshlrev_b32 v72, 16, v108\n\t"
    "v_and_b32 v73, 0xffff0000, v108\n\t"
    "v_pk_mul_f32 v[72:73], v[72:73], s[42:43]\n\t"
    "v_cvt_pk_bf16_f32 v108, v72, v73\n\t"
    "v_lshlrev_b32 v74, 16, v109\n\t"
    "v_and_b32 v75, 0xffff0000, v109\n\t"
    "v_pk_mul_f32 v[74:75], v[74:75], s[42:43]\n\t"
    "v_cvt_pk_bf16_f32 v109, v74, v75\n\t"
    "v_lshlrev_b32 v76, 16, v110\n\t"
    "v_and_b32 v77, 0xffff0000, v110\n\t"
    "v_pk_mul_f32 v[76:77], v[76:77], s[42:43]\n\t"
    "v_cvt_pk_bf16_f32 v110, v76, v77\n\t"
    "v_lshlrev_b32 v78, 16, v111\n\t"
    "v_and_b32 v79, 0xffff0000, v111\n\t"
    "v_pk_mul_f32 v[78:79], v[78:79], s[42:43]\n\t"
    "v_cvt_pk_bf16_f32 v111, v78, v79\n\t"
    "s_waitcnt lgkmcnt(11)\n\t"
    "v_lshlrev_b32 v64, 16, v112\n\t"
    "v_and_b32 v65, 0xffff0000, v112\n\t"
    "v_pk_mul_f32 v[64:65], v[64:65], s[42:43]\n\t"
    "v_cvt_pk_bf16_f32 v112, v64, v65\n\t"
    "v_lshlrev_b32 v66, 16, v113\n\t"
    "v_and_b32 v67, 0xffff0000, v113\n\t"
    "v_pk_mul_f32 v[66:67], v[66:67], s[42:43]\n\t"
    "v_cvt_pk_bf16_f32 v113, v66, v67\n\t"
    "v_lshlrev_b32 v68, 16, v114\n\t"
    "v_and_b32 v69, 0xffff0000, v114\n\t"
    "v_pk_mul_f32 v[68:69], v[68:69], s[42:43]\n\t"
    "v_cvt_pk_bf16_f32 v114, v68, v69\n\t"
    "v_lshlrev_b32 v70, 16, v115\n\t"
    "v_and_b32 v71, 0xffff0000, v115\n\t"
    "v_pk_mul_f32 v[70:71], v[70:71], s[42:43]\n\t"
    "v_cvt_pk_bf16_f32 v115, v70, v71\n\t"
    "s_waitcnt lgkmcnt(10)\n\t"
    "v_lshlrev_b32 v72, 16, v116\n\t"
    "v_and_b32 v73, 0xffff0000, v116\n\t"
    "v_pk_mul_f32 v[72:73], v[72:73], s[42:43]\n\t"
    "v_cvt_pk_bf16_f32 v116, v72, v73\n\t"
    "v_lshlrev_b32 v74, 16, v117\n\t"
    "v_and_b32 v75, 0xffff0000, v117\n\t"
    "v_pk_mul_f32 v[74:75], v[74:75], s[42:43]\n\t"
    "v_cvt_pk_bf16_f32 v117, v74, v75\n\t"
    "v_lshlrev_b32 v76, 16, v118\n\t"
    "v_and_b32 v77, 0xffff0000, v118\n\t"
    "v_pk_mul_f32 v[76:77], v[76:77], s[42:43]\n\t"
    "v_cvt_pk_bf16_f32 v118, v76, v77\n\t"
    "v_lshlrev_b32 v78, 16, v119\n\t"
    "v_and_b32 v79, 0xffff0000, v119\n\t"
    "v_pk_mul_f32 v[78:79], v[78:79], s[42:43]\n\t"
    "v_cvt_pk_bf16_f32 v119, v78, v79\n\t"
    "v_add_u32 v196, %6, v17\n\t"
    "v_add_u32 v196, 65536, v196\n\t"
    "v_add_u32 v197, 4096, v196\n\t"
    "v_add_u32 v20, %6, v3\n\t"
    "v_add_u32 v20, 65536, v20\n\t"
    "v_add_u32 v21, 4096, v20\n\t"
    "v_xor_b32 v8, 0, v20\n\t"
    "v_xor_b32 v9, 64, v20\n\t"
    "v_xor_b32 v10, 128, v20\n\t"
    "v_xor_b32 v11, 192, v20\n\t"
    "v_xor_b32 v12, 0, v21\n\t"
    "v_xor_b32 v13, 64, v21\n\t"
    "v_xor_b32 v14, 128, v21\n\t"
    "v_xor_b32 v15, 192, v21\n\t"
    "s_waitcnt lgkmcnt(9)\n\t"
    "v_lshlrev_b32 v64, 16, v120\n\t"
    "v_and_b32 v65, 0xffff0000, v120\n\t"
    "v_pk_mul_f32 v[64:65], v[64:65], s[42:43]\n\t"
    "v_cvt_pk_bf16_f32 v120, v64, v65\n\t"
    "v_lshlrev_b32 v66, 16, v121\n\t"
    "v_and_b32 v67, 0xffff0000, v121\n\t"
    "v_pk_mul_f32 v[66:67], v[66:67], s[42:43]\n\t"
    "v_cvt_pk_bf16_f32 v121, v66, v67\n\t"
    "v_lshlrev_b32 v68, 16, v122\n\t"
    "v_and_b32 v69, 0xffff0000, v122\n\t"
    "v_pk_mul_f32 v[68:69], v[68:69], s[42:43]\n\t"
    "v_cvt_pk_bf16_f32 v122, v68, v69\n\t"
    "v_lshlrev_b32 v70, 16, v123\n\t"
    "v_and_b32 v71, 0xffff0000, v123\n\t"
    "v_pk_mul_f32 v[70:71], v[70:71], s[42:43]\n\t"
    "v_cvt_pk_bf16_f32 v123, v70, v71\n\t"
    "s_waitcnt lgkmcnt(8)\n\t"
    "v_lshlrev_b32 v72, 16, v124\n\t"
    "v_and_b32 v73, 0xffff0000, v124\n\t"
    "v_pk_mul_f32 v[72:73], v[72:73], s[42:43]\n\t"
    "v_cvt_pk_bf16_f32 v124, v72, v73\n\t"
    "v_lshlrev_b32 v74, 16, v125\n\t"
    "v_and_b32 v75, 0xffff0000, v125\n\t"
    "v_pk_mul_f32 v[74:75], v[74:75], s[42:43]\n\t"
    "v_cvt_pk_bf16_f32 v125, v74, v75\n\t"
    "v_lshlrev_b32 v76, 16, v126\n\t"
    "v_and_b32 v77, 0xffff0000, v126\n\t"
    "v_pk_mul_f32 v[76:77], v[76:77], s[42:43]\n\t"
    "v_cvt_pk_bf16_f32 v126, v76, v77\n\t"
    "v_lshlrev_b32 v78, 16, v127\n\t"
    "v_and_b32 v79, 0xffff0000, v127\n\t"
    "v_pk_mul_f32 v[78:79], v[78:79], s[42:43]\n\t"
    "v_cvt_pk_bf16_f32 v127, v78, v79\n\t"
    "s_waitcnt vmcnt(12)\n\t"
    "ds_read_b128 v[160:163], v8\n\t"
    "ds_read_b128 v[164:167], v9\n\t"
    "ds_read_b128 v[168:171], v10\n\t"
    "ds_read_b128 v[172:175], v11\n\t"
    "ds_read_b128 v[176:179], v12\n\t"
    "ds_read_b128 v[180:183], v13\n\t"
    "ds_read_b128 v[184:187], v14\n\t"
    "ds_read_b128 v[188:191], v15\n\t"
    "ds_write_b128 v194, v[96:99] offset:0\n\t"
    "ds_write_b128 v194, v[100:103] offset:1024\n\t"
    "ds_write_b128 v194, v[104:107] offset:2048\n\t"
    "ds_write_b128 v194, v[108:111] offset:3072\n\t"
    "ds_write_b128 v195, v[112:115] offset:0\n\t"
    "ds_write_b128 v195, v[116:119] offset:1024\n\t"
    "ds_write_b128 v195, v[120:123] offset:2048\n\t"
    "ds_write_b128 v195, v[124:127] offset:3072\n\t"
    "s_waitcnt lgkmcnt(15)\n\t"
    "v_lshlrev_b32 v64, 16, v160\n\t"
    "v_and_b32 v65, 0xffff0000, v160\n\t"
    "v_pk_mul_f32 v[64:65], v[64:65], s[42:43]\n\t"
    "v_cvt_pk_bf16_f32 v160, v64, v65\n\t"
    "v_lshlrev_b32 v66, 16, v161\n\t"
    "v_and_b32 v67, 0xffff0000, v161\n\t"
    "v_pk_mul_f32 v[66:67], v[66:67], s[42:43]\n\t"
    "v_cvt_pk_bf16_f32 v161, v66, v67\n\t"
    "v_lshlrev_b32 v68, 16, v162\n\t"
    "v_and_b32 v69, 0xffff0000, v162\n\t"
    "v_pk_mul_f32 v[68:69], v[68:69], s[42:43]\n\t"
    "v_cvt_pk_bf16_f32 v162, v68, v69\n\t"
    "v_lshlrev_b32 v70, 16, v163\n\t"
    "v_and_b32 v71, 0xffff0000, v163\n\t"
    "v_pk_mul_f32 v[70:71], v[70:71], s[42:43]\n\t"
    "v_cvt_pk_bf16_f32 v163, v70, v71\n\t"
    "s_waitcnt lgkmcnt(14)\n\t"
    "v_lshlrev_b32 v72, 16, v164\n\t"
    "v_and_b32 v73, 0xffff0000, v164\n\t"
    "v_pk_mul_f32 v[72:73], v[72:73], s[42:43]\n\t"
    "v_cvt_pk_bf16_f32 v164, v72, v73\n\t"
    "v_lshlrev_b32 v74, 16, v165\n\t"
    "v_and_b32 v75, 0xffff0000, v165\n\t"
    "v_pk_mul_f32 v[74:75], v[74:75], s[42:43]\n\t"
    "v_cvt_pk_bf16_f32 v165, v74, v75\n\t"
    "v_lshlrev_b32 v76, 16, v166\n\t"
    "v_and_b32 v77, 0xffff0000, v166\n\t"
    "v_pk_mul_f32 v[76:77], v[76:77], s[42:43]\n\t"
    "v_cvt_pk_bf16_f32 v166, v76, v77\n\t"
    "v_lshlrev_b32 v78, 16, v167\n\t"
    "v_and_b32 v79, 0xffff0000, v167\n\t"
    "v_pk_mul_f32 v[78:79], v[78:79], s[42:43]\n\t"
    "v_cvt_pk_bf16_f32 v167, v78, v79\n\t"
    "s_waitcnt lgkmcnt(13)\n\t"
    "v_lshlrev_b32 v64, 16, v168\n\t"
    "v_and_b32 v65, 0xffff0000, v168\n\t"
    "v_pk_mul_f32 v[64:65], v[64:65], s[42:43]\n\t"
    "v_cvt_pk_bf16_f32 v168, v64, v65\n\t"
    "v_lshlrev_b32 v66, 16, v169\n\t"
    "v_and_b32 v67, 0xffff0000, v169\n\t"
    "v_pk_mul_f32 v[66:67], v[66:67], s[42:43]\n\t"
    "v_cvt_pk_bf16_f32 v169, v66, v67\n\t"
    "v_lshlrev_b32 v68, 16, v170\n\t"
    "v_and_b32 v69, 0xffff0000, v170\n\t"
    "v_pk_mul_f32 v[68:69], v[68:69], s[42:43]\n\t"
    "v_cvt_pk_bf16_f32 v170, v68, v69\n\t"
    "v_lshlrev_b32 v70, 16, v171\n\t"
    "v_and_b32 v71, 0xffff0000, v171\n\t"
    "v_pk_mul_f32 v[70:71], v[70:71], s[42:43]\n\t"
    "v_cvt_pk_bf16_f32 v171, v70, v71\n\t"
    "s_waitcnt lgkmcnt(12)\n\t"
    "v_lshlrev_b32 v72, 16, v172\n\t"
    "v_and_b32 v73, 0xffff0000, v172\n\t"
    "v_pk_mul_f32 v[72:73], v[72:73], s[42:43]\n\t"
    "v_cvt_pk_bf16_f32 v172, v72, v73\n\t"
    "v_lshlrev_b32 v74, 16, v173\n\t"
    "v_and_b32 v75, 0xffff0000, v173\n\t"
    "v_pk_mul_f32 v[74:75], v[74:75], s[42:43]\n\t"
    "v_cvt_pk_bf16_f32 v173, v74, v75\n\t"
    "v_lshlrev_b32 v76, 16, v174\n\t"
    "v_and_b32 v77, 0xffff0000, v174\n\t"
    "v_pk_mul_f32 v[76:77], v[76:77], s[42:43]\n\t"
    "v_cvt_pk_bf16_f32 v174, v76, v77\n\t"
    "v_lshlrev_b32 v78, 16, v175\n\t"
    "v_and_b32 v79, 0xffff0000, v175\n\t"
    "v_pk_mul_f32 v[78:79], v[78:79], s[42:43]\n\t"
    "v_cvt_pk_bf16_f32 v175, v78, v79\n\t"
    "s_waitcnt lgkmcnt(11)\n\t"
    "v_lshlrev_b32 v64, 16, v176\n\t"
    "v_and_b32 v65, 0xffff0000, v176\n\t"
    "v_pk_mul_f32 v[64:65], v[64:65], s[42:43]\n\t"
    "v_cvt_pk_bf16_f32 v176, v64, v65\n\t"
    "v_lshlrev_b32 v66, 16, v177\n\t"
    "v_and_b32 v67, 0xffff0000, v177\n\t"
    "v_pk_mul_f32 v[66:67], v[66:67], s[42:43]\n\t"
    "v_cvt_pk_bf16_f32 v177, v66, v67\n\t"
    "v_lshlrev_b32 v68, 16, v178\n\t"
    "v_and_b32 v69, 0xffff0000, v178\n\t"
    "v_pk_mul_f32 v[68:69], v[68:69], s[42:43]\n\t"
    "v_cvt_pk_bf16_f32 v178, v68, v69\n\t"
    "v_lshlrev_b32 v70, 16, v179\n\t"
    "v_and_b32 v71, 0xffff0000, v179\n\t"
    "v_pk_mul_f32 v[70:71], v[70:71], s[42:43]\n\t"
    "v_cvt_pk_bf16_f32 v179, v70, v71\n\t"
    "s_waitcnt lgkmcnt(10)\n\t"
    "v_lshlrev_b32 v72, 16, v180\n\t"
    "v_and_b32 v73, 0xffff0000, v180\n\t"
    "v_pk_mul_f32 v[72:73], v[72:73], s[42:43]\n\t"
    "v_cvt_pk_bf16_f32 v180, v72, v73\n\t"
    "v_lshlrev_b32 v74, 16, v181\n\t"
    "v_and_b32 v75, 0xffff0000, v181\n\t"
    "v_pk_mul_f32 v[74:75], v[74:75], s[42:43]\n\t"
    "v_cvt_pk_bf16_f32 v181, v74, v75\n\t"
    "v_lshlrev_b32 v76, 16, v182\n\t"
    "v_and_b32 v77, 0xffff0000, v182\n\t"
    "v_pk_mul_f32 v[76:77], v[76:77], s[42:43]\n\t"
    "v_cvt_pk_bf16_f32 v182, v76, v77\n\t"
    "v_lshlrev_b32 v78, 16, v183\n\t"
    "v_and_b32 v79, 0xffff0000, v183\n\t"
    "v_pk_mul_f32 v[78:79], v[78:79], s[42:43]\n\t"
    "v_cvt_pk_bf16_f32 v183, v78, v79\n\t"
    "s_waitcnt lgkmcnt(9)\n\t"
    "v_lshlrev_b32 v64, 16, v184\n\t"
    "v_and_b32 v65, 0xffff0000, v184\n\t"
    "v_pk_mul_f32 v[64:65], v[64:65], s[42:43]\n\t"
    "v_cvt_pk_bf16_f32 v184, v64, v65\n\t"
    "v_lshlrev_b32 v66, 16, v185\n\t"
    "v_and_b32 v67, 0xffff0000, v185\n\t"
    "v_pk_mul_f32 v[66:67], v[66:67], s[42:43]\n\t"
    "v_cvt_pk_bf16_f32 v185, v66, v67\n\t"
    "v_lshlrev_b32 v68, 16, v186\n\t"
    "v_and_b32 v69, 0xffff0000, v186\n\t"
    "v_pk_mul_f32 v[68:69], v[68:69], s[42:43]\n\t"
    "v_cvt_pk_bf16_f32 v186, v68, v69\n\t"
    "v_lshlrev_b32 v70, 16, v187\n\t"
    "v_and_b32 v71, 0xffff0000, v187\n\t"
    "v_pk_mul_f32 v[70:71], v[70:71], s[42:43]\n\t"
    "v_cvt_pk_bf16_f32 v187, v70, v71\n\t"
    "s_waitcnt lgkmcnt(8)\n\t"
    "v_lshlrev_b32 v72, 16, v188\n\t"
    "v_and_b32 v73, 0xffff0000, v188\n\t"
    "v_pk_mul_f32 v[72:73], v[72:73], s[42:43]\n\t"
    "v_cvt_pk_bf16_f32 v188, v72, v73\n\t"
    "v_lshlrev_b32 v74, 16, v189\n\t"
    "v_and_b32 v75, 0xffff0000, v189\n\t"
    "v_pk_mul_f32 v[74:75], v[74:75], s[42:43]\n\t"
    "v_cvt_pk_bf16_f32 v189, v74, v75\n\t"
    "v_lshlrev_b32 v76, 16, v190\n\t"
    "v_and_b32 v77, 0xffff0000, v190\n\t"
    "v_pk_mul_f32 v[76:77], v[76:77], s[42:43]\n\t"
    "v_cvt_pk_bf16_f32 v190, v76, v77\n\t"
    "v_lshlrev_b32 v78, 16, v191\n\t"
    "v_and_b32 v79, 0xffff0000, v191\n\t"
    "v_pk_mul_f32 v[78:79], v[78:79], s[42:43]\n\t"
    "v_cvt_pk_bf16_f32 v191, v78, v79\n\t"
    "ds_write_b128 v196, v[160:163] offset:0\n\t"
    "ds_write_b128 v196, v[164:167] offset:1024\n\t"
    "ds_write_b128 v196, v[168:171] offset:2048\n\t"
    "ds_write_b128 v196, v[172:175] offset:3072\n\t"
    "ds_write_b128 v197, v[176:179] offset:0\n\t"
    "ds_write_b128 v197, v[180:183] offset:1024\n\t"
    "ds_write_b128 v197, v[184:187] offset:2048\n\t"
    "ds_write_b128 v197, v[188:191] offset:3072\n\t"
    "s_waitcnt vmcnt(4)\n\t"
    "ds_read_b128 a[0:3], v230 offset:0\n\t"
    "ds_read_b128 a[4:7], v231 offset:0\n\t"
    "ds_read_b128 a[8:11], v230 offset:4096\n\t"
    "ds_read_b128 a[12:15], v231 offset:4096\n\t"
    "ds_read_b128 a[16:19], v230 offset:2048\n\t"
    "ds_read_b128 a[20:23], v231 offset:2048\n\t"
    "ds_read_b128 a[24:27], v230 offset:6144\n\t"
    "ds_read_b128 a[28:31], v231 offset:6144\n\t"
    "v_xor_b32 v237, 32, v234\n\t"
    "v_xor_b32 v246, 64, v234\n\t"
    "v_xor_b32 v247, 96, v234\n\t"
    "v_and_b32 v0, 3, v245\n\t"
    "v_cmp_eq_u32 vcc, 0, v0\n\t"
    "v_mov_b32 v4, 0x3f803f80\n\t"
    "s_nop 1\n\t"
    "v_cndmask_b32 v252, 0, v4, vcc\n\t"
    "v_cndmask_b32 v253, 0, v4, vcc\n\t"
    "v_cndmask_b32 v254, 0, v4, vcc\n\t"
    "v_cndmask_b32 v255, 0, v4, vcc\n\t"
    "v_mov_b32 v248, v227\n\t"
    "v_add_u32 v227, 896, v227\n\t"
    "v_add_u32 v249, 1024, v229\n\t"
    "v_add_u32 v250, 2048, v228\n\t"
    "v_add_u32 v251, 3072, v229\n\t"
    "v_add_u32 v245, 2048, v235\n\t"
    "s_waitcnt vmcnt(0) lgkmcnt(0)\n\t"
    "s_barrier\n\t"
    "v_add_u32 v18, %5, v17\n\t"
    "v_add_u32 v18, 122880, v18\n\t"
    "ds_read_b128 a[192:195], v18 offset:0\n\t"
    "ds_read_b128 a[196:199], v18 offset:1024\n\t"
    "ds_read_b128 a[200:203], v18 offset:2048\n\t"
    "ds_read_b128 a[204:207], v18 offset:3072\n\t"
    "ds_read_b128 a[208:211], v18 offset:12288\n\t"
    "ds_read_b128 a[212:215], v18 offset:13312\n\t"
    "ds_read_b128 a[216:219], v18 offset:14336\n\t"
    "ds_read_b128 a[220:223], v18 offset:15360\n\t"
    "ds_read_b128 a[224:227], v18 offset:24576\n\t"
    "ds_read_b128 a[228:231], v18 offset:25600\n\t"
    "ds_read_b128 a[232:235], v18 offset:26624\n\t"
    "ds_read_b128 a[236:239], v18 offset:27648\n\t"
    "ds_read_b128 a[240:243], v18 offset:36864\n\t"
    "ds_read_b128 a[244:247], v18 offset:37888\n\t"
    "ds_read_b128 a[248:251], v18 offset:38912\n\t"
    "ds_read_b128 a[252:255], v18 offset:39936\n\t"
    "s_waitcnt lgkmcnt(0)\n\t"
    ".p2align 6\n\t"
    "L_main_%=:\n\t"
    "s_add_u32 m0, %10, 0\n\t"
    "s_add_u32 s40, %11, 8192\n\t"
    "buffer_load_dwordx4 v228, %1, s40 offen offset:0 nt lds\n\t"
    "v_mfma_f32_16x16x32_bf16 v[128:131], a[192:195], a[0:3], 0\n\t"
    "s_barrier\n\t"
    "ds_read_b64_tr_b16 a[128:129], v226 offset:0\n\t"
    "v_mfma_f32_16x16x32_bf16 v[128:131], a[196:199], a[4:7], v[128:131]\n\t"
    "ds_read_b64_tr_b16 a[130:131], v226 offset:4096\n\t"
    "v_mfma_f32_16x16x32_bf16 v[128:131], a[200:203], a[8:11], v[128:131]\n\t"
    "ds_read_b64_tr_b16 a[132:133], v225 offset:0\n\t"
    "v_mfma_f32_16x16x32_bf16 v[128:131], a[204:207], a[12:15], v[128:131]\n\t"
    "ds_read_b64_tr_b16 a[134:135], v225 offset:4096\n\t"
    "v_mfma_f32_16x16x32_bf16 v[132:135], a[208:211], a[0:3], 0\n\t"
    "ds_read_b64_tr_b16 a[136:137], v226 offset:512\n\t"
    "v_mfma_f32_16x16x32_bf16 v[132:135], a[212:215], a[4:7], v[132:135]\n\t"
    "ds_read_b64_tr_b16 a[138:139], v226 offset:4608\n\t"
    "s_nop 2\n\t"
    "v_max3_f32 v229, v128, v129, v130\n\t"
    "v_mfma_f32_16x16x32_bf16 v[132:135], a[216:219], a[8:11], v[132:135]\n\t"
    "ds_read_b64_tr_b16 a[140:141], v225 offset:512\n\t"
    "v_mfma_f32_16x16x32_bf16 v[132:135], a[220:223], a[12:15], v[132:135]\n\t"
    "buffer_load_dwordx4 v249, %1, s40 offen offset:1024 nt lds\n\t"
    "v_mfma_f32_16x16x32_bf16 v[136:139], a[192:195], a[16:19], 0\n\t"
    "ds_read_b64_tr_b16 a[142:143], v225 offset:4608\n\t"
    "v_mfma_f32_16x16x32_bf16 v[136:139], a[196:199], a[20:23], v[136:139]\n\t"
    "ds_read_b64_tr_b16 a[144:145], v226 offset:1024\n\t"
    "s_nop 2\n\t"
    "v_max3_f32 v232, v131, v132, v133\n\t"
    "v_mfma_f32_16x16x32_bf16 v[136:139], a[200:203], a[24:27], v[136:139]\n\t"
    "ds_read_b64_tr_b16 a[146:147], v226 offset:5120\n\t"
    "v_max3_f32 v229, v229, v134, v135\n\t"
    "v_mfma_f32_16x16x32_bf16 v[136:139], a[204:207], a[28:31], v[136:139]\n\t"
    "ds_read_b64_tr_b16 a[148:149], v225 offset:1024\n\t"
    "v_max_f32 v229, v229, v232\n\t"
    "v_mfma_f32_16x16x32_bf16 v[140:143], a[208:211], a[16:19], 0\n\t"
    "ds_read_b64_tr_b16 a[150:151], v225 offset:5120\n\t"
    "v_mov_b32 v232, v229\n\t"
    "v_mfma_f32_16x16x32_bf16 v[140:143], a[212:215], a[20:23], v[140:143]\n\t"
    "ds_read_b64_tr_b16 a[152:153], v226 offset:1536\n\t"
    "v_permlane16_swap_b32 v229, v232\n\t"
    "v_mfma_f32_16x16x32_bf16 v[140:143], a[216:219], a[24:27], v[140:143]\n\t"
    "ds_read_b64_tr_b16 a[154:155], v226 offset:5632\n\t"
    "v_max_f32 v229, v229, v232\n\t"
    "v_mfma_f32_16x16x32_bf16 v[140:143], a[220:223], a[28:31], v[140:143]\n\t"
    "buffer_load_dwordx4 v250, %1, s40 offen offset:2048 nt lds\n\t"
    "ds_read_b64_tr_b16 a[156:157], v225 offset:1536\n\t"
    "ds_read_b64_tr_b16 a[158:159], v225 offset:5632\n\t"
    "v_mov_b32 v232, v229\n\t"
    "v_max3_f32 v233, v136, v137, v138\n\t"
    "s_nop 0\n\t"
    "v_permlane32_swap_b32 v229, v232\n\t"
    "v_min_f32_e64 v192, -v229, -v232\n\t"
    "v_min_f32_e64 v193, -v229, -v232\n\t"
    "v_min_f32_e64 v194, -v229, -v232\n\t"
    "v_min_f32_e64 v195, -v229, -v232\n\t"
    "v_max3_f32 v241, v139, v140, v141\n\t"
    "v_max3_f32 v233, v233, v142, v143\n\t"
    "v_mfma_f32_16x16x32_bf16 v[144:147], a[224:227], a[0:3], v[192:195]\n\t"
    "ds_read_b128 a[96:99], v224 offset:0\n\t"
    "v_max_f32 v233, v233, v241\n\t"
    "v_mfma_f32_16x16x32_bf16 v[144:147], a[228:231], a[4:7], v[144:147]\n\t"
    "ds_read_b128 a[100:103], v224 offset:1024\n\t"
    "v_mov_b32 v241, v233\n\t"
    "v_mfma_f32_16x16x32_bf16 v[144:147], a[232:235], a[8:11], v[144:147]\n\t"
    "ds_read_b128 a[104:107], v224 offset:2048\n\t"
    "v_permlane16_swap_b32 v233, v241\n\t"
    "v_mfma_f32_16x16x32_bf16 v[144:147], a[236:239], a[12:15], v[144:147]\n\t"
    "ds_read_b128 a[108:111], v224 offset:3072\n\t"
    "v_max_f32 v233, v233, v241\n\t"
    "v_mfma_f32_16x16x32_bf16 v[148:151], a[240:243], a[0:3], v[192:195]\n\t"
    "ds_read_b128 a[112:115], v224 offset:4096\n\t"
    "v_mov_b32 v241, v233\n\t"
    "v_mfma_f32_16x16x32_bf16 v[148:151], a[244:247], a[4:7], v[148:151]\n\t"
    "ds_read_b128 a[116:119], v224 offset:5120\n\t"
    "v_permlane32_swap_b32 v233, v241\n\t"
    "v_mfma_f32_16x16x32_bf16 v[148:151], a[248:251], a[8:11], v[148:151]\n\t"
    "ds_read_b128 a[120:123], v224 offset:6144\n\t"
    "v_min_f32_e64 v196, -v233, -v241\n\t"
    "v_mfma_f32_16x16x32_bf16 v[148:151], a[252:255], a[12:15], v[148:151]\n\t"
    "buffer_load_dwordx4 v251, %1, s40 offen offset:3072 nt lds\n\t"
    "s_add_u32 m0, %8, 16384\n\t"
    "s_add_u32 s40, %9, 16384\n\t"
    "buffer_load_dwordx4 v227, %3, s40 offen offset:0  lds\n\t"
    "buffer_load_dwordx4 v248, %3, s40 offen offset:1024  lds\n\t"
    "s_add_u32 m0, %8, 24576\n\t"
    "s_add_u32 s40, %9, 24576\n\t"
    "v_min_f32_e64 v197, -v233, -v241\n\t"
    "v_min_f32_e64 v198, -v233, -v241\n\t"
    "v_min_f32_e64 v199, -v233, -v241\n\t"
    "v_add_f32 v128, v192, v128\n\t"
    "v_add_f32 v129, v193, v129\n\t"
    "v_mfma_f32_16x16x32_bf16 v[152:155], a[224:227], a[16:19], v[196:199]\n\t"
    "buffer_load_dwordx4 v227, %3, s40 offen offset:0  lds\n\t"
    "buffer_load_dwordx4 v248, %3, s40 offen offset:1024  lds\n\t"
    "v_mfma_f32_16x16x32_bf16 v[152:155], a[228:231], a[20:23], v[152:155]\n\t"
    "ds_read_b128 a[124:127], v224 offset:7168\n\t"
    "v_add_f32 v130, v194, v130\n\t"
    "v_mfma_f32_16x16x32_bf16 v[152:155], a[232:235], a[24:27], v[152:155]\n\t"
    "v_add_f32 v131, v195, v131\n\t"
    "v_add_f32 v132, v192, v132\n\t"
    "v_mfma_f32_16x16x32_bf16 v[152:155], a[236:239], a[28:31], v[152:155]\n\t"
    "v_exp_f32 v128, v128\n\t"
    "v_mfma_f32_16x16x32_bf16 v[156:159], a[240:243], a[16:19], v[196:199]\n\t"
    "v_exp_f32 v129, v129\n\t"
    "v_mfma_f32_16x16x32_bf16 v[156:159], a[244:247], a[20:23], v[156:159]\n\t"
    "v_add_f32 v133, v193, v133\n\t"
    "v_add_f32 v134, v194, v134\n\t"
    "v_mfma_f32_16x16x32_bf16 v[156:159], a[248:251], a[24:27], v[156:159]\n\t"
    "v_add_f32 v135, v195, v135\n\t"
    "v_cvt_pk_bf16_f32 v128, v128, v129\n\t"
    "v_mfma_f32_16x16x32_bf16 v[156:159], a[252:255], a[28:31], v[156:159]\n\t"
    "s_waitcnt lgkmcnt(1)\n\t"
    "v_exp_f32 v130, v130\n\t"
    "v_exp_f32 v131, v131\n\t"
    "v_exp_f32 v132, v132\n\t"
    "v_exp_f32 v133, v133\n\t"
    "v_exp_f32 v134, v134\n\t"
    "v_exp_f32 v135, v135\n\t"
    "v_cvt_pk_bf16_f32 v129, v130, v131\n\t"
    "v_cvt_pk_bf16_f32 v130, v132, v133\n\t"
    "v_cvt_pk_bf16_f32 v131, v134, v135\n\t"
    "v_add_f32 v136, v196, v136\n\t"
    "v_add_f32 v137, v197, v137\n\t"
    "v_mfma_f32_16x16x32_bf16 v[0:3], a[128:131], v[128:131], 0\n\t"
    "v_add_f32 v138, v198, v138\n\t"
    "v_add_f32 v139, v199, v139\n\t"
    "v_mfma_f32_16x16x32_bf16 v[4:7], a[132:135], v[128:131], 0\n\t"
    "v_exp_f32 v136, v136\n\t"
    "v_mfma_f32_16x16x32_bf16 v[8:11], a[136:139], v[128:131], 0\n\t"
    "v_exp_f32 v137, v137\n\t"
    "v_mfma_f32_16x16x32_bf16 v[12:15], a[140:143], v[128:131], 0\n\t"
    "v_add_f32 v140, v196, v140\n\t"
    "v_add_f32 v141, v197, v141\n\t"
    "v_mfma_f32_16x16x32_bf16 v[16:19], a[144:147], v[128:131], 0\n\t"
    "v_add_f32 v142, v198, v142\n\t"
    "v_add_f32 v143, v199, v143\n\t"
    "v_mfma_f32_16x16x32_bf16 v[20:23], a[148:151], v[128:131], 0\n\t"
    "v_exp_f32 v138, v138\n\t"
    "v_mfma_f32_16x16x32_bf16 v[24:27], a[152:155], v[128:131], 0\n\t"
    "v_exp_f32 v139, v139\n\t"
    "v_mfma_f32_16x16x32_bf16 v[28:31], a[156:159], v[128:131], 0\n\t"
    "v_exp_f32 v140, v140\n\t"
    "v_exp_f32 v141, v141\n\t"
    "v_exp_f32 v142, v142\n\t"
    "v_exp_f32 v143, v143\n\t"
    "v_cvt_pk_bf16_f32 v136, v136, v137\n\t"
    "v_cvt_pk_bf16_f32 v137, v138, v139\n\t"
    "v_cvt_pk_bf16_f32 v138, v140, v141\n\t"
    "v_cvt_pk_bf16_f32 v139, v142, v143\n\t"
    "v_exp_f32 v144, v144\n\t"
    "s_nop 0\n\t"
    "v_mfma_f32_16x16x32_bf16 v[32:35], a[128:131], v[136:139], 0\n\t"
    "ds_read_b64_tr_b16 a[128:129], v226 offset:8192\n\t"
    "ds_read_b64_tr_b16 a[130:131], v226 offset:12288\n\t"
    "v_mfma_f32_16x16x32_bf16 v[36:39], a[132:135], v[136:139], 0\n\t"
    "ds_read_b64_tr_b16 a[132:133], v225 offset:8192\n\t"
    "ds_read_b64_tr_b16 a[134:135], v225 offset:12288\n\t"
    "v_mfma_f32_16x16x32_bf16 v[40:43], a[136:139], v[136:139], 0\n\t"
    "ds_read_b64_tr_b16 a[136:137], v226 offset:8704\n\t"
    "ds_read_b64_tr_b16 a[138:139], v226 offset:12800\n\t"
    "v_mfma_f32_16x16x32_bf16 v[44:47], a[140:143], v[136:139], 0\n\t"
    "ds_read_b64_tr_b16 a[140:141], v225 offset:8704\n\t"
    "ds_read_b64_tr_b16 a[142:143], v225 offset:12800\n\t"
    "v_mfma_f32_16x16x32_bf16 v[48:51], a[144:147], v[136:139], 0\n\t"
    "s_add_u32 m0, %10, 4096\n\t"
    "s_add_u32 s40, %11, 8320\n\t"
    "buffer_load_dwordx4 v228, %1, s40 offen offset:0 nt lds\n\t"
    "ds_read_b64_tr_b16 a[144:145], v226 offset:9216\n\t"
    "ds_read_b64_tr_b16 a[146:147], v226 offset:13312\n\t"
    "v_mfma_f32_16x16x32_bf16 v[52:55], a[148:151], v[136:139], 0\n\t"
    "ds_read_b64_tr_b16 a[148:149], v225 offset:9216\n\t"
    "ds_read_b64_tr_b16 a[150:151], v225 offset:13312\n\t"
    "v_mfma_f32_16x16x32_bf16 v[56:59], a[152:155], v[136:139], 0\n\t"
    "ds_read_b64_tr_b16 a[152:153], v226 offset:9728\n\t"
    "ds_read_b64_tr_b16 a[154:155], v226 offset:13824\n\t"
    "v_mfma_f32_16x16x32_bf16 v[60:63], a[156:159], v[136:139], 0\n\t"
    "ds_read_b64_tr_b16 a[156:157], v225 offset:9728\n\t"
    "ds_read_b64_tr_b16 a[158:159], v225 offset:13824\n\t"
    "v_mfma_f32_16x16x32_bf16 v[208:211], v[252:255], v[128:131], 0\n\t"
    "v_exp_f32 v145, v145\n\t"
    "v_mfma_f32_16x16x32_bf16 v[212:215], v[252:255], v[136:139], 0\n\t"
    "v_exp_f32 v146, v146\n\t"
    "v_mfma_f32_16x16x32_bf16 v[128:131], a[96:99], a[0:3], v[192:195]\n\t"
    "v_exp_f32 v147, v147\n\t"
    "v_mfma_f32_16x16x32_bf16 v[128:131], a[100:103], a[4:7], v[128:131]\n\t"
    "v_exp_f32 v148, v148\n\t"
    "v_mfma_f32_16x16x32_bf16 v[128:131], a[104:107], a[8:11], v[128:131]\n\t"
    "buffer_load_dwordx4 v249, %1, s40 offen offset:1024 nt lds\n\t"
    "v_mfma_f32_16x16x32_bf16 v[128:131], a[108:111], a[12:15], v[128:131]\n\t"
    "v_exp_f32 v149, v149\n\t"
    "v_mfma_f32_16x16x32_bf16 v[132:135], a[112:115], a[0:3], v[192:195]\n\t"
    "v_exp_f32 v150, v150\n\t"
    "v_mfma_f32_16x16x32_bf16 v[132:135], a[116:119], a[4:7], v[132:135]\n\t"
    "v_exp_f32 v151, v151\n\t"
    "v_mfma_f32_16x16x32_bf16 v[132:135], a[120:123], a[8:11], v[132:135]\n\t"
    "s_waitcnt lgkmcnt(8)\n\t"
    "v_cvt_pk_bf16_f32 v144, v144, v145\n\t"
    "v_cvt_pk_bf16_f32 v145, v146, v147\n\t"
    "v_mfma_f32_16x16x32_bf16 v[132:135], a[124:127], a[12:15], v[132:135]\n\t"
    "v_cvt_pk_bf16_f32 v146, v148, v149\n\t"
    "v_cvt_pk_bf16_f32 v147, v150, v151\n\t"
    "v_mfma_f32_16x16x32_bf16 v[136:139], a[96:99], a[16:19], v[196:199]\n\t"
    "ds_read_b128 a[96:99], v224 offset:8192\n\t"
    "v_mfma_f32_16x16x32_bf16 v[136:139], a[100:103], a[20:23], v[136:139]\n\t"
    "ds_read_b128 a[100:103], v224 offset:9216\n\t"
    "v_mfma_f32_16x16x32_bf16 v[136:139], a[104:107], a[24:27], v[136:139]\n\t"
    "buffer_load_dwordx4 v250, %1, s40 offen offset:2048 nt lds\n\t"
    "v_mfma_f32_16x16x32_bf16 v[136:139], a[108:111], a[28:31], v[136:139]\n\t"
    "ds_read_b128 a[104:107], v224 offset:10240\n\t"
    "v_mfma_f32_16x16x32_bf16 v[140:143], a[112:115], a[16:19], v[196:199]\n\t"
    "ds_read_b128 a[108:111], v224 offset:11264\n\t"
    "v_mfma_f32_16x16x32_bf16 v[140:143], a[116:119], a[20:23], v[140:143]\n\t"
    "ds_read_b128 a[112:115], v224 offset:12288\n\t"
    "v_mfma_f32_16x16x32_bf16 v[140:143], a[120:123], a[24:27], v[140:143]\n\t"
    "ds_read_b128 a[116:119], v224 offset:13312\n\t"
    "v_mfma_f32_16x16x32_bf16 v[140:143], a[124:127], a[28:31], v[140:143]\n\t"
    "ds_read_b128 a[120:123], v224 offset:14336\n\t"
    "v_mfma_f32_16x16x32_bf16 v[0:3], a[128:131], v[144:147], v[0:3]\n\t"
    "ds_read_b128 a[124:127], v224 offset:15360\n\t"
    "v_mfma_f32_16x16x32_bf16 v[4:7], a[132:135], v[144:147], v[4:7]\n\t"
    "v_exp_f32 v152, v152\n\t"
    "v_mfma_f32_16x16x32_bf16 v[8:11], a[136:139], v[144:147], v[8:11]\n\t"
    "buffer_load_dwordx4 v251, %1, s40 offen offset:3072 nt lds\n\t"
    "v_mfma_f32_16x16x32_bf16 v[12:15], a[140:143], v[144:147], v[12:15]\n\t"
    "s_waitcnt lgkmcnt(8)\n\t"
    "v_exp_f32 v153, v153\n\t"
    "v_mfma_f32_16x16x32_bf16 v[16:19], a[144:147], v[144:147], v[16:19]\n\t"
    "v_exp_f32 v154, v154\n\t"
    "v_mfma_f32_16x16x32_bf16 v[20:23], a[148:151], v[144:147], v[20:23]\n\t"
    "v_exp_f32 v155, v155\n\t"
    "v_mfma_f32_16x16x32_bf16 v[24:27], a[152:155], v[144:147], v[24:27]\n\t"
    "v_exp_f32 v156, v156\n\t"
    "v_mfma_f32_16x16x32_bf16 v[28:31], a[156:159], v[144:147], v[28:31]\n\t"
    "v_exp_f32 v157, v157\n\t"
    "v_exp_f32 v158, v158\n\t"
    "v_exp_f32 v159, v159\n\t"
    "v_cvt_pk_bf16_f32 v152, v152, v153\n\t"
    "v_cvt_pk_bf16_f32 v153, v154, v155\n\t"
    "v_cvt_pk_bf16_f32 v154, v156, v157\n\t"
    "v_cvt_pk_bf16_f32 v155, v158, v159\n\t"
    "v_exp_f32 v128, v128\n\t"
    "s_nop 0\n\t"
    "v_mfma_f32_16x16x32_bf16 v[32:35], a[128:131], v[152:155], v[32:35]\n\t"
    "v_exp_f32 v129, v129\n\t"
    "v_mfma_f32_16x16x32_bf16 v[36:39], a[132:135], v[152:155], v[36:39]\n\t"
    "v_exp_f32 v130, v130\n\t"
    "v_mfma_f32_16x16x32_bf16 v[40:43], a[136:139], v[152:155], v[40:43]\n\t"
    "v_exp_f32 v131, v131\n\t"
    "v_mfma_f32_16x16x32_bf16 v[44:47], a[140:143], v[152:155], v[44:47]\n\t"
    "v_exp_f32 v132, v132\n\t"
    "v_mfma_f32_16x16x32_bf16 v[48:51], a[144:147], v[152:155], v[48:51]\n\t"
    "v_exp_f32 v133, v133\n\t"
    "v_mfma_f32_16x16x32_bf16 v[52:55], a[148:151], v[152:155], v[52:55]\n\t"
    "v_exp_f32 v134, v134\n\t"
    "v_mfma_f32_16x16x32_bf16 v[56:59], a[152:155], v[152:155], v[56:59]\n\t"
    "v_exp_f32 v135, v135\n\t"
    "v_mfma_f32_16x16x32_bf16 v[60:63], a[156:159], v[152:155], v[60:63]\n\t"
    "v_cvt_pk_bf16_f32 v128, v128, v129\n\t"
    "v_cvt_pk_bf16_f32 v129, v130, v131\n\t"
    "v_mfma_f32_16x16x32_bf16 v[208:211], v[252:255], v[144:147], v[208:211]\n\t"
    "v_cvt_pk_bf16_f32 v130, v132, v133\n\t"
    "v_cvt_pk_bf16_f32 v131, v134, v135\n\t"
    "v_mfma_f32_16x16x32_bf16 v[212:215], v[252:255], v[152:155], v[212:215]\n\t"
    "s_waitcnt lgkmcnt(0)\n\t"
    "v_exp_f32 v136, v136\n\t"
    "v_mfma_f32_16x16x32_bf16 v[144:147], a[96:99], a[0:3], v[192:195]\n\t"
    "v_exp_f32 v137, v137\n\t"
    "v_mfma_f32_16x16x32_bf16 v[144:147], a[100:103], a[4:7], v[144:147]\n\t"
    "s_waitcnt vmcnt(4)\n\t"
    "s_barrier\n\t"
    "s_add_u32 m0, %8, 0\n\t"
    "s_add_u32 s40, %9, 32768\n\t"
    "buffer_load_dwordx4 v227, %3, s40 offen offset:0  lds\n\t"
    "buffer_load_dwordx4 v248, %3, s40 offen offset:1024  lds\n\t"
    "s_add_u32 m0, %8, 8192\n\t"
    "s_add_u32 s40, %9, 40960\n\t"
    "buffer_load_dwordx4 v227, %3, s40 offen offset:0  lds\n\t"
    "v_mfma_f32_16x16x32_bf16 v[144:147], a[104:107], a[8:11], v[144:147]\n\t"
    "buffer_load_dwordx4 v248, %3, s40 offen offset:1024  lds\n\t"
    "v_mfma_f32_16x16x32_bf16 v[144:147], a[108:111], a[12:15], v[144:147]\n\t"
    "ds_read_b64_tr_b16 a[128:129], v226 offset:16384\n\t"
    "v_cvt_pk_bf16_f32 v136, v136, v137\n\t"
    "v_mfma_f32_16x16x32_bf16 v[148:151], a[112:115], a[0:3], v[192:195]\n\t"
    "ds_read_b64_tr_b16 a[130:131], v226 offset:20480\n\t"
    "v_mfma_f32_16x16x32_bf16 v[148:151], a[116:119], a[4:7], v[148:151]\n\t"
    "ds_read_b64_tr_b16 a[132:133], v225 offset:16384\n\t"
    "ds_read_b64_tr_b16 a[134:135], v225 offset:20480\n\t"
    "v_mfma_f32_16x16x32_bf16 v[148:151], a[120:123], a[8:11], v[148:151]\n\t"
    "ds_read_b64_tr_b16 a[136:137], v226 offset:16896\n\t"
    "ds_read_b64_tr_b16 a[138:139], v226 offset:20992\n\t"
    "v_mfma_f32_16x16x32_bf16 v[148:151], a[124:127], a[12:15], v[148:151]\n\t"
    "ds_read_b64_tr_b16 a[140:141], v225 offset:16896\n\t"
    "ds_read_b64_tr_b16 a[142:143], v225 offset:20992\n\t"
    "v_mfma_f32_16x16x32_bf16 v[152:155], a[96:99], a[16:19], v[196:199]\n\t"
    "ds_read_b64_tr_b16 a[144:145], v226 offset:17408\n\t"
    "ds_read_b64_tr_b16 a[146:147], v226 offset:21504\n\t"
    "v_mfma_f32_16x16x32_bf16 v[152:155], a[100:103], a[20:23], v[152:155]\n\t"
    "ds_read_b64_tr_b16 a[148:149], v225 offset:17408\n\t"
    "ds_read_b64_tr_b16 a[150:151], v225 offset:21504\n\t"
    "v_mfma_f32_16x16x32_bf16 v[152:155], a[104:107], a[24:27], v[152:155]\n\t"
    "ds_read_b64_tr_b16 a[152:153], v226 offset:17920\n\t"
    "ds_read_b64_tr_b16 a[154:155], v226 offset:22016\n\t"
    "v_mfma_f32_16x16x32_bf16 v[152:155], a[108:111], a[28:31], v[152:155]\n\t"
    "ds_read_b64_tr_b16 a[156:157], v225 offset:17920\n\t"
    "ds_read_b64_tr_b16 a[158:159], v225 offset:22016\n\t"
    "v_mfma_f32_16x16x32_bf16 v[156:159], a[112:115], a[16:19], v[196:199]\n\t"
    "ds_read_b128 a[96:99], v224 offset:16384\n\t"
    "v_mfma_f32_16x16x32_bf16 v[156:159], a[116:119], a[20:23], v[156:159]\n\t"
    "ds_read_b128 a[100:103], v224 offset:17408\n\t"
    "v_mfma_f32_16x16x32_bf16 v[156:159], a[120:123], a[24:27], v[156:159]\n\t"
    "ds_read_b128 a[104:107], v224 offset:18432\n\t"
    "v_mfma_f32_16x16x32_bf16 v[156:159], a[124:127], a[28:31], v[156:159]\n\t"
    "s_waitcnt lgkmcnt(15)\n\t"
    "ds_read_b128 a[108:111], v224 offset:19456\n\t"
    "v_mfma_f32_16x16x32_bf16 v[0:3], a[128:131], v[128:131], v[0:3]\n\t"
    "ds_read_b128 a[112:115], v224 offset:20480\n\t"
    "v_mfma_f32_16x16x32_bf16 v[4:7], a[132:135], v[128:131], v[4:7]\n\t"
    "s_waitcnt lgkmcnt(15)\n\t"
    "ds_read_b128 a[116:119], v224 offset:21504\n\t"
    "v_mfma_f32_16x16x32_bf16 v[8:11], a[136:139], v[128:131], v[8:11]\n\t"
    "s_waitcnt lgkmcnt(14)\n\t"
    "ds_read_b128 a[120:123], v224 offset:22528\n\t"
    "v_mfma_f32_16x16x32_bf16 v[12:15], a[140:143], v[128:131], v[12:15]\n\t"
    "s_waitcnt lgkmcnt(13)\n\t"
    "ds_read_b128 a[124:127], v224 offset:23552\n\t"
    "v_mfma_f32_16x16x32_bf16 v[16:19], a[144:147], v[128:131], v[16:19]\n\t"
    "s_waitcnt lgkmcnt(12)\n\t"
    "v_exp_f32 v138, v138\n\t"
    "v_mfma_f32_16x16x32_bf16 v[20:23], a[148:151], v[128:131], v[20:23]\n\t"
    "s_waitcnt lgkmcnt(10)\n\t"
    "v_exp_f32 v139, v139\n\t"
    "v_mfma_f32_16x16x32_bf16 v[24:27], a[152:155], v[128:131], v[24:27]\n\t"
    "s_waitcnt lgkmcnt(8)\n\t"
    "v_exp_f32 v140, v140\n\t"
    "v_mfma_f32_16x16x32_bf16 v[28:31], a[156:159], v[128:131], v[28:31]\n\t"
    "v_exp_f32 v141, v141\n\t"
    "v_exp_f32 v142, v142\n\t"
    "v_exp_f32 v143, v143\n\t"
    "v_cvt_pk_bf16_f32 v137, v138, v139\n\t"
    "v_cvt_pk_bf16_f32 v138, v140, v141\n\t"
    "v_cvt_pk_bf16_f32 v139, v142, v143\n\t"
    "v_exp_f32 v144, v144\n\t"
    "s_nop 0\n\t"
    "v_mfma_f32_16x16x32_bf16 v[32:35], a[128:131], v[136:139], v[32:35]\n\t"
    "ds_read_b64_tr_b16 a[128:129], v226 offset:24576\n\t"
    "v_mfma_f32_16x16x32_bf16 v[36:39], a[132:135], v[136:139], v[36:39]\n\t"
    "ds_read_b64_tr_b16 a[130:131], v226 offset:28672\n\t"
    "ds_read_b64_tr_b16 a[132:133], v225 offset:24576\n\t"
    "ds_read_b64_tr_b16 a[134:135], v225 offset:28672\n\t"
    "v_mfma_f32_16x16x32_bf16 v[40:43], a[136:139], v[136:139], v[40:43]\n\t"
    "ds_read_b64_tr_b16 a[136:137], v226 offset:25088\n\t"
    "ds_read_b64_tr_b16 a[138:139], v226 offset:29184\n\t"
    "v_mfma_f32_16x16x32_bf16 v[44:47], a[140:143], v[136:139], v[44:47]\n\t"
    "ds_read_b64_tr_b16 a[140:141], v225 offset:25088\n\t"
    "ds_read_b64_tr_b16 a[142:143], v225 offset:29184\n\t"
    "v_mfma_f32_16x16x32_bf16 v[48:51], a[144:147], v[136:139], v[48:51]\n\t"
    "ds_read_b64_tr_b16 a[144:145], v226 offset:25600\n\t"
    "ds_read_b64_tr_b16 a[146:147], v226 offset:29696\n\t"
    "v_mfma_f32_16x16x32_bf16 v[52:55], a[148:151], v[136:139], v[52:55]\n\t"
    "ds_read_b64_tr_b16 a[148:149], v225 offset:25600\n\t"
    "ds_read_b64_tr_b16 a[150:151], v225 offset:29696\n\t"
    "v_mfma_f32_16x16x32_bf16 v[56:59], a[152:155], v[136:139], v[56:59]\n\t"
    "ds_read_b64_tr_b16 a[152:153], v226 offset:26112\n\t"
    "ds_read_b64_tr_b16 a[154:155], v226 offset:30208\n\t"
    "v_mfma_f32_16x16x32_bf16 v[60:63], a[156:159], v[136:139], v[60:63]\n\t"
    "ds_read_b64_tr_b16 a[156:157], v225 offset:26112\n\t"
    "ds_read_b64_tr_b16 a[158:159], v225 offset:30208\n\t"
    "v_mfma_f32_16x16x32_bf16 v[208:211], v[252:255], v[128:131], v[208:211]\n\t"
    "v_exp_f32 v145, v145\n\t"
    "v_mfma_f32_16x16x32_bf16 v[212:215], v[252:255], v[136:139], v[212:215]\n\t"
    "s_waitcnt lgkmcnt(15)\n\t"
    "v_exp_f32 v146, v146\n\t"
    "v_mfma_f32_16x16x32_bf16 v[128:131], a[96:99], a[0:3], v[192:195]\n\t"
    "v_exp_f32 v147, v147\n\t"
    "v_mfma_f32_16x16x32_bf16 v[128:131], a[100:103], a[4:7], v[128:131]\n\t"
    "v_exp_f32 v148, v148\n\t"
    "v_mfma_f32_16x16x32_bf16 v[128:131], a[104:107], a[8:11], v[128:131]\n\t"
    "v_exp_f32 v149, v149\n\t"
    "v_mfma_f32_16x16x32_bf16 v[128:131], a[108:111], a[12:15], v[128:131]\n\t"
    "v_exp_f32 v150, v150\n\t"
    "v_mfma_f32_16x16x32_bf16 v[132:135], a[112:115], a[0:3], v[192:195]\n\t"
    "v_exp_f32 v151, v151\n\t"
    "v_mfma_f32_16x16x32_bf16 v[132:135], a[116:119], a[4:7], v[132:135]\n\t"
    "v_cvt_pk_bf16_f32 v144, v144, v145\n\t"
    "v_cvt_pk_bf16_f32 v145, v146, v147\n\t"
    "v_mfma_f32_16x16x32_bf16 v[132:135], a[120:123], a[8:11], v[132:135]\n\t"
    "v_cvt_pk_bf16_f32 v146, v148, v149\n\t"
    "v_cvt_pk_bf16_f32 v147, v150, v151\n\t"
    "v_mfma_f32_16x16x32_bf16 v[132:135], a[124:127], a[12:15], v[132:135]\n\t"
    "v_exp_f32 v152, v152\n\t"
    "v_mfma_f32_16x16x32_bf16 v[136:139], a[96:99], a[16:19], v[196:199]\n\t"
    "ds_read_b128 a[96:99], v224 offset:24576\n\t"
    "v_mfma_f32_16x16x32_bf16 v[136:139], a[100:103], a[20:23], v[136:139]\n\t"
    "ds_read_b128 a[100:103], v224 offset:25600\n\t"
    "v_mfma_f32_16x16x32_bf16 v[136:139], a[104:107], a[24:27], v[136:139]\n\t"
    "ds_read_b128 a[104:107], v224 offset:26624\n\t"
    "v_mfma_f32_16x16x32_bf16 v[136:139], a[108:111], a[28:31], v[136:139]\n\t"
    "ds_read_b128 a[108:111], v224 offset:27648\n\t"
    "v_mfma_f32_16x16x32_bf16 v[140:143], a[112:115], a[16:19], v[196:199]\n\t"
    "ds_read_b128 a[112:115], v224 offset:28672\n\t"
    "v_mfma_f32_16x16x32_bf16 v[140:143], a[116:119], a[20:23], v[140:143]\n\t"
    "ds_read_b128 a[116:119], v224 offset:29696\n\t"
    "v_mfma_f32_16x16x32_bf16 v[140:143], a[120:123], a[24:27], v[140:143]\n\t"
    "ds_read_b128 a[120:123], v224 offset:30720\n\t"
    "v_mfma_f32_16x16x32_bf16 v[140:143], a[124:127], a[28:31], v[140:143]\n\t"
    "s_waitcnt lgkmcnt(7)\n\t"
    "ds_read_b128 a[124:127], v224 offset:31744\n\t"
    "v_mfma_f32_16x16x32_bf16 v[0:3], a[128:131], v[144:147], v[0:3]\n\t"
    "v_exp_f32 v153, v153\n\t"
    "v_mfma_f32_16x16x32_bf16 v[4:7], a[132:135], v[144:147], v[4:7]\n\t"
    "v_exp_f32 v154, v154\n\t"
    "v_mfma_f32_16x16x32_bf16 v[8:11], a[136:139], v[144:147], v[8:11]\n\t"
    "v_exp_f32 v155, v155\n\t"
    "v_mfma_f32_16x16x32_bf16 v[12:15], a[140:143], v[144:147], v[12:15]\n\t"
    "v_exp_f32 v156, v156\n\t"
    "v_mfma_f32_16x16x32_bf16 v[16:19], a[144:147], v[144:147], v[16:19]\n\t"
    "v_exp_f32 v157, v157\n\t"
    "v_mfma_f32_16x16x32_bf16 v[20:23], a[148:151], v[144:147], v[20:23]\n\t"
    "v_exp_f32 v158, v158\n\t"
    "v_mfma_f32_16x16x32_bf16 v[24:27], a[152:155], v[144:147], v[24:27]\n\t"
    "v_exp_f32 v159, v159\n\t"
    "v_mfma_f32_16x16x32_bf16 v[28:31], a[156:159], v[144:147], v[28:31]\n\t"
    "v_cvt_pk_bf16_f32 v152, v152, v153\n\t"
    "v_cvt_pk_bf16_f32 v153, v154, v155\n\t"
    "v_cvt_pk_bf16_f32 v154, v156, v157\n\t"
    "v_cvt_pk_bf16_f32 v155, v158, v159\n\t"
    "v_exp_f32 v128, v128\n\t"
    "s_nop 0\n\t"
    "v_mfma_f32_16x16x32_bf16 v[32:35], a[128:131], v[152:155], v[32:35]\n\t"
    "v_exp_f32 v129, v129\n\t"
    "v_mfma_f32_16x16x32_bf16 v[36:39], a[132:135], v[152:155], v[36:39]\n\t"
    "v_exp_f32 v130, v130\n\t"
    "v_mfma_f32_16x16x32_bf16 v[40:43], a[136:139], v[152:155], v[40:43]\n\t"
    "v_exp_f32 v131, v131\n\t"
    "v_mfma_f32_16x16x32_bf16 v[44:47], a[140:143], v[152:155], v[44:47]\n\t"
    "v_exp_f32 v132, v132\n\t"
    "v_mfma_f32_16x16x32_bf16 v[48:51], a[144:147], v[152:155], v[48:51]\n\t"
    "v_exp_f32 v133, v133\n\t"
    "v_mfma_f32_16x16x32_bf16 v[52:55], a[148:151], v[152:155], v[52:55]\n\t"
    "v_exp_f32 v134, v134\n\t"
    "v_mfma_f32_16x16x32_bf16 v[56:59], a[152:155], v[152:155], v[56:59]\n\t"
    "v_exp_f32 v135, v135\n\t"
    "v_mfma_f32_16x16x32_bf16 v[60:63], a[156:159], v[152:155], v[60:63]\n\t"
    "ds_read_b128 a[32:35], v230 offset:0\n\t"
    "ds_read_b128 a[36:39], v231 offset:0\n\t"
    "ds_read_b128 a[48:51], v230 offset:2048\n\t"
    "ds_read_b128 a[52:55], v231 offset:2048\n\t"
    "v_mfma_f32_16x16x32_bf16 v[208:211], v[252:255], v[144:147], v[208:211]\n\t"
    "v_cvt_pk_bf16_f32 v128, v128, v129\n\t"
    "v_cvt_pk_bf16_f32 v129, v130, v131\n\t"
    "v_mfma_f32_16x16x32_bf16 v[212:215], v[252:255], v[152:155], v[212:215]\n\t"
    "s_waitcnt lgkmcnt(4)\n\t"
    "v_cvt_pk_bf16_f32 v130, v132, v133\n\t"
    "v_cvt_pk_bf16_f32 v131, v134, v135\n\t"
    "v_mfma_f32_16x16x32_bf16 v[144:147], a[96:99], a[0:3], v[192:195]\n\t"
    "v_exp_f32 v136, v136\n\t"
    "v_mfma_f32_16x16x32_bf16 v[144:147], a[100:103], a[4:7], v[144:147]\n\t"
    "v_exp_f32 v137, v137\n\t"
    "v_mfma_f32_16x16x32_bf16 v[144:147], a[104:107], a[8:11], v[144:147]\n\t"
    "s_waitcnt vmcnt(0)\n\t"
    "s_barrier\n\t"
    "s_add_u32 m0, %8, 16384\n\t"
    "s_add_u32 s40, %9, 49152\n\t"
    "buffer_load_dwordx4 v227, %3, s40 offen offset:0  lds\n\t"
    "buffer_load_dwordx4 v248, %3, s40 offen offset:1024  lds\n\t"
    "s_add_u32 m0, %8, 24576\n\t"
    "s_add_u32 s40, %9, 57344\n\t"
    "buffer_load_dwordx4 v227, %3, s40 offen offset:0  lds\n\t"
    "v_mfma_f32_16x16x32_bf16 v[144:147], a[108:111], a[12:15], v[144:147]\n\t"
    "buffer_load_dwordx4 v248, %3, s40 offen offset:1024  lds\n\t"
    "v_mfma_f32_16x16x32_bf16 v[148:151], a[112:115], a[0:3], v[192:195]\n\t"
    "ds_read_b64_tr_b16 a[128:129], v226 offset:0\n\t"
    "ds_read_b64_tr_b16 a[130:131], v226 offset:4096\n\t"
    "v_mfma_f32_16x16x32_bf16 v[148:151], a[116:119], a[4:7], v[148:151]\n\t"
    "ds_read_b64_tr_b16 a[132:133], v225 offset:0\n\t"
    "ds_read_b64_tr_b16 a[134:135], v225 offset:4096\n\t"
    "v_mfma_f32_16x16x32_bf16 v[148:151], a[120:123], a[8:11], v[148:151]\n\t"
    "ds_read_b64_tr_b16 a[136:137], v226 offset:512\n\t"
    "ds_read_b64_tr_b16 a[138:139], v226 offset:4608\n\t"
    "v_mfma_f32_16x16x32_bf16 v[148:151], a[124:127], a[12:15], v[148:151]\n\t"
    "ds_read_b64_tr_b16 a[140:141], v225 offset:512\n\t"
    "ds_read_b64_tr_b16 a[142:143], v225 offset:4608\n\t"
    "v_mfma_f32_16x16x32_bf16 v[152:155], a[96:99], a[16:19], v[196:199]\n\t"
    "ds_read_b64_tr_b16 a[144:145], v226 offset:1024\n\t"
    "ds_read_b64_tr_b16 a[146:147], v226 offset:5120\n\t"
    "v_mfma_f32_16x16x32_bf16 v[152:155], a[100:103], a[20:23], v[152:155]\n\t"
    "ds_read_b64_tr_b16 a[148:149], v225 offset:1024\n\t"
    "ds_read_b64_tr_b16 a[150:151], v225 offset:5120\n\t"
    "v_mfma_f32_16x16x32_bf16 v[152:155], a[104:107], a[24:27], v[152:155]\n\t"
    "ds_read_b64_tr_b16 a[152:153], v226 offset:1536\n\t"
    "ds_read_b64_tr_b16 a[154:155], v226 offset:5632\n\t"
    "v_mfma_f32_16x16x32_bf16 v[152:155], a[108:111], a[28:31], v[152:155]\n\t"
    "ds_read_b64_tr_b16 a[156:157], v225 offset:1536\n\t"
    "ds_read_b64_tr_b16 a[158:159], v225 offset:5632\n\t"
    "v_mfma_f32_16x16x32_bf16 v[156:159], a[112:115], a[16:19], v[196:199]\n\t"
    "ds_read_b128 a[96:99], v224 offset:32768\n\t"
    "v_cvt_pk_bf16_f32 v136, v136, v137\n\t"
    "v_mfma_f32_16x16x32_bf16 v[156:159], a[116:119], a[20:23], v[156:159]\n\t"
    "ds_read_b128 a[100:103], v224 offset:33792\n\t"
    "v_mfma_f32_16x16x32_bf16 v[156:159], a[120:123], a[24:27], v[156:159]\n\t"
    "ds_read_b128 a[104:107], v224 offset:34816\n\t"
    "v_mfma_f32_16x16x32_bf16 v[156:159], a[124:127], a[28:31], v[156:159]\n\t"
    "s_waitcnt lgkmcnt(15)\n\t"
    "ds_read_b128 a[108:111], v224 offset:35840\n\t"
    "v_mfma_f32_16x16x32_bf16 v[0:3], a[128:131], v[128:131], v[0:3]\n\t"
    "s_add_u32 m0, %10, 0\n\t"
    "s_add_u32 s40, %11, 65536\n\t"
    "ds_read_b128 a[112:115], v224 offset:36864\n\t"
    "v_mfma_f32_16x16x32_bf16 v[4:7], a[132:135], v[128:131], v[4:7]\n\t"
    "s_waitcnt lgkmcnt(15)\n\t"
    "ds_read_b128 a[116:119], v224 offset:37888\n\t"
    "v_mfma_f32_16x16x32_bf16 v[8:11], a[136:139], v[128:131], v[8:11]\n\t"
    "s_waitcnt lgkmcnt(14)\n\t"
    "ds_read_b128 a[120:123], v224 offset:38912\n\t"
    "v_mfma_f32_16x16x32_bf16 v[12:15], a[140:143], v[128:131], v[12:15]\n\t"
    "s_waitcnt lgkmcnt(13)\n\t"
    "ds_read_b128 a[124:127], v224 offset:39936\n\t"
    "v_mfma_f32_16x16x32_bf16 v[16:19], a[144:147], v[128:131], v[16:19]\n\t"
    "s_waitcnt lgkmcnt(12)\n\t"
    "v_exp_f32 v138, v138\n\t"
    "v_mfma_f32_16x16x32_bf16 v[20:23], a[148:151], v[128:131], v[20:23]\n\t"
    "s_waitcnt lgkmcnt(10)\n\t"
    "v_exp_f32 v139, v139\n\t"
    "v_mfma_f32_16x16x32_bf16 v[24:27], a[152:155], v[128:131], v[24:27]\n\t"
    "s_waitcnt lgkmcnt(8)\n\t"
    "v_exp_f32 v140, v140\n\t"
    "v_mfma_f32_16x16x32_bf16 v[28:31], a[156:159], v[128:131], v[28:31]\n\t"
    "buffer_load_dwordx4 v228, %1, s40 offen offset:0 nt lds\n\t"
    "buffer_load_dwordx4 v249, %1, s40 offen offset:1024 nt lds\n\t"
    "v_exp_f32 v141, v141\n\t"
    "v_exp_f32 v142, v142\n\t"
    "v_exp_f32 v143, v143\n\t"
    "v_cvt_pk_bf16_f32 v137, v138, v139\n\t"
    "v_cvt_pk_bf16_f32 v138, v140, v141\n\t"
    "v_cvt_pk_bf16_f32 v139, v142, v143\n\t"
    "v_exp_f32 v144, v144\n\t"
    "s_nop 0\n\t"
    "v_mfma_f32_16x16x32_bf16 v[32:35], a[128:131], v[136:139], v[32:35]\n\t"
    "ds_read_b64_tr_b16 a[128:129], v226 offset:8192\n\t"
    "v_mfma_f32_16x16x32_bf16 v[36:39], a[132:135], v[136:139], v[36:39]\n\t"
    "ds_read_b64_tr_b16 a[130:131], v226 offset:12288\n\t"
    "v_mfma_f32_16x16x32_bf16 v[40:43], a[136:139], v[136:139], v[40:43]\n\t"
    "ds_read_b64_tr_b16 a[132:133], v225 offset:8192\n\t"
    "v_mfma_f32_16x16x32_bf16 v[44:47], a[140:143], v[136:139], v[44:47]\n\t"
    "ds_read_b64_tr_b16 a[134:135], v225 offset:12288\n\t"
    "v_mfma_f32_16x16x32_bf16 v[48:51], a[144:147], v[136:139], v[48:51]\n\t"
    "ds_read_b64_tr_b16 a[136:137], v226 offset:8704\n\t"
    "v_mfma_f32_16x16x32_bf16 v[52:55], a[148:151], v[136:139], v[52:55]\n\t"
    "ds_read_b64_tr_b16 a[138:139], v226 offset:12800\n\t"
    "v_mfma_f32_16x16x32_bf16 v[56:59], a[152:155], v[136:139], v[56:59]\n\t"
    "ds_read_b64_tr_b16 a[140:141], v225 offset:8704\n\t"
    "v_mfma_f32_16x16x32_bf16 v[60:63], a[156:159], v[136:139], v[60:63]\n\t"
    "ds_read_b64_tr_b16 a[142:143], v225 offset:12800\n\t"
    "v_mfma_f32_16x16x32_bf16 v[208:211], v[252:255], v[128:131], v[208:211]\n\t"
    "ds_read_b64_tr_b16 a[144:145], v226 offset:9216\n\t"
    "v_mfma_f32_16x16x32_bf16 v[212:215], v[252:255], v[136:139], v[212:215]\n\t"
    "s_waitcnt lgkmcnt(9)\n\t"
    "ds_read_b64_tr_b16 a[146:147], v226 offset:13312\n\t"
    "v_mfma_f32_16x16x32_bf16 v[128:131], a[96:99], a[0:3], v[192:195]\n\t"
    "ds_read_b64_tr_b16 a[148:149], v225 offset:9216\n\t"
    "v_mfma_f32_16x16x32_bf16 v[128:131], a[100:103], a[4:7], v[128:131]\n\t"
    "ds_read_b64_tr_b16 a[150:151], v225 offset:13312\n\t"
    "v_mfma_f32_16x16x32_bf16 v[128:131], a[104:107], a[8:11], v[128:131]\n\t"
    "ds_read_b64_tr_b16 a[152:153], v226 offset:9728\n\t"
    "v_mfma_f32_16x16x32_bf16 v[128:131], a[108:111], a[12:15], v[128:131]\n\t"
    "ds_read_b64_tr_b16 a[154:155], v226 offset:13824\n\t"
    "v_mfma_f32_16x16x32_bf16 v[132:135], a[112:115], a[0:3], v[192:195]\n\t"
    "ds_read_b64_tr_b16 a[156:157], v225 offset:9728\n\t"
    "v_mfma_f32_16x16x32_bf16 v[132:135], a[116:119], a[4:7], v[132:135]\n\t"
    "ds_read_b64_tr_b16 a[158:159], v225 offset:13824\n\t"
    "v_mfma_f32_16x16x32_bf16 v[132:135], a[120:123], a[8:11], v[132:135]\n\t"
    "v_exp_f32 v145, v145\n\t"
    "v_mfma_f32_16x16x32_bf16 v[132:135], a[124:127], a[12:15], v[132:135]\n\t"
    "v_exp_f32 v146, v146\n\t"
    "v_mfma_f32_16x16x32_bf16 v[136:139], a[96:99], a[16:19], v[196:199]\n\t"
    "v_exp_f32 v147, v147\n\t"
    "v_mfma_f32_16x16x32_bf16 v[136:139], a[100:103], a[20:23], v[136:139]\n\t"
    "v_exp_f32 v148, v148\n\t"
    "v_mfma_f32_16x16x32_bf16 v[136:139], a[104:107], a[24:27], v[136:139]\n\t"
    "v_exp_f32 v149, v149\n\t"
    "v_mfma_f32_16x16x32_bf16 v[136:139], a[108:111], a[28:31], v[136:139]\n\t"
    "v_exp_f32 v150, v150\n\t"
    "v_mfma_f32_16x16x32_bf16 v[140:143], a[112:115], a[16:19], v[196:199]\n\t"
    "v_exp_f32 v151, v151\n\t"
    "v_mfma_f32_16x16x32_bf16 v[140:143], a[116:119], a[20:23], v[140:143]\n\t"
    "v_cvt_pk_bf16_f32 v144, v144, v145\n\t"
    "v_cvt_pk_bf16_f32 v145, v146, v147\n\t"
    "ds_read_b128 a[40:43], v230 offset:4096\n\t"
    "ds_read_b128 a[44:47], v231 offset:4096\n\t"
    "ds_read_b128 a[56:59], v230 offset:6144\n\t"
    "ds_read_b128 a[60:63], v231 offset:6144\n\t"
    "v_mfma_f32_16x16x32_bf16 v[140:143], a[120:123], a[24:27], v[140:143]\n\t"
    "v_cvt_pk_bf16_f32 v146, v148, v149\n\t"
    "v_cvt_pk_bf16_f32 v147, v150, v151\n\t"
    "v_mfma_f32_16x16x32_bf16 v[140:143], a[124:127], a[28:31], v[140:143]\n\t"
    "v_exp_f32 v152, v152\n\t"
    "v_mfma_f32_16x16x32_bf16 v[160:163], a[96:99], a[32:35], 0\n\t"
    "v_exp_f32 v153, v153\n\t"
    "v_mfma_f32_16x16x32_bf16 v[160:163], a[100:103], a[36:39], v[160:163]\n\t"
    "s_waitcnt lgkmcnt(3)\n\t"
    "buffer_load_dwordx4 v250, %1, s40 offen offset:2048 nt lds\n\t"
    "buffer_load_dwordx4 v251, %1, s40 offen offset:3072 nt lds\n\t"
    "v_exp_f32 v154, v154\n\t"
    "v_exp_f32 v155, v155\n\t"
    "v_exp_f32 v156, v156\n\t"
    "v_exp_f32 v157, v157\n\t"
    "v_exp_f32 v158, v158\n\t"
    "v_mfma_f32_16x16x32_bf16 v[160:163], a[104:107], a[40:43], v[160:163]\n\t"
    "s_waitcnt lgkmcnt(2)\n\t"
    "v_exp_f32 v159, v159\n\t"
    "v_mfma_f32_16x16x32_bf16 v[160:163], a[108:111], a[44:47], v[160:163]\n\t"
    "v_cvt_pk_bf16_f32 v152, v152, v153\n\t"
    "v_cvt_pk_bf16_f32 v153, v154, v155\n\t"
    "v_mfma_f32_16x16x32_bf16 v[164:167], a[112:115], a[32:35], 0\n\t"
    "v_cvt_pk_bf16_f32 v154, v156, v157\n\t"
    "v_cvt_pk_bf16_f32 v155, v158, v159\n\t"
    "v_mfma_f32_16x16x32_bf16 v[164:167], a[116:119], a[36:39], v[164:167]\n\t"
    "v_exp_f32 v128, v128\n\t"
    "v_mfma_f32_16x16x32_bf16 v[164:167], a[120:123], a[40:43], v[164:167]\n\t"
    "v_max3_f32 v229, v160, v161, v162\n\t"
    "v_mfma_f32_16x16x32_bf16 v[164:167], a[124:127], a[44:47], v[164:167]\n\t"
    "v_exp_f32 v129, v129\n\t"
    "v_mfma_f32_16x16x32_bf16 v[168:171], a[96:99], a[48:51], 0\n\t"
    "ds_read_b128 a[96:99], v224 offset:40960\n\t"
    "v_cvt_pk_bf16_f32 v128, v128, v129\n\t"
    "v_mfma_f32_16x16x32_bf16 v[168:171], a[100:103], a[52:55], v[168:171]\n\t"
    "s_waitcnt lgkmcnt(1)\n\t"
    "ds_read_b128 a[100:103], v224 offset:41984\n\t"
    "v_mfma_f32_16x16x32_bf16 v[168:171], a[104:107], a[56:59], v[168:171]\n\t"
    "ds_read_b128 a[104:107], v224 offset:43008\n\t"
    "v_max3_f32 v232, v163, v164, v165\n\t"
    "v_mfma_f32_16x16x32_bf16 v[168:171], a[108:111], a[60:63], v[168:171]\n\t"
    "ds_read_b128 a[108:111], v224 offset:44032\n\t"
    "v_max3_f32 v229, v229, v166, v167\n\t"
    "v_mfma_f32_16x16x32_bf16 v[172:175], a[112:115], a[48:51], 0\n\t"
    "ds_read_b128 a[112:115], v224 offset:45056\n\t"
    "v_max_f32 v229, v229, v232\n\t"
    "v_mfma_f32_16x16x32_bf16 v[172:175], a[116:119], a[52:55], v[172:175]\n\t"
    "ds_read_b128 a[116:119], v224 offset:46080\n\t"
    "v_mov_b32 v232, v229\n\t"
    "v_mfma_f32_16x16x32_bf16 v[172:175], a[120:123], a[56:59], v[172:175]\n\t"
    "ds_read_b128 a[120:123], v224 offset:47104\n\t"
    "v_permlane16_swap_b32 v229, v232\n\t"
    "v_mfma_f32_16x16x32_bf16 v[172:175], a[124:127], a[60:63], v[172:175]\n\t"
    "ds_read_b128 a[124:127], v224 offset:48128\n\t"
    "v_max_f32 v229, v229, v232\n\t"
    "v_mfma_f32_16x16x32_bf16 v[0:3], a[128:131], v[144:147], v[0:3]\n\t"
    "v_mov_b32 v232, v229\n\t"
    "s_waitcnt vmcnt(4)\n\t"
    "s_barrier\n\t"
    "s_add_u32 m0, %8, 0\n\t"
    "s_add_u32 s40, %9, 65536\n\t"
    "buffer_load_dwordx4 v227, %3, s40 offen offset:0  lds\n\t"
    "buffer_load_dwordx4 v248, %3, s40 offen offset:1024  lds\n\t"
    "s_add_u32 m0, %8, 8192\n\t"
    "s_add_u32 s40, %9, 73728\n\t"
    "v_mfma_f32_16x16x32_bf16 v[4:7], a[132:135], v[144:147], v[4:7]\n\t"
    "v_permlane32_swap_b32 v229, v232\n\t"
    "v_min_f32_e64 v200, -v229, -v232\n\t"
    "v_mfma_f32_16x16x32_bf16 v[8:11], a[136:139], v[144:147], v[8:11]\n\t"
    "v_min_f32_e64 v201, -v229, -v232\n\t"
    "v_min_f32_e64 v202, -v229, -v232\n\t"
    "v_mfma_f32_16x16x32_bf16 v[12:15], a[140:143], v[144:147], v[12:15]\n\t"
    "v_min_f32_e64 v203, -v229, -v232\n\t"
    "v_max3_f32 v233, v168, v169, v170\n\t"
    "v_mfma_f32_16x16x32_bf16 v[16:19], a[144:147], v[144:147], v[16:19]\n\t"
    "v_max3_f32 v241, v171, v172, v173\n\t"
    "v_max3_f32 v233, v233, v174, v175\n\t"
    "v_mfma_f32_16x16x32_bf16 v[20:23], a[148:151], v[144:147], v[20:23]\n\t"
    "v_max_f32 v233, v233, v241\n\t"
    "v_mov_b32 v241, v233\n\t"
    "v_mfma_f32_16x16x32_bf16 v[24:27], a[152:155], v[144:147], v[24:27]\n\t"
    "buffer_load_dwordx4 v227, %3, s40 offen offset:0  lds\n\t"
    "v_mfma_f32_16x16x32_bf16 v[28:31], a[156:159], v[144:147], v[28:31]\n\t"
    "v_permlane16_swap_b32 v233, v241\n\t"
    "v_max_f32 v233, v233, v241\n\t"
    "v_mfma_f32_16x16x32_bf16 v[32:35], a[128:131], v[152:155], v[32:35]\n\t"
    "ds_read_b64_tr_b16 a[128:129], v226 offset:16384\n\t"
    "v_mov_b32 v241, v233\n\t"
    "v_mfma_f32_16x16x32_bf16 v[36:39], a[132:135], v[152:155], v[36:39]\n\t"
    "ds_read_b64_tr_b16 a[130:131], v226 offset:20480\n\t"
    "v_permlane32_swap_b32 v233, v241\n\t"
    "v_mfma_f32_16x16x32_bf16 v[40:43], a[136:139], v[152:155], v[40:43]\n\t"
    "ds_read_b64_tr_b16 a[132:133], v225 offset:16384\n\t"
    "v_min_f32_e64 v204, -v233, -v241\n\t"
    "v_mfma_f32_16x16x32_bf16 v[44:47], a[140:143], v[152:155], v[44:47]\n\t"
    "ds_read_b64_tr_b16 a[134:135], v225 offset:20480\n\t"
    "v_min_f32_e64 v205, -v233, -v241\n\t"
    "v_mfma_f32_16x16x32_bf16 v[48:51], a[144:147], v[152:155], v[48:51]\n\t"
    "buffer_load_dwordx4 v248, %3, s40 offen offset:1024  lds\n\t"
    "v_mfma_f32_16x16x32_bf16 v[52:55], a[148:151], v[152:155], v[52:55]\n\t"
    "ds_read_b64_tr_b16 a[136:137], v226 offset:16896\n\t"
    "v_min_f32_e64 v206, -v233, -v241\n\t"
    "v_mfma_f32_16x16x32_bf16 v[56:59], a[152:155], v[152:155], v[56:59]\n\t"
    "ds_read_b64_tr_b16 a[138:139], v226 offset:20992\n\t"
    "v_min_f32_e64 v207, -v233, -v241\n\t"
    "v_mfma_f32_16x16x32_bf16 v[60:63], a[156:159], v[152:155], v[60:63]\n\t"
    "ds_read_b64_tr_b16 a[140:141], v225 offset:16896\n\t"
    "v_add_f32 v160, v200, v160\n\t"
    "v_mfma_f32_16x16x32_bf16 v[208:211], v[252:255], v[144:147], v[208:211]\n\t"
    "ds_read_b64_tr_b16 a[142:143], v225 offset:20992\n\t"
    "v_add_f32 v161, v201, v161\n\t"
    "v_mfma_f32_16x16x32_bf16 v[212:215], v[252:255], v[152:155], v[212:215]\n\t"
    "s_waitcnt lgkmcnt(8)\n\t"
    "ds_read_b64_tr_b16 a[144:145], v226 offset:17408\n\t"
    "v_add_f32 v162, v202, v162\n\t"
    "v_mfma_f32_16x16x32_bf16 v[144:147], a[96:99], a[0:3], v[192:195]\n\t"
    "ds_read_b64_tr_b16 a[146:147], v226 offset:21504\n\t"
    "v_add_f32 v163, v203, v163\n\t"
    "v_mfma_f32_16x16x32_bf16 v[144:147], a[100:103], a[4:7], v[144:147]\n\t"
    "ds_read_b64_tr_b16 a[148:149], v225 offset:17408\n\t"
    "v_add_f32 v164, v200, v164\n\t"
    "v_mfma_f32_16x16x32_bf16 v[144:147], a[104:107], a[8:11], v[144:147]\n\t"
    "ds_read_b64_tr_b16 a[150:151], v225 offset:21504\n\t"
    "v_add_f32 v165, v201, v165\n\t"
    "v_mfma_f32_16x16x32_bf16 v[144:147], a[108:111], a[12:15], v[144:147]\n\t"
    "ds_read_b64_tr_b16 a[152:153], v226 offset:17920\n\t"
    "v_add_f32 v166, v202, v166\n\t"
    "v_mfma_f32_16x16x32_bf16 v[148:151], a[112:115], a[0:3], v[192:195]\n\t"
    "ds_read_b64_tr_b16 a[154:155], v226 offset:22016\n\t"
    "v_add_f32 v167, v203, v167\n\t"
    "v_mfma_f32_16x16x32_bf16 v[148:151], a[116:119], a[4:7], v[148:151]\n\t"
    "ds_read_b64_tr_b16 a[156:157], v225 offset:17920\n\t"
    "v_add_f32 v168, v204, v168\n\t"
    "v_mfma_f32_16x16x32_bf16 v[148:151], a[120:123], a[8:11], v[148:151]\n\t"
    "ds_read_b64_tr_b16 a[158:159], v225 offset:22016\n\t"
    "v_add_f32 v169, v205, v169\n\t"
    "v_mfma_f32_16x16x32_bf16 v[148:151], a[124:127], a[12:15], v[148:151]\n\t"
    "v_exp_f32 v130, v130\n\t"
    "v_mfma_f32_16x16x32_bf16 v[152:155], a[96:99], a[16:19], v[196:199]\n\t"
    "v_exp_f32 v131, v131\n\t"
    "v_mfma_f32_16x16x32_bf16 v[152:155], a[100:103], a[20:23], v[152:155]\n\t"
    "v_exp_f32 v132, v132\n\t"
    "v_mfma_f32_16x16x32_bf16 v[152:155], a[104:107], a[24:27], v[152:155]\n\t"
    "s_add_u32 m0, %10, 4096\n\t"
    "s_add_u32 s40, %11, 65664\n\t"
    "v_cvt_pk_bf16_f32 v129, v130, v131\n\t"
    "v_mfma_f32_16x16x32_bf16 v[152:155], a[108:111], a[28:31], v[152:155]\n\t"
    "v_exp_f32 v133, v133\n\t"
    "v_mfma_f32_16x16x32_bf16 v[156:159], a[112:115], a[16:19], v[196:199]\n\t"
    "v_exp_f32 v134, v134\n\t"
    "v_mfma_f32_16x16x32_bf16 v[156:159], a[116:119], a[20:23], v[156:159]\n\t"
    "v_exp_f32 v135, v135\n\t"
    "v_mfma_f32_16x16x32_bf16 v[156:159], a[120:123], a[24:27], v[156:159]\n\t"
    "v_cvt_pk_bf16_f32 v130, v132, v133\n\t"
    "v_cvt_pk_bf16_f32 v131, v134, v135\n\t"
    "v_mfma_f32_16x16x32_bf16 v[156:159], a[124:127], a[28:31], v[156:159]\n\t"
    "v_exp_f32 v136, v136\n\t"
    "v_mfma_f32_16x16x32_bf16 v[176:179], a[96:99], a[32:35], v[200:203]\n\t"
    "v_exp_f32 v137, v137\n\t"
    "v_mfma_f32_16x16x32_bf16 v[176:179], a[100:103], a[36:39], v[176:179]\n\t"
    "v_exp_f32 v138, v138\n\t"
    "v_mfma_f32_16x16x32_bf16 v[176:179], a[104:107], a[40:43], v[176:179]\n\t"
    "v_exp_f32 v139, v139\n\t"
    "v_mfma_f32_16x16x32_bf16 v[176:179], a[108:111], a[44:47], v[176:179]\n\t"
    "v_exp_f32 v140, v140\n\t"
    "v_mfma_f32_16x16x32_bf16 v[180:183], a[112:115], a[32:35], v[200:203]\n\t"
    "v_exp_f32 v141, v141\n\t"
    "v_mfma_f32_16x16x32_bf16 v[180:183], a[116:119], a[36:39], v[180:183]\n\t"
    "v_exp_f32 v142, v142\n\t"
    "v_mfma_f32_16x16x32_bf16 v[180:183], a[120:123], a[40:43], v[180:183]\n\t"
    "v_exp_f32 v143, v143\n\t"
    "v_mfma_f32_16x16x32_bf16 v[180:183], a[124:127], a[44:47], v[180:183]\n\t"
    "v_cvt_pk_bf16_f32 v136, v136, v137\n\t"
    "v_cvt_pk_bf16_f32 v137, v138, v139\n\t"
    "v_mfma_f32_16x16x32_bf16 v[184:187], a[96:99], a[48:51], v[204:207]\n\t"
    "ds_read_b128 a[96:99], v224 offset:49152\n\t"
    "v_cvt_pk_bf16_f32 v138, v140, v141\n\t"
    "v_mfma_f32_16x16x32_bf16 v[184:187], a[100:103], a[52:55], v[184:187]\n\t"
    "ds_read_b128 a[100:103], v224 offset:50176\n\t"
    "v_cvt_pk_bf16_f32 v139, v142, v143\n\t"
    "v_mfma_f32_16x16x32_bf16 v[184:187], a[104:107], a[56:59], v[184:187]\n\t"
    "ds_read_b128 a[104:107], v224 offset:51200\n\t"
    "v_add_f32 v170, v206, v170\n\t"
    "v_mfma_f32_16x16x32_bf16 v[184:187], a[108:111], a[60:63], v[184:187]\n\t"
    "ds_read_b128 a[108:111], v224 offset:52224\n\t"
    "v_add_f32 v171, v207, v171\n\t"
    "v_mfma_f32_16x16x32_bf16 v[188:191], a[112:115], a[48:51], v[204:207]\n\t"
    "ds_read_b128 a[112:115], v224 offset:53248\n\t"
    "v_add_f32 v172, v204, v172\n\t"
    "v_mfma_f32_16x16x32_bf16 v[188:191], a[116:119], a[52:55], v[188:191]\n\t"
    "ds_read_b128 a[116:119], v224 offset:54272\n\t"
    "v_add_f32 v173, v205, v173\n\t"
    "v_mfma_f32_16x16x32_bf16 v[188:191], a[120:123], a[56:59], v[188:191]\n\t"
    "ds_read_b128 a[120:123], v224 offset:55296\n\t"
    "v_add_f32 v174, v206, v174\n\t"
    "v_mfma_f32_16x16x32_bf16 v[188:191], a[124:127], a[60:63], v[188:191]\n\t"
    "s_waitcnt lgkmcnt(7)\n\t"
    "ds_read_b128 a[124:127], v224 offset:56320\n\t"
    "v_add_f32 v175, v207, v175\n\t"
    "v_mfma_f32_16x16x32_bf16 v[0:3], a[128:131], v[128:131], v[0:3]\n\t"
    "v_exp_f32 v160, v160\n\t"
    "v_mfma_f32_16x16x32_bf16 v[4:7], a[132:135], v[128:131], v[4:7]\n\t"
    "v_exp_f32 v161, v161\n\t"
    "v_mfma_f32_16x16x32_bf16 v[8:11], a[136:139], v[128:131], v[8:11]\n\t"
    "v_exp_f32 v162, v162\n\t"
    "v_mfma_f32_16x16x32_bf16 v[12:15], a[140:143], v[128:131], v[12:15]\n\t"
    "buffer_load_dwordx4 v228, %1, s40 offen offset:0 nt lds\n\t"
    "v_mfma_f32_16x16x32_bf16 v[16:19], a[144:147], v[128:131], v[16:19]\n\t"
    "v_exp_f32 v163, v163\n\t"
    "v_mfma_f32_16x16x32_bf16 v[20:23], a[148:151], v[128:131], v[20:23]\n\t"
    "v_exp_f32 v164, v164\n\t"
    "v_mfma_f32_16x16x32_bf16 v[24:27], a[152:155], v[128:131], v[24:27]\n\t"
    "v_exp_f32 v165, v165\n\t"
    "v_mfma_f32_16x16x32_bf16 v[28:31], a[156:159], v[128:131], v[28:31]\n\t"
    "v_exp_f32 v166, v166\n\t"
    "v_mfma_f32_16x16x32_bf16 v[32:35], a[128:131], v[136:139], v[32:35]\n\t"
    "v_exp_f32 v167, v167\n\t"
    "v_mfma_f32_16x16x32_bf16 v[36:39], a[132:135], v[136:139], v[36:39]\n\t"
    "v_cvt_pk_bf16_f32 v160, v160, v161\n\t"
    "v_cvt_pk_bf16_f32 v161, v162, v163\n\t"
    "v_mfma_f32_16x16x32_bf16 v[40:43], a[136:139], v[136:139], v[40:43]\n\t"
    "v_cvt_pk_bf16_f32 v162, v164, v165\n\t"
    "v_cvt_pk_bf16_f32 v163, v166, v167\n\t"
    "v_mfma_f32_16x16x32_bf16 v[44:47], a[140:143], v[136:139], v[44:47]\n\t"
    "buffer_load_dwordx4 v249, %1, s40 offen offset:1024 nt lds\n\t"
    "v_mfma_f32_16x16x32_bf16 v[48:51], a[144:147], v[136:139], v[48:51]\n\t"
    "v_exp_f32 v168, v168\n\t"
    "v_mfma_f32_16x16x32_bf16 v[52:55], a[148:151], v[136:139], v[52:55]\n\t"
    "v_exp_f32 v169, v169\n\t"
    "v_mfma_f32_16x16x32_bf16 v[56:59], a[152:155], v[136:139], v[56:59]\n\t"
    "v_exp_f32 v170, v170\n\t"
    "v_mfma_f32_16x16x32_bf16 v[60:63], a[156:159], v[136:139], v[60:63]\n\t"
    "v_exp_f32 v171, v171\n\t"
    "v_mfma_f32_16x16x32_bf16 v[208:211], v[252:255], v[128:131], v[208:211]\n\t"
    "v_exp_f32 v172, v172\n\t"
    "v_mfma_f32_16x16x32_bf16 v[212:215], v[252:255], v[136:139], v[212:215]\n\t"
    "s_waitcnt lgkmcnt(0)\n\t"
    "v_exp_f32 v173, v173\n\t"
    "v_mfma_f32_16x16x32_bf16 v[128:131], a[96:99], a[0:3], v[192:195]\n\t"
    "v_exp_f32 v174, v174\n\t"
    "v_mfma_f32_16x16x32_bf16 v[128:131], a[100:103], a[4:7], v[128:131]\n\t"
    "buffer_load_dwordx4 v250, %1, s40 offen offset:2048 nt lds\n\t"
    "v_mfma_f32_16x16x32_bf16 v[128:131], a[104:107], a[8:11], v[128:131]\n\t"
    "v_exp_f32 v175, v175\n\t"
    "v_mfma_f32_16x16x32_bf16 v[128:131], a[108:111], a[12:15], v[128:131]\n\t"
    "v_cvt_pk_bf16_f32 v168, v168, v169\n\t"
    "v_cvt_pk_bf16_f32 v169, v170, v171\n\t"
    "v_mfma_f32_16x16x32_bf16 v[132:135], a[112:115], a[0:3], v[192:195]\n\t"
    "v_cvt_pk_bf16_f32 v170, v172, v173\n\t"
    "v_cvt_pk_bf16_f32 v171, v174, v175\n\t"
    "v_mfma_f32_16x16x32_bf16 v[132:135], a[116:119], a[4:7], v[132:135]\n\t"
    "v_exp_f32 v144, v144\n\t"
    "v_mfma_f32_16x16x32_bf16 v[132:135], a[120:123], a[8:11], v[132:135]\n\t"
    "v_exp_f32 v145, v145\n\t"
    "v_mfma_f32_16x16x32_bf16 v[132:135], a[124:127], a[12:15], v[132:135]\n\t"
    "v_exp_f32 v146, v146\n\t"
    "v_mfma_f32_16x16x32_bf16 v[136:139], a[96:99], a[16:19], v[196:199]\n\t"
    "v_exp_f32 v147, v147\n\t"
    "v_mfma_f32_16x16x32_bf16 v[136:139], a[100:103], a[20:23], v[136:139]\n\t"
    "buffer_load_dwordx4 v251, %1, s40 offen offset:3072 nt lds\n\t"
    "v_mfma_f32_16x16x32_bf16 v[136:139], a[104:107], a[24:27], v[136:139]\n\t"
    "v_exp_f32 v148, v148\n\t"
    "v_mfma_f32_16x16x32_bf16 v[136:139], a[108:111], a[28:31], v[136:139]\n\t"
    "v_exp_f32 v149, v149\n\t"
    "v_mfma_f32_16x16x32_bf16 v[140:143], a[112:115], a[16:19], v[196:199]\n\t"
    "v_exp_f32 v150, v150\n\t"
    "v_mfma_f32_16x16x32_bf16 v[140:143], a[116:119], a[20:23], v[140:143]\n\t"
    "v_exp_f32 v151, v151\n\t"
    "v_mfma_f32_16x16x32_bf16 v[140:143], a[120:123], a[24:27], v[140:143]\n\t"
    "v_cvt_pk_bf16_f32 v144, v144, v145\n\t"
    "v_cvt_pk_bf16_f32 v145, v146, v147\n\t"
    "v_mfma_f32_16x16x32_bf16 v[140:143], a[124:127], a[28:31], v[140:143]\n\t"
    "v_cvt_pk_bf16_f32 v146, v148, v149\n\t"
    "v_cvt_pk_bf16_f32 v147, v150, v151\n\t"
    "v_mfma_f32_16x16x32_bf16 v[64:67], a[128:131], v[160:163], 0\n\t"
    "v_exp_f32 v152, v152\n\t"
    "v_mfma_f32_16x16x32_bf16 v[68:71], a[132:135], v[160:163], 0\n\t"
    "v_exp_f32 v153, v153\n\t"
    "v_mfma_f32_16x16x32_bf16 v[72:75], a[136:139], v[160:163], 0\n\t"
    "v_exp_f32 v154, v154\n\t"
    "v_mfma_f32_16x16x32_bf16 v[76:79], a[140:143], v[160:163], 0\n\t"
    "v_exp_f32 v155, v155\n\t"
    "v_mfma_f32_16x16x32_bf16 v[80:83], a[144:147], v[160:163], 0\n\t"
    "v_exp_f32 v156, v156\n\t"
    "v_mfma_f32_16x16x32_bf16 v[84:87], a[148:151], v[160:163], 0\n\t"
    "v_exp_f32 v157, v157\n\t"
    "v_mfma_f32_16x16x32_bf16 v[88:91], a[152:155], v[160:163], 0\n\t"
    "v_exp_f32 v158, v158\n\t"
    "v_mfma_f32_16x16x32_bf16 v[92:95], a[156:159], v[160:163], 0\n\t"
    "v_exp_f32 v159, v159\n\t"
    "v_mfma_f32_16x16x32_bf16 v[96:99], a[128:131], v[168:171], 0\n\t"
    "ds_read_b64_tr_b16 a[128:129], v226 offset:24576\n\t"
    "v_cvt_pk_bf16_f32 v152, v152, v153\n\t"
    "v_mfma_f32_16x16x32_bf16 v[100:103], a[132:135], v[168:171], 0\n\t"
    "ds_read_b64_tr_b16 a[130:131], v226 offset:28672\n\t"
    "ds_read_b64_tr_b16 a[132:133], v225 offset:24576\n\t"
    "ds_read_b64_tr_b16 a[134:135], v225 offset:28672\n\t"
    "v_mfma_f32_16x16x32_bf16 v[104:107], a[136:139], v[168:171], 0\n\t"
    "ds_read_b64_tr_b16 a[136:137], v226 offset:25088\n\t"
    "ds_read_b64_tr_b16 a[138:139], v226 offset:29184\n\t"
    "v_mfma_f32_16x16x32_bf16 v[108:111], a[140:143], v[168:171], 0\n\t"
    "ds_read_b64_tr_b16 a[140:141], v225 offset:25088\n\t"
    "ds_read_b64_tr_b16 a[142:143], v225 offset:29184\n\t"
    "v_mfma_f32_16x16x32_bf16 v[112:115], a[144:147], v[168:171], 0\n\t"
    "ds_read_b64_tr_b16 a[144:145], v226 offset:25600\n\t"
    "ds_read_b64_tr_b16 a[146:147], v226 offset:29696\n\t"
    "v_mfma_f32_16x16x32_bf16 v[116:119], a[148:151], v[168:171], 0\n\t"
    "ds_read_b64_tr_b16 a[148:149], v225 offset:25600\n\t"
    "ds_read_b64_tr_b16 a[150:151], v225 offset:29696\n\t"
    "v_mfma_f32_16x16x32_bf16 v[120:123], a[152:155], v[168:171], 0\n\t"
    "ds_read_b64_tr_b16 a[152:153], v226 offset:26112\n\t"
    "ds_read_b64_tr_b16 a[154:155], v226 offset:30208\n\t"
    "v_mfma_f32_16x16x32_bf16 v[124:127], a[156:159], v[168:171], 0\n\t"
    "ds_read_b64_tr_b16 a[156:157], v225 offset:26112\n\t"
    "ds_read_b64_tr_b16 a[158:159], v225 offset:30208\n\t"
    "v_mfma_f32_16x16x32_bf16 v[216:219], v[252:255], v[160:163], 0\n\t"
    "v_cvt_pk_bf16_f32 v153, v154, v155\n\t"
    "v_cvt_pk_bf16_f32 v154, v156, v157\n\t"
    "v_mfma_f32_16x16x32_bf16 v[220:223], v[252:255], v[168:171], 0\n\t"
    "v_cvt_pk_bf16_f32 v155, v158, v159\n\t"
    "v_mfma_f32_16x16x32_bf16 v[160:163], a[96:99], a[32:35], v[200:203]\n\t"
    "v_exp_f32 v176, v176\n\t"
    "v_mfma_f32_16x16x32_bf16 v[160:163], a[100:103], a[36:39], v[160:163]\n\t"
    "v_exp_f32 v177, v177\n\t"
    "v_mfma_f32_16x16x32_bf16 v[160:163], a[104:107], a[40:43], v[160:163]\n\t"
    "v_exp_f32 v178, v178\n\t"
    "v_mfma_f32_16x16x32_bf16 v[160:163], a[108:111], a[44:47], v[160:163]\n\t"
    "v_exp_f32 v179, v179\n\t"
    "v_mfma_f32_16x16x32_bf16 v[164:167], a[112:115], a[32:35], v[200:203]\n\t"
    "v_exp_f32 v180, v180\n\t"
    "v_mfma_f32_16x16x32_bf16 v[164:167], a[116:119], a[36:39], v[164:167]\n\t"
    "v_exp_f32 v181, v181\n\t"
    "v_mfma_f32_16x16x32_bf16 v[164:167], a[120:123], a[40:43], v[164:167]\n\t"
    "s_waitcnt lgkmcnt(0)\n\t"
    "v_exp_f32 v182, v182\n\t"
    "s_waitcnt vmcnt(4)\n\t"
    "s_barrier\n\t"
    "s_add_u32 m0, %8, 16384\n\t"
    "s_add_u32 s40, %9, 81920\n\t"
    "v_mfma_f32_16x16x32_bf16 v[164:167], a[124:127], a[44:47], v[164:167]\n\t"
    "v_exp_f32 v183, v183\n\t"
    "v_mfma_f32_16x16x32_bf16 v[168:171], a[96:99], a[48:51], v[204:207]\n\t"
    "ds_read_b128 a[96:99], v224 offset:57344\n\t"
    "v_cvt_pk_bf16_f32 v176, v176, v177\n\t"
    "v_mfma_f32_16x16x32_bf16 v[168:171], a[100:103], a[52:55], v[168:171]\n\t"
    "ds_read_b128 a[100:103], v224 offset:58368\n\t"
    "v_cvt_pk_bf16_f32 v177, v178, v179\n\t"
    "v_mfma_f32_16x16x32_bf16 v[168:171], a[104:107], a[56:59], v[168:171]\n\t"
    "buffer_load_dwordx4 v227, %3, s40 offen offset:0  lds\n\t"
    "v_mfma_f32_16x16x32_bf16 v[168:171], a[108:111], a[60:63], v[168:171]\n\t"
    "ds_read_b128 a[104:107], v224 offset:59392\n\t"
    "v_cvt_pk_bf16_f32 v178, v180, v181\n\t"
    "v_mfma_f32_16x16x32_bf16 v[172:175], a[112:115], a[48:51], v[204:207]\n\t"
    "ds_read_b128 a[108:111], v224 offset:60416\n\t"
    "v_cvt_pk_bf16_f32 v179, v182, v183\n\t"
    "v_mfma_f32_16x16x32_bf16 v[172:175], a[116:119], a[52:55], v[172:175]\n\t"
    "ds_read_b128 a[112:115], v224 offset:61440\n\t"
    "v_mfma_f32_16x16x32_bf16 v[172:175], a[120:123], a[56:59], v[172:175]\n\t"
    "ds_read_b128 a[116:119], v224 offset:62464\n\t"
    "v_mfma_f32_16x16x32_bf16 v[172:175], a[124:127], a[60:63], v[172:175]\n\t"
    "ds_read_b128 a[120:123], v224 offset:63488\n\t"
    "v_mfma_f32_16x16x32_bf16 v[0:3], a[128:131], v[144:147], v[0:3]\n\t"
    "buffer_load_dwordx4 v248, %3, s40 offen offset:1024  lds\n\t"
    "s_add_u32 m0, %8, 24576\n\t"
    "s_add_u32 s40, %9, 90112\n\t"
    "v_mfma_f32_16x16x32_bf16 v[4:7], a[132:135], v[144:147], v[4:7]\n\t"
    "ds_read_b128 a[124:127], v224 offset:64512\n\t"
    "v_mfma_f32_16x16x32_bf16 v[8:11], a[136:139], v[144:147], v[8:11]\n\t"
    "v_exp_f32 v184, v184\n\t"
    "v_mfma_f32_16x16x32_bf16 v[12:15], a[140:143], v[144:147], v[12:15]\n\t"
    "v_exp_f32 v185, v185\n\t"
    "v_mfma_f32_16x16x32_bf16 v[16:19], a[144:147], v[144:147], v[16:19]\n\t"
    "v_exp_f32 v186, v186\n\t"
    "v_mfma_f32_16x16x32_bf16 v[20:23], a[148:151], v[144:147], v[20:23]\n\t"
    "v_exp_f32 v187, v187\n\t"
    "v_mfma_f32_16x16x32_bf16 v[24:27], a[152:155], v[144:147], v[24:27]\n\t"
    "buffer_load_dwordx4 v227, %3, s40 offen offset:0  lds\n\t"
    "v_mfma_f32_16x16x32_bf16 v[28:31], a[156:159], v[144:147], v[28:31]\n\t"
    "v_exp_f32 v188, v188\n\t"
    "v_mfma_f32_16x16x32_bf16 v[32:35], a[128:131], v[152:155], v[32:35]\n\t"
    "v_exp_f32 v189, v189\n\t"
    "v_mfma_f32_16x16x32_bf16 v[36:39], a[132:135], v[152:155], v[36:39]\n\t"
    "v_exp_f32 v190, v190\n\t"
    "v_mfma_f32_16x16x32_bf16 v[40:43], a[136:139], v[152:155], v[40:43]\n\t"
    "v_exp_f32 v191, v191\n\t"
    "v_mfma_f32_16x16x32_bf16 v[44:47], a[140:143], v[152:155], v[44:47]\n\t"
    "v_cvt_pk_bf16_f32 v184, v184, v185\n\t"
    "v_cvt_pk_bf16_f32 v185, v186, v187\n\t"
    "v_mfma_f32_16x16x32_bf16 v[48:51], a[144:147], v[152:155], v[48:51]\n\t"
    "buffer_load_dwordx4 v248, %3, s40 offen offset:1024  lds\n\t"
    "v_mfma_f32_16x16x32_bf16 v[52:55], a[148:151], v[152:155], v[52:55]\n\t"
    "v_cvt_pk_bf16_f32 v186, v188, v189\n\t"
    "v_cvt_pk_bf16_f32 v187, v190, v191\n\t"
    "v_mfma_f32_16x16x32_bf16 v[56:59], a[152:155], v[152:155], v[56:59]\n\t"
    "v_exp_f32 v128, v128\n\t"
    "v_mfma_f32_16x16x32_bf16 v[60:63], a[156:159], v[152:155], v[60:63]\n\t"
    "v_exp_f32 v129, v129\n\t"
    "v_mfma_f32_16x16x32_bf16 v[208:211], v[252:255], v[144:147], v[208:211]\n\t"
    "v_exp_f32 v130, v130\n\t"
    "v_mfma_f32_16x16x32_bf16 v[212:215], v[252:255], v[152:155], v[212:215]\n\t"
    "v_exp_f32 v131, v131\n\t"
    "v_mfma_f32_16x16x32_bf16 v[64:67], a[128:131], v[176:179], v[64:67]\n\t"
    "v_exp_f32 v132, v132\n\t"
    "v_mfma_f32_16x16x32_bf16 v[68:71], a[132:135], v[176:179], v[68:71]\n\t"
    "v_exp_f32 v133, v133\n\t"
    "v_mfma_f32_16x16x32_bf16 v[72:75], a[136:139], v[176:179], v[72:75]\n\t"
    "v_exp_f32 v134, v134\n\t"
    "v_mfma_f32_16x16x32_bf16 v[76:79], a[140:143], v[176:179], v[76:79]\n\t"
    "v_exp_f32 v135, v135\n\t"
    "v_mfma_f32_16x16x32_bf16 v[80:83], a[144:147], v[176:179], v[80:83]\n\t"
    "v_cvt_pk_bf16_f32 v128, v128, v129\n\t"
    "v_cvt_pk_bf16_f32 v129, v130, v131\n\t"
    "v_mfma_f32_16x16x32_bf16 v[84:87], a[148:151], v[176:179], v[84:87]\n\t"
    "v_cvt_pk_bf16_f32 v130, v132, v133\n\t"
    "v_cvt_pk_bf16_f32 v131, v134, v135\n\t"
    "v_mfma_f32_16x16x32_bf16 v[88:91], a[152:155], v[176:179], v[88:91]\n\t"
    "ds_read_b128 a[64:67], v230 offset:0\n\t"
    "v_mfma_f32_16x16x32_bf16 v[92:95], a[156:159], v[176:179], v[92:95]\n\t"
    "ds_read_b128 a[68:71], v231 offset:0\n\t"
    "v_mfma_f32_16x16x32_bf16 v[96:99], a[128:131], v[184:187], v[96:99]\n\t"
    "ds_read_b128 a[80:83], v230 offset:2048\n\t"
    "v_mfma_f32_16x16x32_bf16 v[100:103], a[132:135], v[184:187], v[100:103]\n\t"
    "ds_read_b128 a[84:87], v231 offset:2048\n\t"
    "v_mfma_f32_16x16x32_bf16 v[104:107], a[136:139], v[184:187], v[104:107]\n\t"
    "ds_read_b64_tr_b16 a[128:129], v226 offset:0\n\t"
    "v_mfma_f32_16x16x32_bf16 v[108:111], a[140:143], v[184:187], v[108:111]\n\t"
    "ds_read_b64_tr_b16 a[130:131], v226 offset:4096\n\t"
    "v_mfma_f32_16x16x32_bf16 v[112:115], a[144:147], v[184:187], v[112:115]\n\t"
    "ds_read_b64_tr_b16 a[132:133], v225 offset:0\n\t"
    "v_mfma_f32_16x16x32_bf16 v[116:119], a[148:151], v[184:187], v[116:119]\n\t"
    "ds_read_b64_tr_b16 a[134:135], v225 offset:4096\n\t"
    "v_mfma_f32_16x16x32_bf16 v[120:123], a[152:155], v[184:187], v[120:123]\n\t"
    "ds_read_b64_tr_b16 a[136:137], v226 offset:512\n\t"
    "v_mfma_f32_16x16x32_bf16 v[124:127], a[156:159], v[184:187], v[124:127]\n\t"
    "ds_read_b64_tr_b16 a[138:139], v226 offset:4608\n\t"
    "v_mfma_f32_16x16x32_bf16 v[216:219], v[252:255], v[176:179], v[216:219]\n\t"
    "ds_read_b64_tr_b16 a[140:141], v225 offset:512\n\t"
    "v_mfma_f32_16x16x32_bf16 v[220:223], v[252:255], v[184:187], v[220:223]\n\t"
    "s_waitcnt lgkmcnt(11)\n\t"
    "ds_read_b64_tr_b16 a[142:143], v225 offset:4608\n\t"
    "v_mfma_f32_16x16x32_bf16 v[144:147], a[96:99], a[0:3], v[192:195]\n\t"
    "ds_read_b64_tr_b16 a[144:145], v226 offset:1024\n\t"
    "v_mfma_f32_16x16x32_bf16 v[144:147], a[100:103], a[4:7], v[144:147]\n\t"
    "ds_read_b64_tr_b16 a[146:147], v226 offset:5120\n\t"
    "v_mfma_f32_16x16x32_bf16 v[144:147], a[104:107], a[8:11], v[144:147]\n\t"
    "ds_read_b64_tr_b16 a[148:149], v225 offset:1024\n\t"
    "v_mfma_f32_16x16x32_bf16 v[144:147], a[108:111], a[12:15], v[144:147]\n\t"
    "ds_read_b64_tr_b16 a[150:151], v225 offset:5120\n\t"
    "v_mfma_f32_16x16x32_bf16 v[148:151], a[112:115], a[0:3], v[192:195]\n\t"
    "ds_read_b64_tr_b16 a[152:153], v226 offset:1536\n\t"
    "v_mfma_f32_16x16x32_bf16 v[148:151], a[116:119], a[4:7], v[148:151]\n\t"
    "ds_read_b64_tr_b16 a[154:155], v226 offset:5632\n\t"
    "v_mfma_f32_16x16x32_bf16 v[148:151], a[120:123], a[8:11], v[148:151]\n\t"
    "ds_read_b64_tr_b16 a[156:157], v225 offset:1536\n\t"
    "v_mfma_f32_16x16x32_bf16 v[148:151], a[124:127], a[12:15], v[148:151]\n\t"
    "ds_read_b64_tr_b16 a[158:159], v225 offset:5632\n\t"
    "v_mfma_f32_16x16x32_bf16 v[152:155], a[96:99], a[16:19], v[196:199]\n\t"
    "v_exp_f32 v136, v136\n\t"
    "v_mfma_f32_16x16x32_bf16 v[152:155], a[100:103], a[20:23], v[152:155]\n\t"
    "v_exp_f32 v137, v137\n\t"
    "v_mfma_f32_16x16x32_bf16 v[152:155], a[104:107], a[24:27], v[152:155]\n\t"
    "v_exp_f32 v138, v138\n\t"
    "v_mfma_f32_16x16x32_bf16 v[152:155], a[108:111], a[28:31], v[152:155]\n\t"
    "v_exp_f32 v139, v139\n\t"
    "v_mfma_f32_16x16x32_bf16 v[156:159], a[112:115], a[16:19], v[196:199]\n\t"
    "v_exp_f32 v140, v140\n\t"
    "v_mfma_f32_16x16x32_bf16 v[156:159], a[116:119], a[20:23], v[156:159]\n\t"
    "v_exp_f32 v141, v141\n\t"
    "v_mfma_f32_16x16x32_bf16 v[156:159], a[120:123], a[24:27], v[156:159]\n\t"
    "v_exp_f32 v142, v142\n\t"
    "v_mfma_f32_16x16x32_bf16 v[156:159], a[124:127], a[28:31], v[156:159]\n\t"
    "v_exp_f32 v143, v143\n\t"
    "v_mfma_f32_16x16x32_bf16 v[176:179], a[96:99], a[32:35], v[200:203]\n\t"
    "v_cvt_pk_bf16_f32 v136, v136, v137\n\t"
    "v_cvt_pk_bf16_f32 v137, v138, v139\n\t"
    "v_mfma_f32_16x16x32_bf16 v[176:179], a[100:103], a[36:39], v[176:179]\n\t"
    "v_cvt_pk_bf16_f32 v138, v140, v141\n\t"
    "v_cvt_pk_bf16_f32 v139, v142, v143\n\t"
    "v_mfma_f32_16x16x32_bf16 v[176:179], a[104:107], a[40:43], v[176:179]\n\t"
    "v_exp_f32 v160, v160\n\t"
    "v_mfma_f32_16x16x32_bf16 v[176:179], a[108:111], a[44:47], v[176:179]\n\t"
    "v_exp_f32 v161, v161\n\t"
    "v_mfma_f32_16x16x32_bf16 v[180:183], a[112:115], a[32:35], v[200:203]\n\t"
    "v_exp_f32 v162, v162\n\t"
    "v_mfma_f32_16x16x32_bf16 v[180:183], a[116:119], a[36:39], v[180:183]\n\t"
    "v_exp_f32 v163, v163\n\t"
    "v_mfma_f32_16x16x32_bf16 v[180:183], a[120:123], a[40:43], v[180:183]\n\t"
    "v_exp_f32 v164, v164\n\t"
    "v_mfma_f32_16x16x32_bf16 v[180:183], a[124:127], a[44:47], v[180:183]\n\t"
    "v_exp_f32 v165, v165\n\t"
    "v_mfma_f32_16x16x32_bf16 v[184:187], a[96:99], a[48:51], v[204:207]\n\t"
    "ds_read_b128 a[96:99], v244 offset:0\n\t"
    "v_cvt_pk_bf16_f32 v160, v160, v161\n\t"
    "v_mfma_f32_16x16x32_bf16 v[184:187], a[100:103], a[52:55], v[184:187]\n\t"
    "ds_read_b128 a[100:103], v244 offset:1024\n\t"
    "v_cvt_pk_bf16_f32 v161, v162, v163\n\t"
    "v_mfma_f32_16x16x32_bf16 v[184:187], a[104:107], a[56:59], v[184:187]\n\t"
    "ds_read_b128 a[104:107], v244 offset:2048\n\t"
    "v_cvt_pk_bf16_f32 v162, v164, v165\n\t"
    "v_mfma_f32_16x16x32_bf16 v[184:187], a[108:111], a[60:63], v[184:187]\n\t"
    "ds_read_b128 a[108:111], v244 offset:3072\n\t"
    "v_mfma_f32_16x16x32_bf16 v[188:191], a[112:115], a[48:51], v[204:207]\n\t"
    "ds_read_b128 a[112:115], v244 offset:4096\n\t"
    "v_mfma_f32_16x16x32_bf16 v[188:191], a[116:119], a[52:55], v[188:191]\n\t"
    "ds_read_b128 a[116:119], v244 offset:5120\n\t"
    "v_mfma_f32_16x16x32_bf16 v[188:191], a[120:123], a[56:59], v[188:191]\n\t"
    "ds_read_b128 a[120:123], v244 offset:6144\n\t"
    "v_mfma_f32_16x16x32_bf16 v[188:191], a[124:127], a[60:63], v[188:191]\n\t"
    "s_waitcnt lgkmcnt(7)\n\t"
    "ds_read_b128 a[124:127], v244 offset:7168\n\t"
    "v_mfma_f32_16x16x32_bf16 v[0:3], a[128:131], v[128:131], v[0:3]\n\t"
    "v_exp_f32 v166, v166\n\t"
    "v_mfma_f32_16x16x32_bf16 v[4:7], a[132:135], v[128:131], v[4:7]\n\t"
    "v_exp_f32 v167, v167\n\t"
    "v_mfma_f32_16x16x32_bf16 v[8:11], a[136:139], v[128:131], v[8:11]\n\t"
    "v_cvt_pk_bf16_f32 v163, v166, v167\n\t"
    "v_mfma_f32_16x16x32_bf16 v[12:15], a[140:143], v[128:131], v[12:15]\n\t"
    "v_exp_f32 v168, v168\n\t"
    "v_mfma_f32_16x16x32_bf16 v[16:19], a[144:147], v[128:131], v[16:19]\n\t"
    "v_exp_f32 v169, v169\n\t"
    "v_mfma_f32_16x16x32_bf16 v[20:23], a[148:151], v[128:131], v[20:23]\n\t"
    "v_exp_f32 v170, v170\n\t"
    "v_mfma_f32_16x16x32_bf16 v[24:27], a[152:155], v[128:131], v[24:27]\n\t"
    "v_exp_f32 v171, v171\n\t"
    "v_mfma_f32_16x16x32_bf16 v[28:31], a[156:159], v[128:131], v[28:31]\n\t"
    "v_exp_f32 v172, v172\n\t"
    "v_mfma_f32_16x16x32_bf16 v[32:35], a[128:131], v[136:139], v[32:35]\n\t"
    "v_exp_f32 v173, v173\n\t"
    "v_mfma_f32_16x16x32_bf16 v[36:39], a[132:135], v[136:139], v[36:39]\n\t"
    "v_exp_f32 v174, v174\n\t"
    "v_mfma_f32_16x16x32_bf16 v[40:43], a[136:139], v[136:139], v[40:43]\n\t"
    "v_exp_f32 v175, v175\n\t"
    "v_mfma_f32_16x16x32_bf16 v[44:47], a[140:143], v[136:139], v[44:47]\n\t"
    "v_cvt_pk_bf16_f32 v168, v168, v169\n\t"
    "v_cvt_pk_bf16_f32 v169, v170, v171\n\t"
    "v_mfma_f32_16x16x32_bf16 v[48:51], a[144:147], v[136:139], v[48:51]\n\t"
    "v_cvt_pk_bf16_f32 v170, v172, v173\n\t"
    "v_cvt_pk_bf16_f32 v171, v174, v175\n\t"
    "v_mfma_f32_16x16x32_bf16 v[52:55], a[148:151], v[136:139], v[52:55]\n\t"
    "v_exp_f32 v144, v144\n\t"
    "v_mfma_f32_16x16x32_bf16 v[56:59], a[152:155], v[136:139], v[56:59]\n\t"
    "v_exp_f32 v145, v145\n\t"
    "v_mfma_f32_16x16x32_bf16 v[60:63], a[156:159], v[136:139], v[60:63]\n\t"
    "v_exp_f32 v146, v146\n\t"
    "v_mfma_f32_16x16x32_bf16 v[208:211], v[252:255], v[128:131], v[208:211]\n\t"
    "v_exp_f32 v147, v147\n\t"
    "v_mfma_f32_16x16x32_bf16 v[212:215], v[252:255], v[136:139], v[212:215]\n\t"
    "v_exp_f32 v148, v148\n\t"
    "v_mfma_f32_16x16x32_bf16 v[64:67], a[128:131], v[160:163], v[64:67]\n\t"
    "v_exp_f32 v149, v149\n\t"
    "v_mfma_f32_16x16x32_bf16 v[68:71], a[132:135], v[160:163], v[68:71]\n\t"
    "v_exp_f32 v150, v150\n\t"
    "v_mfma_f32_16x16x32_bf16 v[72:75], a[136:139], v[160:163], v[72:75]\n\t"
    "v_exp_f32 v151, v151\n\t"
    "v_mfma_f32_16x16x32_bf16 v[76:79], a[140:143], v[160:163], v[76:79]\n\t"
    "v_cvt_pk_bf16_f32 v144, v144, v145\n\t"
    "v_cvt_pk_bf16_f32 v145, v146, v147\n\t"
    "v_mfma_f32_16x16x32_bf16 v[80:83], a[144:147], v[160:163], v[80:83]\n\t"
    "v_cvt_pk_bf16_f32 v146, v148, v149\n\t"
    "v_cvt_pk_bf16_f32 v147, v150, v151\n\t"
    "v_mfma_f32_16x16x32_bf16 v[84:87], a[148:151], v[160:163], v[84:87]\n\t"
    "v_exp_f32 v152, v152\n\t"
    "v_mfma_f32_16x16x32_bf16 v[88:91], a[152:155], v[160:163], v[88:91]\n\t"
    "v_exp_f32 v153, v153\n\t"
    "v_mfma_f32_16x16x32_bf16 v[92:95], a[156:159], v[160:163], v[92:95]\n\t"
    "v_exp_f32 v154, v154\n\t"
    "v_mfma_f32_16x16x32_bf16 v[96:99], a[128:131], v[168:171], v[96:99]\n\t"
    "ds_read_b64_tr_b16 a[128:129], v226 offset:8192\n\t"
    "v_cvt_pk_bf16_f32 v152, v152, v153\n\t"
    "v_mfma_f32_16x16x32_bf16 v[100:103], a[132:135], v[168:171], v[100:103]\n\t"
    "ds_read_b64_tr_b16 a[130:131], v226 offset:12288\n\t"
    "v_mfma_f32_16x16x32_bf16 v[104:107], a[136:139], v[168:171], v[104:107]\n\t"
    "ds_read_b64_tr_b16 a[132:133], v225 offset:8192\n\t"
    "v_mfma_f32_16x16x32_bf16 v[108:111], a[140:143], v[168:171], v[108:111]\n\t"
    "ds_read_b64_tr_b16 a[134:135], v225 offset:12288\n\t"
    "v_mfma_f32_16x16x32_bf16 v[112:115], a[144:147], v[168:171], v[112:115]\n\t"
    "ds_read_b64_tr_b16 a[136:137], v226 offset:8704\n\t"
    "v_mfma_f32_16x16x32_bf16 v[116:119], a[148:151], v[168:171], v[116:119]\n\t"
    "ds_read_b64_tr_b16 a[138:139], v226 offset:12800\n\t"
    "v_mfma_f32_16x16x32_bf16 v[120:123], a[152:155], v[168:171], v[120:123]\n\t"
    "ds_read_b64_tr_b16 a[140:141], v225 offset:8704\n\t"
    "v_mfma_f32_16x16x32_bf16 v[124:127], a[156:159], v[168:171], v[124:127]\n\t"
    "ds_read_b64_tr_b16 a[142:143], v225 offset:12800\n\t"
    "v_mfma_f32_16x16x32_bf16 v[216:219], v[252:255], v[160:163], v[216:219]\n\t"
    "ds_read_b64_tr_b16 a[144:145], v226 offset:9216\n\t"
    "v_mfma_f32_16x16x32_bf16 v[220:223], v[252:255], v[168:171], v[220:223]\n\t"
    "s_waitcnt lgkmcnt(9)\n\t"
    "ds_read_b64_tr_b16 a[146:147], v226 offset:13312\n\t"
    "v_mfma_f32_16x16x32_bf16 v[128:131], a[96:99], a[0:3], v[192:195]\n\t"
    "ds_read_b64_tr_b16 a[148:149], v225 offset:9216\n\t"
    "v_mfma_f32_16x16x32_bf16 v[128:131], a[100:103], a[4:7], v[128:131]\n\t"
    "ds_read_b64_tr_b16 a[150:151], v225 offset:13312\n\t"
    "v_mfma_f32_16x16x32_bf16 v[128:131], a[104:107], a[8:11], v[128:131]\n\t"
    "ds_read_b64_tr_b16 a[152:153], v226 offset:9728\n\t"
    "v_mfma_f32_16x16x32_bf16 v[128:131], a[108:111], a[12:15], v[128:131]\n\t"
    "ds_read_b64_tr_b16 a[154:155], v226 offset:13824\n\t"
    "v_mfma_f32_16x16x32_bf16 v[132:135], a[112:115], a[0:3], v[192:195]\n\t"
    "ds_read_b64_tr_b16 a[156:157], v225 offset:9728\n\t"
    "v_mfma_f32_16x16x32_bf16 v[132:135], a[116:119], a[4:7], v[132:135]\n\t"
    "ds_read_b64_tr_b16 a[158:159], v225 offset:13824\n\t"
    "v_mfma_f32_16x16x32_bf16 v[132:135], a[120:123], a[8:11], v[132:135]\n\t"
    "v_exp_f32 v155, v155\n\t"
    "v_mfma_f32_16x16x32_bf16 v[132:135], a[124:127], a[12:15], v[132:135]\n\t"
    "v_exp_f32 v156, v156\n\t"
    "v_mfma_f32_16x16x32_bf16 v[136:139], a[96:99], a[16:19], v[196:199]\n\t"
    "v_exp_f32 v157, v157\n\t"
    "v_mfma_f32_16x16x32_bf16 v[136:139], a[100:103], a[20:23], v[136:139]\n\t"
    "v_exp_f32 v158, v158\n\t"
    "v_mfma_f32_16x16x32_bf16 v[136:139], a[104:107], a[24:27], v[136:139]\n\t"
    "v_exp_f32 v159, v159\n\t"
    "v_mfma_f32_16x16x32_bf16 v[136:139], a[108:111], a[28:31], v[136:139]\n\t"
    "v_cvt_pk_bf16_f32 v153, v154, v155\n\t"
    "v_cvt_pk_bf16_f32 v154, v156, v157\n\t"
    "v_mfma_f32_16x16x32_bf16 v[140:143], a[112:115], a[16:19], v[196:199]\n\t"
    "v_cvt_pk_bf16_f32 v155, v158, v159\n\t"
    "v_mfma_f32_16x16x32_bf16 v[140:143], a[116:119], a[20:23], v[140:143]\n\t"
    "v_exp_f32 v176, v176\n\t"
    "v_mfma_f32_16x16x32_bf16 v[140:143], a[120:123], a[24:27], v[140:143]\n\t"
    "v_exp_f32 v177, v177\n\t"
    "v_mfma_f32_16x16x32_bf16 v[140:143], a[124:127], a[28:31], v[140:143]\n\t"
    "s_waitcnt lgkmcnt(0)\n\t"
    "s_waitcnt vmcnt(0)\n\t"
    "s_barrier\n\t"
    "v_exp_f32 v178, v178\n\t"
    "v_mfma_f32_16x16x32_bf16 v[160:163], a[96:99], a[32:35], v[200:203]\n\t"
    "s_add_u32 m0, %8, 0\n\t"
    "s_add_u32 s40, %9, 0\n\t"
    "v_cvt_pk_bf16_f32 v176, v176, v177\n\t"
    "v_mfma_f32_16x16x32_bf16 v[160:163], a[100:103], a[36:39], v[160:163]\n\t"
    "v_exp_f32 v179, v179\n\t"
    "v_mfma_f32_16x16x32_bf16 v[160:163], a[104:107], a[40:43], v[160:163]\n\t"
    "v_exp_f32 v180, v180\n\t"
    "v_mfma_f32_16x16x32_bf16 v[160:163], a[108:111], a[44:47], v[160:163]\n\t"
    "v_exp_f32 v181, v181\n\t"
    "v_mfma_f32_16x16x32_bf16 v[164:167], a[112:115], a[32:35], v[200:203]\n\t"
    "v_exp_f32 v182, v182\n\t"
    "v_mfma_f32_16x16x32_bf16 v[164:167], a[116:119], a[36:39], v[164:167]\n\t"
    "v_exp_f32 v183, v183\n\t"
    "v_mfma_f32_16x16x32_bf16 v[164:167], a[120:123], a[40:43], v[164:167]\n\t"
    "v_cvt_pk_bf16_f32 v177, v178, v179\n\t"
    "v_cvt_pk_bf16_f32 v178, v180, v181\n\t"
    "v_mfma_f32_16x16x32_bf16 v[164:167], a[124:127], a[44:47], v[164:167]\n\t"
    "v_cvt_pk_bf16_f32 v179, v182, v183\n\t"
    "v_mfma_f32_16x16x32_bf16 v[168:171], a[96:99], a[48:51], v[204:207]\n\t"
    "ds_read_b128 a[96:99], v244 offset:8192\n\t"
    "v_mfma_f32_16x16x32_bf16 v[168:171], a[100:103], a[52:55], v[168:171]\n\t"
    "ds_read_b128 a[100:103], v244 offset:9216\n\t"
    "v_mfma_f32_16x16x32_bf16 v[168:171], a[104:107], a[56:59], v[168:171]\n\t"
    "buffer_load_dwordx4 v227, %3, s40 offen offset:0  lds\n\t"
    "v_mfma_f32_16x16x32_bf16 v[168:171], a[108:111], a[60:63], v[168:171]\n\t"
    "ds_read_b128 a[104:107], v244 offset:10240\n\t"
    "v_mfma_f32_16x16x32_bf16 v[172:175], a[112:115], a[48:51], v[204:207]\n\t"
    "ds_read_b128 a[108:111], v244 offset:11264\n\t"
    "v_mfma_f32_16x16x32_bf16 v[172:175], a[116:119], a[52:55], v[172:175]\n\t"
    "ds_read_b128 a[112:115], v244 offset:12288\n\t"
    "v_mfma_f32_16x16x32_bf16 v[172:175], a[120:123], a[56:59], v[172:175]\n\t"
    "ds_read_b128 a[116:119], v244 offset:13312\n\t"
    "v_mfma_f32_16x16x32_bf16 v[172:175], a[124:127], a[60:63], v[172:175]\n\t"
    "ds_read_b128 a[120:123], v244 offset:14336\n\t"
    "v_mfma_f32_16x16x32_bf16 v[0:3], a[128:131], v[144:147], v[0:3]\n\t"
    "buffer_load_dwordx4 v248, %3, s40 offen offset:1024  lds\n\t"
    "s_add_u32 m0, %8, 8192\n\t"
    "s_add_u32 s40, %9, 8192\n\t"
    "v_mfma_f32_16x16x32_bf16 v[4:7], a[132:135], v[144:147], v[4:7]\n\t"
    "ds_read_b128 a[124:127], v244 offset:15360\n\t"
    "v_mfma_f32_16x16x32_bf16 v[8:11], a[136:139], v[144:147], v[8:11]\n\t"
    "v_exp_f32 v184, v184\n\t"
    "v_mfma_f32_16x16x32_bf16 v[12:15], a[140:143], v[144:147], v[12:15]\n\t"
    "v_exp_f32 v185, v185\n\t"
    "v_mfma_f32_16x16x32_bf16 v[16:19], a[144:147], v[144:147], v[16:19]\n\t"
    "v_exp_f32 v186, v186\n\t"
    "v_mfma_f32_16x16x32_bf16 v[20:23], a[148:151], v[144:147], v[20:23]\n\t"
    "v_exp_f32 v187, v187\n\t"
    "v_mfma_f32_16x16x32_bf16 v[24:27], a[152:155], v[144:147], v[24:27]\n\t"
    "buffer_load_dwordx4 v227, %3, s40 offen offset:0  lds\n\t"
    "v_mfma_f32_16x16x32_bf16 v[28:31], a[156:159], v[144:147], v[28:31]\n\t"
    "v_exp_f32 v188, v188\n\t"
    "v_mfma_f32_16x16x32_bf16 v[32:35], a[128:131], v[152:155], v[32:35]\n\t"
    "v_exp_f32 v189, v189\n\t"
    "v_mfma_f32_16x16x32_bf16 v[36:39], a[132:135], v[152:155], v[36:39]\n\t"
    "v_exp_f32 v190, v190\n\t"
    "v_mfma_f32_16x16x32_bf16 v[40:43], a[136:139], v[152:155], v[40:43]\n\t"
    "v_exp_f32 v191, v191\n\t"
    "v_mfma_f32_16x16x32_bf16 v[44:47], a[140:143], v[152:155], v[44:47]\n\t"
    "v_cvt_pk_bf16_f32 v184, v184, v185\n\t"
    "v_cvt_pk_bf16_f32 v185, v186, v187\n\t"
    "v_mfma_f32_16x16x32_bf16 v[48:51], a[144:147], v[152:155], v[48:51]\n\t"
    "buffer_load_dwordx4 v248, %3, s40 offen offset:1024  lds\n\t"
    "v_mfma_f32_16x16x32_bf16 v[52:55], a[148:151], v[152:155], v[52:55]\n\t"
    "v_cvt_pk_bf16_f32 v186, v188, v189\n\t"
    "v_cvt_pk_bf16_f32 v187, v190, v191\n\t"
    "v_mfma_f32_16x16x32_bf16 v[56:59], a[152:155], v[152:155], v[56:59]\n\t"
    "v_exp_f32 v128, v128\n\t"
    "v_mfma_f32_16x16x32_bf16 v[60:63], a[156:159], v[152:155], v[60:63]\n\t"
    "v_exp_f32 v129, v129\n\t"
    "v_mfma_f32_16x16x32_bf16 v[208:211], v[252:255], v[144:147], v[208:211]\n\t"
    "ds_read_b128 a[72:75], v230 offset:4096\n\t"
    "v_cvt_pk_bf16_f32 v128, v128, v129\n\t"
    "v_mfma_f32_16x16x32_bf16 v[212:215], v[252:255], v[152:155], v[212:215]\n\t"
    "ds_read_b128 a[76:79], v231 offset:4096\n\t"
    "v_mfma_f32_16x16x32_bf16 v[64:67], a[128:131], v[176:179], v[64:67]\n\t"
    "ds_read_b128 a[88:91], v230 offset:6144\n\t"
    "v_mfma_f32_16x16x32_bf16 v[68:71], a[132:135], v[176:179], v[68:71]\n\t"
    "ds_read_b128 a[92:95], v231 offset:6144\n\t"
    "v_mfma_f32_16x16x32_bf16 v[72:75], a[136:139], v[176:179], v[72:75]\n\t"
    "v_exp_f32 v130, v130\n\t"
    "v_mfma_f32_16x16x32_bf16 v[76:79], a[140:143], v[176:179], v[76:79]\n\t"
    "v_exp_f32 v131, v131\n\t"
    "v_mfma_f32_16x16x32_bf16 v[80:83], a[144:147], v[176:179], v[80:83]\n\t"
    "v_exp_f32 v132, v132\n\t"
    "v_mfma_f32_16x16x32_bf16 v[84:87], a[148:151], v[176:179], v[84:87]\n\t"
    "v_exp_f32 v133, v133\n\t"
    "v_mfma_f32_16x16x32_bf16 v[88:91], a[152:155], v[176:179], v[88:91]\n\t"
    "v_exp_f32 v134, v134\n\t"
    "v_mfma_f32_16x16x32_bf16 v[92:95], a[156:159], v[176:179], v[92:95]\n\t"
    "v_exp_f32 v135, v135\n\t"
    "v_mfma_f32_16x16x32_bf16 v[96:99], a[128:131], v[184:187], v[96:99]\n\t"
    "ds_read_b64_tr_b16 a[128:129], v226 offset:16384\n\t"
    "v_cvt_pk_bf16_f32 v129, v130, v131\n\t"
    "v_mfma_f32_16x16x32_bf16 v[100:103], a[132:135], v[184:187], v[100:103]\n\t"
    "ds_read_b64_tr_b16 a[130:131], v226 offset:20480\n\t"
    "v_cvt_pk_bf16_f32 v130, v132, v133\n\t"
    "v_mfma_f32_16x16x32_bf16 v[104:107], a[136:139], v[184:187], v[104:107]\n\t"
    "s_add_u32 m0, %10, 0\n\t"
    "s_add_u32 s40, %11, 73728\n\t"
    "ds_read_b64_tr_b16 a[132:133], v225 offset:16384\n\t"
    "v_mfma_f32_16x16x32_bf16 v[108:111], a[140:143], v[184:187], v[108:111]\n\t"
    "ds_read_b64_tr_b16 a[134:135], v225 offset:20480\n\t"
    "v_cvt_pk_bf16_f32 v131, v134, v135\n\t"
    "v_mfma_f32_16x16x32_bf16 v[112:115], a[144:147], v[184:187], v[112:115]\n\t"
    "ds_read_b64_tr_b16 a[136:137], v226 offset:16896\n\t"
    "v_mfma_f32_16x16x32_bf16 v[116:119], a[148:151], v[184:187], v[116:119]\n\t"
    "ds_read_b64_tr_b16 a[138:139], v226 offset:20992\n\t"
    "v_mfma_f32_16x16x32_bf16 v[120:123], a[152:155], v[184:187], v[120:123]\n\t"
    "ds_read_b64_tr_b16 a[140:141], v225 offset:16896\n\t"
    "v_mfma_f32_16x16x32_bf16 v[124:127], a[156:159], v[184:187], v[124:127]\n\t"
    "ds_read_b64_tr_b16 a[142:143], v225 offset:20992\n\t"
    "v_mfma_f32_16x16x32_bf16 v[216:219], v[252:255], v[176:179], v[216:219]\n\t"
    "ds_read_b64_tr_b16 a[144:145], v226 offset:17408\n\t"
    "v_mfma_f32_16x16x32_bf16 v[220:223], v[252:255], v[184:187], v[220:223]\n\t"
    "s_waitcnt lgkmcnt(9)\n\t"
    "ds_read_b64_tr_b16 a[146:147], v226 offset:21504\n\t"
    "v_mfma_f32_16x16x32_bf16 v[144:147], a[96:99], a[0:3], v[192:195]\n\t"
    "ds_read_b64_tr_b16 a[148:149], v225 offset:17408\n\t"
    "v_mfma_f32_16x16x32_bf16 v[144:147], a[100:103], a[4:7], v[144:147]\n\t"
    "ds_read_b64_tr_b16 a[150:151], v225 offset:21504\n\t"
    "v_mfma_f32_16x16x32_bf16 v[144:147], a[104:107], a[8:11], v[144:147]\n\t"
    "ds_read_b64_tr_b16 a[152:153], v226 offset:17920\n\t"
    "v_mfma_f32_16x16x32_bf16 v[144:147], a[108:111], a[12:15], v[144:147]\n\t"
    "ds_read_b64_tr_b16 a[154:155], v226 offset:22016\n\t"
    "v_mfma_f32_16x16x32_bf16 v[148:151], a[112:115], a[0:3], v[192:195]\n\t"
    "ds_read_b64_tr_b16 a[156:157], v225 offset:17920\n\t"
    "v_mfma_f32_16x16x32_bf16 v[148:151], a[116:119], a[4:7], v[148:151]\n\t"
    "ds_read_b64_tr_b16 a[158:159], v225 offset:22016\n\t"
    "v_mfma_f32_16x16x32_bf16 v[148:151], a[120:123], a[8:11], v[148:151]\n\t"
    "v_exp_f32 v136, v136\n\t"
    "v_mfma_f32_16x16x32_bf16 v[148:151], a[124:127], a[12:15], v[148:151]\n\t"
    "v_exp_f32 v137, v137\n\t"
    "v_mfma_f32_16x16x32_bf16 v[152:155], a[96:99], a[16:19], v[196:199]\n\t"
    "v_exp_f32 v138, v138\n\t"
    "v_mfma_f32_16x16x32_bf16 v[152:155], a[100:103], a[20:23], v[152:155]\n\t"
    "v_exp_f32 v139, v139\n\t"
    "v_mfma_f32_16x16x32_bf16 v[152:155], a[104:107], a[24:27], v[152:155]\n\t"
    "v_exp_f32 v140, v140\n\t"
    "v_mfma_f32_16x16x32_bf16 v[152:155], a[108:111], a[28:31], v[152:155]\n\t"
    "v_exp_f32 v141, v141\n\t"
    "v_mfma_f32_16x16x32_bf16 v[156:159], a[112:115], a[16:19], v[196:199]\n\t"
    "v_exp_f32 v142, v142\n\t"
    "v_mfma_f32_16x16x32_bf16 v[156:159], a[116:119], a[20:23], v[156:159]\n\t"
    "v_exp_f32 v143, v143\n\t"
    "v_mfma_f32_16x16x32_bf16 v[156:159], a[120:123], a[24:27], v[156:159]\n\t"
    "v_cvt_pk_bf16_f32 v136, v136, v137\n\t"
    "v_cvt_pk_bf16_f32 v137, v138, v139\n\t"
    "v_mfma_f32_16x16x32_bf16 v[156:159], a[124:127], a[28:31], v[156:159]\n\t"
    "v_cvt_pk_bf16_f32 v138, v140, v141\n\t"
    "v_cvt_pk_bf16_f32 v139, v142, v143\n\t"
    "v_mfma_f32_16x16x32_bf16 v[176:179], a[96:99], a[32:35], v[200:203]\n\t"
    "v_exp_f32 v160, v160\n\t"
    "v_mfma_f32_16x16x32_bf16 v[176:179], a[100:103], a[36:39], v[176:179]\n\t"
    "buffer_load_dwordx4 v228, %1, s40 offen offset:0 nt lds\n\t"
    "v_mfma_f32_16x16x32_bf16 v[176:179], a[104:107], a[40:43], v[176:179]\n\t"
    "v_exp_f32 v161, v161\n\t"
    "v_mfma_f32_16x16x32_bf16 v[176:179], a[108:111], a[44:47], v[176:179]\n\t"
    "v_exp_f32 v162, v162\n\t"
    "v_mfma_f32_16x16x32_bf16 v[180:183], a[112:115], a[32:35], v[200:203]\n\t"
    "v_exp_f32 v163, v163\n\t"
    "v_mfma_f32_16x16x32_bf16 v[180:183], a[116:119], a[36:39], v[180:183]\n\t"
    "v_exp_f32 v164, v164\n\t"
    "v_mfma_f32_16x16x32_bf16 v[180:183], a[120:123], a[40:43], v[180:183]\n\t"
    "v_exp_f32 v165, v165\n\t"
    "v_mfma_f32_16x16x32_bf16 v[180:183], a[124:127], a[44:47], v[180:183]\n\t"
    "v_exp_f32 v166, v166\n\t"
    "v_mfma_f32_16x16x32_bf16 v[184:187], a[96:99], a[48:51], v[204:207]\n\t"
    "v_exp_f32 v167, v167\n\t"
    "v_mfma_f32_16x16x32_bf16 v[184:187], a[100:103], a[52:55], v[184:187]\n\t"
    "buffer_load_dwordx4 v249, %1, s40 offen offset:1024 nt lds\n\t"
    "v_mfma_f32_16x16x32_bf16 v[184:187], a[104:107], a[56:59], v[184:187]\n\t"
    "v_cvt_pk_bf16_f32 v160, v160, v161\n\t"
    "v_cvt_pk_bf16_f32 v161, v162, v163\n\t"
    "v_mfma_f32_16x16x32_bf16 v[184:187], a[108:111], a[60:63], v[184:187]\n\t"
    "v_cvt_pk_bf16_f32 v162, v164, v165\n\t"
    "v_cvt_pk_bf16_f32 v163, v166, v167\n\t"
    "v_mfma_f32_16x16x32_bf16 v[188:191], a[112:115], a[48:51], v[204:207]\n\t"
    "v_exp_f32 v168, v168\n\t"
    "v_mfma_f32_16x16x32_bf16 v[188:191], a[116:119], a[52:55], v[188:191]\n\t"
    "v_exp_f32 v169, v169\n\t"
    "v_mfma_f32_16x16x32_bf16 v[188:191], a[120:123], a[56:59], v[188:191]\n\t"
    "v_exp_f32 v170, v170\n\t"
    "v_mfma_f32_16x16x32_bf16 v[188:191], a[124:127], a[60:63], v[188:191]\n\t"
    "s_waitcnt lgkmcnt(0)\n\t"
    "v_exp_f32 v171, v171\n\t"
    "v_mfma_f32_16x16x32_bf16 v[0:3], a[128:131], v[128:131], v[0:3]\n\t"
    "v_exp_f32 v172, v172\n\t"
    "v_mfma_f32_16x16x32_bf16 v[4:7], a[132:135], v[128:131], v[4:7]\n\t"
    "buffer_load_dwordx4 v250, %1, s40 offen offset:2048 nt lds\n\t"
    "v_mfma_f32_16x16x32_bf16 v[8:11], a[136:139], v[128:131], v[8:11]\n\t"
    "v_exp_f32 v173, v173\n\t"
    "v_mfma_f32_16x16x32_bf16 v[12:15], a[140:143], v[128:131], v[12:15]\n\t"
    "v_exp_f32 v174, v174\n\t"
    "v_mfma_f32_16x16x32_bf16 v[16:19], a[144:147], v[128:131], v[16:19]\n\t"
    "v_exp_f32 v175, v175\n\t"
    "v_mfma_f32_16x16x32_bf16 v[20:23], a[148:151], v[128:131], v[20:23]\n\t"
    "v_cvt_pk_bf16_f32 v168, v168, v169\n\t"
    "v_cvt_pk_bf16_f32 v169, v170, v171\n\t"
    "v_mfma_f32_16x16x32_bf16 v[24:27], a[152:155], v[128:131], v[24:27]\n\t"
    "v_cvt_pk_bf16_f32 v170, v172, v173\n\t"
    "v_cvt_pk_bf16_f32 v171, v174, v175\n\t"
    "v_mfma_f32_16x16x32_bf16 v[28:31], a[156:159], v[128:131], v[28:31]\n\t"
    "v_exp_f32 v144, v144\n\t"
    "v_mfma_f32_16x16x32_bf16 v[32:35], a[128:131], v[136:139], v[32:35]\n\t"
    "v_exp_f32 v145, v145\n\t"
    "v_mfma_f32_16x16x32_bf16 v[36:39], a[132:135], v[136:139], v[36:39]\n\t"
    "buffer_load_dwordx4 v251, %1, s40 offen offset:3072 nt lds\n\t"
    "v_mfma_f32_16x16x32_bf16 v[40:43], a[136:139], v[136:139], v[40:43]\n\t"
    "v_exp_f32 v146, v146\n\t"
    "v_mfma_f32_16x16x32_bf16 v[44:47], a[140:143], v[136:139], v[44:47]\n\t"
    "v_exp_f32 v147, v147\n\t"
    "v_mfma_f32_16x16x32_bf16 v[48:51], a[144:147], v[136:139], v[48:51]\n\t"
    "v_exp_f32 v148, v148\n\t"
    "v_mfma_f32_16x16x32_bf16 v[52:55], a[148:151], v[136:139], v[52:55]\n\t"
    "v_exp_f32 v149, v149\n\t"
    "v_mfma_f32_16x16x32_bf16 v[56:59], a[152:155], v[136:139], v[56:59]\n\t"
    "v_exp_f32 v150, v150\n\t"
    "v_mfma_f32_16x16x32_bf16 v[60:63], a[156:159], v[136:139], v[60:63]\n\t"
    "v_exp_f32 v151, v151\n\t"
    "v_mfma_f32_16x16x32_bf16 v[208:211], v[252:255], v[128:131], v[208:211]\n\t"
    "v_cvt_pk_bf16_f32 v144, v144, v145\n\t"
    "v_cvt_pk_bf16_f32 v145, v146, v147\n\t"
    "v_mfma_f32_16x16x32_bf16 v[212:215], v[252:255], v[136:139], v[212:215]\n\t"
    "v_exp_f32 v152, v152\n\t"
    "v_mfma_f32_16x16x32_bf16 v[64:67], a[128:131], v[160:163], v[64:67]\n\t"
    "v_exp_f32 v153, v153\n\t"
    "v_mfma_f32_16x16x32_bf16 v[68:71], a[132:135], v[160:163], v[68:71]\n\t"
    "v_cvt_pk_bf16_f32 v146, v148, v149\n\t"
    "v_cvt_pk_bf16_f32 v147, v150, v151\n\t"
    "v_mfma_f32_16x16x32_bf16 v[72:75], a[136:139], v[160:163], v[72:75]\n\t"
    "v_exp_f32 v154, v154\n\t"
    "v_mfma_f32_16x16x32_bf16 v[76:79], a[140:143], v[160:163], v[76:79]\n\t"
    "v_exp_f32 v155, v155\n\t"
    "v_mfma_f32_16x16x32_bf16 v[80:83], a[144:147], v[160:163], v[80:83]\n\t"
    "v_exp_f32 v156, v156\n\t"
    "v_mfma_f32_16x16x32_bf16 v[84:87], a[148:151], v[160:163], v[84:87]\n\t"
    "v_exp_f32 v157, v157\n\t"
    "v_mfma_f32_16x16x32_bf16 v[88:91], a[152:155], v[160:163], v[88:91]\n\t"
    "v_exp_f32 v158, v158\n\t"
    "v_mfma_f32_16x16x32_bf16 v[92:95], a[156:159], v[160:163], v[92:95]\n\t"
    "v_exp_f32 v159, v159\n\t"
    "v_mfma_f32_16x16x32_bf16 v[96:99], a[128:131], v[168:171], v[96:99]\n\t"
    "ds_read_b64_tr_b16 a[128:129], v226 offset:24576\n\t"
    "v_cvt_pk_bf16_f32 v152, v152, v153\n\t"
    "v_mfma_f32_16x16x32_bf16 v[100:103], a[132:135], v[168:171], v[100:103]\n\t"
    "ds_read_b64_tr_b16 a[130:131], v226 offset:28672\n\t"
    "v_cvt_pk_bf16_f32 v153, v154, v155\n\t"
    "v_mfma_f32_16x16x32_bf16 v[104:107], a[136:139], v[168:171], v[104:107]\n\t"
    "ds_read_b64_tr_b16 a[132:133], v225 offset:24576\n\t"
    "v_cvt_pk_bf16_f32 v154, v156, v157\n\t"
    "v_mfma_f32_16x16x32_bf16 v[108:111], a[140:143], v[168:171], v[108:111]\n\t"
    "ds_read_b64_tr_b16 a[134:135], v225 offset:28672\n\t"
    "v_cvt_pk_bf16_f32 v155, v158, v159\n\t"
    "v_mfma_f32_16x16x32_bf16 v[112:115], a[144:147], v[168:171], v[112:115]\n\t"
    "ds_read_b64_tr_b16 a[136:137], v226 offset:25088\n\t"
    "v_mfma_f32_16x16x32_bf16 v[116:119], a[148:151], v[168:171], v[116:119]\n\t"
    "ds_read_b64_tr_b16 a[138:139], v226 offset:29184\n\t"
    "v_mfma_f32_16x16x32_bf16 v[120:123], a[152:155], v[168:171], v[120:123]\n\t"
    "ds_read_b64_tr_b16 a[140:141], v225 offset:25088\n\t"
    "v_mfma_f32_16x16x32_bf16 v[124:127], a[156:159], v[168:171], v[124:127]\n\t"
    "ds_read_b64_tr_b16 a[142:143], v225 offset:29184\n\t"
    "v_mfma_f32_16x16x32_bf16 v[216:219], v[252:255], v[160:163], v[216:219]\n\t"
    "ds_read_b64_tr_b16 a[144:145], v226 offset:25600\n\t"
    "v_mfma_f32_16x16x32_bf16 v[220:223], v[252:255], v[168:171], v[220:223]\n\t"
    "ds_read_b64_tr_b16 a[146:147], v226 offset:29696\n\t"
    "v_mfma_f32_16x16x32_bf16 v[128:131], a[192:195], a[64:67], 0\n\t"
    "ds_read_b64_tr_b16 a[148:149], v225 offset:25600\n\t"
    "v_mfma_f32_16x16x32_bf16 v[128:131], a[196:199], a[68:71], v[128:131]\n\t"
    "ds_read_b64_tr_b16 a[150:151], v225 offset:29696\n\t"
    "v_mfma_f32_16x16x32_bf16 v[128:131], a[200:203], a[72:75], v[128:131]\n\t"
    "ds_read_b64_tr_b16 a[152:153], v226 offset:26112\n\t"
    "v_mfma_f32_16x16x32_bf16 v[128:131], a[204:207], a[76:79], v[128:131]\n\t"
    "ds_read_b64_tr_b16 a[154:155], v226 offset:30208\n\t"
    "v_mfma_f32_16x16x32_bf16 v[132:135], a[208:211], a[64:67], 0\n\t"
    "ds_read_b64_tr_b16 a[156:157], v225 offset:26112\n\t"
    "v_mfma_f32_16x16x32_bf16 v[132:135], a[212:215], a[68:71], v[132:135]\n\t"
    "ds_read_b64_tr_b16 a[158:159], v225 offset:30208\n\t"
    "v_mfma_f32_16x16x32_bf16 v[132:135], a[216:219], a[72:75], v[132:135]\n\t"
    "v_exp_f32 v176, v176\n\t"
    "v_mfma_f32_16x16x32_bf16 v[132:135], a[220:223], a[76:79], v[132:135]\n\t"
    "v_exp_f32 v177, v177\n\t"
    "v_mfma_f32_16x16x32_bf16 v[136:139], a[192:195], a[80:83], 0\n\t"
    "v_exp_f32 v178, v178\n\t"
    "v_mfma_f32_16x16x32_bf16 v[136:139], a[196:199], a[84:87], v[136:139]\n\t"
    "v_exp_f32 v179, v179\n\t"
    "v_mfma_f32_16x16x32_bf16 v[136:139], a[200:203], a[88:91], v[136:139]\n\t"
    "v_exp_f32 v180, v180\n\t"
    "v_mfma_f32_16x16x32_bf16 v[136:139], a[204:207], a[92:95], v[136:139]\n\t"
    "v_exp_f32 v181, v181\n\t"
    "v_mfma_f32_16x16x32_bf16 v[140:143], a[208:211], a[80:83], 0\n\t"
    "v_exp_f32 v182, v182\n\t"
    "v_mfma_f32_16x16x32_bf16 v[140:143], a[212:215], a[84:87], v[140:143]\n\t"
    "v_exp_f32 v183, v183\n\t"
    "v_mfma_f32_16x16x32_bf16 v[140:143], a[216:219], a[88:91], v[140:143]\n\t"
    "v_cvt_pk_bf16_f32 v176, v176, v177\n\t"
    "v_cvt_pk_bf16_f32 v177, v178, v179\n\t"
    "v_mfma_f32_16x16x32_bf16 v[140:143], a[220:223], a[92:95], v[140:143]\n\t"
    "s_waitcnt lgkmcnt(0)\n\t"
    "s_waitcnt vmcnt(4)\n\t"
    "s_barrier\n\t"
    "v_cvt_pk_bf16_f32 v178, v180, v181\n\t"
    "v_cvt_pk_bf16_f32 v179, v182, v183\n\t"
    "v_mfma_f32_16x16x32_bf16 v[160:163], a[192:195], a[32:35], v[200:203]\n\t"
    "s_add_u32 m0, %8, 16384\n\t"
    "s_add_u32 s40, %9, 16384\n\t"
    "v_max3_f32 v229, v128, v129, v130\n\t"
    "v_mfma_f32_16x16x32_bf16 v[160:163], a[196:199], a[36:39], v[160:163]\n\t"
    "v_max3_f32 v232, v131, v132, v133\n\t"
    "v_max3_f32 v229, v229, v134, v135\n\t"
    "v_mfma_f32_16x16x32_bf16 v[160:163], a[200:203], a[40:43], v[160:163]\n\t"
    "v_max_f32 v229, v229, v232\n\t"
    "v_mov_b32 v232, v229\n\t"
    "v_mfma_f32_16x16x32_bf16 v[160:163], a[204:207], a[44:47], v[160:163]\n\t"
    "v_max3_f32 v233, v136, v137, v138\n\t"
    "v_permlane16_swap_b32 v229, v232\n\t"
    "v_mfma_f32_16x16x32_bf16 v[164:167], a[208:211], a[32:35], v[200:203]\n\t"
    "v_max_f32 v229, v229, v232\n\t"
    "v_mov_b32 v232, v229\n\t"
    "v_mfma_f32_16x16x32_bf16 v[164:167], a[212:215], a[36:39], v[164:167]\n\t"
    "v_max3_f32 v241, v139, v140, v141\n\t"
    "v_permlane32_swap_b32 v229, v232\n\t"
    "v_mfma_f32_16x16x32_bf16 v[164:167], a[216:219], a[40:43], v[164:167]\n\t"
    "v_max3_f32 v233, v233, v142, v143\n\t"
    "v_min_f32_e64 v192, -v229, -v232\n\t"
    "v_mfma_f32_16x16x32_bf16 v[164:167], a[220:223], a[44:47], v[164:167]\n\t"
    "v_min_f32_e64 v193, -v229, -v232\n\t"
    "v_min_f32_e64 v194, -v229, -v232\n\t"
    "v_mfma_f32_16x16x32_bf16 v[168:171], a[192:195], a[48:51], v[204:207]\n\t"
    "v_min_f32_e64 v195, -v229, -v232\n\t"
    "v_max_f32 v233, v233, v241\n\t"
    "v_mfma_f32_16x16x32_bf16 v[168:171], a[196:199], a[52:55], v[168:171]\n\t"
    "v_mov_b32 v241, v233\n\t"
    "v_add_f32 v128, v192, v128\n\t"
    "v_mfma_f32_16x16x32_bf16 v[168:171], a[200:203], a[56:59], v[168:171]\n\t"
    "buffer_load_dwordx4 v227, %3, s40 offen offset:0  lds\n\t"
    "v_mfma_f32_16x16x32_bf16 v[168:171], a[204:207], a[60:63], v[168:171]\n\t"
    "v_permlane16_swap_b32 v233, v241\n\t"
    "v_max_f32 v233, v233, v241\n\t"
    "v_mfma_f32_16x16x32_bf16 v[172:175], a[208:211], a[48:51], v[204:207]\n\t"
    "v_mov_b32 v241, v233\n\t"
    "v_add_f32 v129, v193, v129\n\t"
    "v_mfma_f32_16x16x32_bf16 v[172:175], a[212:215], a[52:55], v[172:175]\n\t"
    "v_exp_f32 v184, v184\n\t"
    "v_mfma_f32_16x16x32_bf16 v[172:175], a[216:219], a[56:59], v[172:175]\n\t"
    "v_exp_f32 v185, v185\n\t"
    "v_mfma_f32_16x16x32_bf16 v[172:175], a[220:223], a[60:63], v[172:175]\n\t"
    "v_exp_f32 v186, v186\n\t"
    "v_mfma_f32_16x16x32_bf16 v[208:211], v[252:255], v[144:147], v[208:211]\n\t"
    "buffer_load_dwordx4 v248, %3, s40 offen offset:1024  lds\n\t"
    "s_add_u32 m0, %8, 24576\n\t"
    "s_add_u32 s40, %9, 24576\n\t"
    "v_mfma_f32_16x16x32_bf16 v[212:215], v[252:255], v[152:155], v[212:215]\n\t"
    "v_exp_f32 v187, v187\n\t"
    "v_mfma_f32_16x16x32_bf16 v[0:3], a[128:131], v[144:147], v[0:3]\n\t"
    "ds_read_b128 a[96:99], v224 offset:0\n\t"
    "v_permlane32_swap_b32 v233, v241\n\t"
    "v_mfma_f32_16x16x32_bf16 v[4:7], a[132:135], v[144:147], v[4:7]\n\t"
    "ds_read_b128 a[100:103], v224 offset:1024\n\t"
    "v_mov_b32 v229, v208\n\t"
    "v_mfma_f32_16x16x32_bf16 v[8:11], a[136:139], v[144:147], v[8:11]\n\t"
    "ds_read_b128 a[104:107], v224 offset:2048\n\t"
    "v_cvt_pk_bf16_f32 v184, v184, v185\n\t"
    "v_mfma_f32_16x16x32_bf16 v[12:15], a[140:143], v[144:147], v[12:15]\n\t"
    "ds_read_b128 a[108:111], v224 offset:3072\n\t"
    "v_min_f32_e64 v196, -v233, -v241\n\t"
    "v_mfma_f32_16x16x32_bf16 v[16:19], a[144:147], v[144:147], v[16:19]\n\t"
    "buffer_load_dwordx4 v227, %3, s40 offen offset:0  lds\n\t"
    "v_mfma_f32_16x16x32_bf16 v[20:23], a[148:151], v[144:147], v[20:23]\n\t"
    "ds_read_b128 a[112:115], v224 offset:4096\n\t"
    "v_min_f32_e64 v197, -v233, -v241\n\t"
    "v_mfma_f32_16x16x32_bf16 v[24:27], a[152:155], v[144:147], v[24:27]\n\t"
    "ds_read_b128 a[116:119], v224 offset:5120\n\t"
    "v_min_f32_e64 v198, -v233, -v241\n\t"
    "v_mfma_f32_16x16x32_bf16 v[28:31], a[156:159], v[144:147], v[28:31]\n\t"
    "ds_read_b128 a[120:123], v224 offset:6144\n\t"
    "v_min_f32_e64 v199, -v233, -v241\n\t"
    "v_mfma_f32_16x16x32_bf16 v[32:35], a[128:131], v[152:155], v[32:35]\n\t"
    "ds_read_b128 a[124:127], v224 offset:7168\n\t"
    "v_cvt_pk_bf16_f32 v185, v186, v187\n\t"
    "v_mfma_f32_16x16x32_bf16 v[36:39], a[132:135], v[152:155], v[36:39]\n\t"
    "v_rcp_f32 v242, v229\n\t"
    "v_mfma_f32_16x16x32_bf16 v[40:43], a[136:139], v[152:155], v[40:43]\n\t"
    "buffer_load_dwordx4 v248, %3, s40 offen offset:1024  lds\n\t"
    "v_mfma_f32_16x16x32_bf16 v[44:47], a[140:143], v[152:155], v[44:47]\n\t"
    "v_mul_f32 v0, v242, v0\n\t"
    "v_mul_f32 v1, v242, v1\n\t"
    "v_mfma_f32_16x16x32_bf16 v[48:51], a[144:147], v[152:155], v[48:51]\n\t"
    "v_mul_f32 v2, v242, v2\n\t"
    "v_mul_f32 v3, v242, v3\n\t"
    "v_mfma_f32_16x16x32_bf16 v[52:55], a[148:151], v[152:155], v[52:55]\n\t"
    "v_cvt_pk_bf16_f32 v0, v0, v1\n\t"
    "v_cvt_pk_bf16_f32 v1, v2, v3\n\t"
    "v_mfma_f32_16x16x32_bf16 v[56:59], a[152:155], v[152:155], v[56:59]\n\t"
    "v_exp_f32 v188, v188\n\t"
    "v_mfma_f32_16x16x32_bf16 v[60:63], a[156:159], v[152:155], v[60:63]\n\t"
    "v_exp_f32 v189, v189\n\t"
    "v_mfma_f32_16x16x32_bf16 v[64:67], a[128:131], v[176:179], v[64:67]\n\t"
    "v_exp_f32 v190, v190\n\t"
    "v_mfma_f32_16x16x32_bf16 v[68:71], a[132:135], v[176:179], v[68:71]\n\t"
    "v_exp_f32 v191, v191\n\t"
    "v_mfma_f32_16x16x32_bf16 v[72:75], a[136:139], v[176:179], v[72:75]\n\t"
    "v_mov_b32 v233, v212\n\t"
    "v_cvt_pk_bf16_f32 v186, v188, v189\n\t"
    "v_mfma_f32_16x16x32_bf16 v[76:79], a[140:143], v[176:179], v[76:79]\n\t"
    "v_cvt_pk_bf16_f32 v187, v190, v191\n\t"
    "v_mul_f32 v4, v242, v4\n\t"
    "ds_write_b64 v234, v[0:1] offset:0\n\t"
    "v_mfma_f32_16x16x32_bf16 v[80:83], a[144:147], v[176:179], v[80:83]\n\t"
    "v_rcp_f32 v243, v233\n\t"
    "v_mfma_f32_16x16x32_bf16 v[84:87], a[148:151], v[176:179], v[84:87]\n\t"
    "v_mul_f32 v32, v243, v32\n\t"
    "v_mul_f32 v33, v243, v33\n\t"
    "v_mfma_f32_16x16x32_bf16 v[88:91], a[152:155], v[176:179], v[88:91]\n\t"
    "v_mul_f32 v34, v243, v34\n\t"
    "v_mul_f32 v35, v243, v35\n\t"
    "v_mfma_f32_16x16x32_bf16 v[92:95], a[156:159], v[176:179], v[92:95]\n\t"
    "v_cvt_pk_bf16_f32 v32, v32, v33\n\t"
    "v_cvt_pk_bf16_f32 v33, v34, v35\n\t"
    "ds_write_b64 v234, v[32:33] offset:2048\n\t"
    "v_mfma_f32_16x16x32_bf16 v[96:99], a[128:131], v[184:187], v[96:99]\n\t"
    "ds_read_b64_tr_b16 a[128:129], v226 offset:0\n\t"
    "v_mul_f32 v5, v242, v5\n\t"
    "v_mfma_f32_16x16x32_bf16 v[100:103], a[132:135], v[184:187], v[100:103]\n\t"
    "ds_read_b64_tr_b16 a[130:131], v226 offset:4096\n\t"
    "v_mul_f32 v6, v242, v6\n\t"
    "v_mfma_f32_16x16x32_bf16 v[104:107], a[136:139], v[184:187], v[104:107]\n\t"
    "s_add_u32 m0, %10, 4096\n\t"
    "s_add_u32 s40, %11, 73856\n\t"
    "ds_read_b64_tr_b16 a[132:133], v225 offset:0\n\t"
    "v_mfma_f32_16x16x32_bf16 v[108:111], a[140:143], v[184:187], v[108:111]\n\t"
    "ds_read_b64_tr_b16 a[134:135], v225 offset:4096\n\t"
    "v_mul_f32 v7, v242, v7\n\t"
    "v_mfma_f32_16x16x32_bf16 v[112:115], a[144:147], v[184:187], v[112:115]\n\t"
    "ds_read_b64_tr_b16 a[136:137], v226 offset:512\n\t"
    "v_cvt_pk_bf16_f32 v4, v4, v5\n\t"
    "v_mfma_f32_16x16x32_bf16 v[116:119], a[148:151], v[184:187], v[116:119]\n\t"
    "ds_read_b64_tr_b16 a[138:139], v226 offset:4608\n\t"
    "v_cvt_pk_bf16_f32 v5, v6, v7\n\t"
    "ds_write_b64 v237, v[4:5] offset:0\n\t"
    "v_mfma_f32_16x16x32_bf16 v[120:123], a[152:155], v[184:187], v[120:123]\n\t"
    "ds_read_b64_tr_b16 a[140:141], v225 offset:512\n\t"
    "v_mul_f32 v36, v243, v36\n\t"
    "v_mfma_f32_16x16x32_bf16 v[124:127], a[156:159], v[184:187], v[124:127]\n\t"
    "ds_read_b64_tr_b16 a[142:143], v225 offset:4608\n\t"
    "v_mul_f32 v37, v243, v37\n\t"
    "v_mfma_f32_16x16x32_bf16 v[216:219], v[252:255], v[176:179], v[216:219]\n\t"
    "ds_read_b64_tr_b16 a[144:145], v226 offset:1024\n\t"
    "v_mul_f32 v38, v243, v38\n\t"
    "v_mfma_f32_16x16x32_bf16 v[220:223], v[252:255], v[184:187], v[220:223]\n\t"
    "ds_read_b64_tr_b16 a[146:147], v226 offset:5120\n\t"
    "v_mul_f32 v39, v243, v39\n\t"
    "v_mfma_f32_16x16x32_bf16 v[144:147], a[224:227], a[64:67], v[192:195]\n\t"
    "ds_read_b64_tr_b16 a[148:149], v225 offset:1024\n\t"
    "v_cvt_pk_bf16_f32 v36, v36, v37\n\t"
    "v_mfma_f32_16x16x32_bf16 v[144:147], a[228:231], a[68:71], v[144:147]\n\t"
    "ds_read_b64_tr_b16 a[150:151], v225 offset:5120\n\t"
    "v_cvt_pk_bf16_f32 v37, v38, v39\n\t"
    "ds_write_b64 v237, v[36:37] offset:2048\n\t"
    "v_mfma_f32_16x16x32_bf16 v[144:147], a[232:235], a[72:75], v[144:147]\n\t"
    "ds_read_b64_tr_b16 a[152:153], v226 offset:1536\n\t"
    "v_mul_f32 v8, v242, v8\n\t"
    "v_mfma_f32_16x16x32_bf16 v[144:147], a[236:239], a[76:79], v[144:147]\n\t"
    "ds_read_b64_tr_b16 a[154:155], v226 offset:5632\n\t"
    "v_mul_f32 v9, v242, v9\n\t"
    "v_mfma_f32_16x16x32_bf16 v[148:151], a[240:243], a[64:67], v[192:195]\n\t"
    "ds_read_b64_tr_b16 a[156:157], v225 offset:1536\n\t"
    "v_mul_f32 v10, v242, v10\n\t"
    "v_mfma_f32_16x16x32_bf16 v[148:151], a[244:247], a[68:71], v[148:151]\n\t"
    "ds_read_b64_tr_b16 a[158:159], v225 offset:5632\n\t"
    "v_mul_f32 v11, v242, v11\n\t"
    "v_mfma_f32_16x16x32_bf16 v[148:151], a[248:251], a[72:75], v[148:151]\n\t"
    "v_cvt_pk_bf16_f32 v8, v8, v9\n\t"
    "v_cvt_pk_bf16_f32 v9, v10, v11\n\t"
    "ds_write_b64 v246, v[8:9] offset:0\n\t"
    "v_mfma_f32_16x16x32_bf16 v[148:151], a[252:255], a[76:79], v[148:151]\n\t"
    "v_mul_f32 v40, v243, v40\n\t"
    "v_mul_f32 v41, v243, v41\n\t"
    "v_mfma_f32_16x16x32_bf16 v[152:155], a[224:227], a[80:83], v[196:199]\n\t"
    "v_mul_f32 v42, v243, v42\n\t"
    "v_mul_f32 v43, v243, v43\n\t"
    "v_mfma_f32_16x16x32_bf16 v[152:155], a[228:231], a[84:87], v[152:155]\n\t"
    "v_cvt_pk_bf16_f32 v40, v40, v41\n\t"
    "v_cvt_pk_bf16_f32 v41, v42, v43\n\t"
    "ds_write_b64 v246, v[40:41] offset:2048\n\t"
    "v_mfma_f32_16x16x32_bf16 v[152:155], a[232:235], a[88:91], v[152:155]\n\t"
    "v_mul_f32 v12, v242, v12\n\t"
    "v_mul_f32 v13, v242, v13\n\t"
    "v_mfma_f32_16x16x32_bf16 v[152:155], a[236:239], a[92:95], v[152:155]\n\t"
    "v_mul_f32 v14, v242, v14\n\t"
    "v_mul_f32 v15, v242, v15\n\t"
    "v_mfma_f32_16x16x32_bf16 v[156:159], a[240:243], a[80:83], v[196:199]\n\t"
    "v_cvt_pk_bf16_f32 v12, v12, v13\n\t"
    "v_cvt_pk_bf16_f32 v13, v14, v15\n\t"
    "ds_write_b64 v247, v[12:13] offset:0\n\t"
    "v_mfma_f32_16x16x32_bf16 v[156:159], a[244:247], a[84:87], v[156:159]\n\t"
    "v_mul_f32 v44, v243, v44\n\t"
    "v_mul_f32 v45, v243, v45\n\t"
    "v_mfma_f32_16x16x32_bf16 v[156:159], a[248:251], a[88:91], v[156:159]\n\t"
    "v_mul_f32 v46, v243, v46\n\t"
    "v_mul_f32 v47, v243, v47\n\t"
    "v_mfma_f32_16x16x32_bf16 v[156:159], a[252:255], a[92:95], v[156:159]\n\t"
    "v_cvt_pk_bf16_f32 v44, v44, v45\n\t"
    "v_cvt_pk_bf16_f32 v45, v46, v47\n\t"
    "ds_write_b64 v247, v[44:45] offset:2048\n\t"
    "v_mfma_f32_16x16x32_bf16 v[176:179], a[224:227], a[32:35], v[200:203]\n\t"
    "v_mul_f32 v16, v242, v16\n\t"
    "v_mul_f32 v17, v242, v17\n\t"
    "v_mfma_f32_16x16x32_bf16 v[176:179], a[228:231], a[36:39], v[176:179]\n\t"
    "buffer_load_dwordx4 v228, %1, s40 offen offset:0 nt lds\n\t"
    "v_mfma_f32_16x16x32_bf16 v[176:179], a[232:235], a[40:43], v[176:179]\n\t"
    "v_mul_f32 v18, v242, v18\n\t"
    "v_mul_f32 v19, v242, v19\n\t"
    "v_mfma_f32_16x16x32_bf16 v[176:179], a[236:239], a[44:47], v[176:179]\n\t"
    "ds_read_b128 a[160:163], v235 offset:0\n\t"
    "v_cvt_pk_bf16_f32 v16, v16, v17\n\t"
    "v_mfma_f32_16x16x32_bf16 v[180:183], a[240:243], a[32:35], v[200:203]\n\t"
    "v_cvt_pk_bf16_f32 v17, v18, v19\n\t"
    "v_mul_f32 v48, v243, v48\n\t"
    "v_mfma_f32_16x16x32_bf16 v[180:183], a[244:247], a[36:39], v[180:183]\n\t"
    "ds_read2_b64 a[164:167], v235 offset0:129 offset1:128\n\t"
    "v_mul_f32 v49, v243, v49\n\t"
    "v_mfma_f32_16x16x32_bf16 v[180:183], a[248:251], a[40:43], v[180:183]\n\t"
    "v_mul_f32 v50, v243, v50\n\t"
    "v_mul_f32 v51, v243, v51\n\t"
    "v_mfma_f32_16x16x32_bf16 v[180:183], a[252:255], a[44:47], v[180:183]\n\t"
    "ds_read_b128 a[168:171], v235 offset:2048\n\t"
    "v_cvt_pk_bf16_f32 v48, v48, v49\n\t"
    "v_mfma_f32_16x16x32_bf16 v[184:187], a[224:227], a[48:51], v[204:207]\n\t"
    "v_exp_f32 v160, v160\n\t"
    "v_mfma_f32_16x16x32_bf16 v[184:187], a[228:231], a[52:55], v[184:187]\n\t"
    "buffer_load_dwordx4 v249, %1, s40 offen offset:1024 nt lds\n\t"
    "ds_read2_b64 a[172:175], v245 offset0:129 offset1:128\n\t"
    "v_mfma_f32_16x16x32_bf16 v[184:187], a[232:235], a[56:59], v[184:187]\n\t"
    "v_exp_f32 v161, v161\n\t"
    "v_mfma_f32_16x16x32_bf16 v[184:187], a[236:239], a[60:63], v[184:187]\n\t"
    "v_cvt_pk_bf16_f32 v49, v50, v51\n\t"
    "v_cvt_pk_bf16_f32 v160, v160, v161\n\t"
    "v_mfma_f32_16x16x32_bf16 v[188:191], a[240:243], a[48:51], v[204:207]\n\t"
    "v_exp_f32 v162, v162\n\t"
    "v_mfma_f32_16x16x32_bf16 v[188:191], a[244:247], a[52:55], v[188:191]\n\t"
    "v_exp_f32 v163, v163\n\t"
    "v_mfma_f32_16x16x32_bf16 v[188:191], a[248:251], a[56:59], v[188:191]\n\t"
    "v_exp_f32 v164, v164\n\t"
    "v_mfma_f32_16x16x32_bf16 v[188:191], a[252:255], a[60:63], v[188:191]\n\t"
    "s_waitcnt lgkmcnt(4)\n\t"
    "buffer_load_dwordx4 v250, %1, s40 offen offset:2048 nt lds\n\t"
    "ds_write_b64 v234, v[16:17] offset:0\n\t"
    "ds_write_b64 v234, v[48:49] offset:2048\n\t"
    "v_exp_f32 v165, v165\n\t"
    "v_exp_f32 v166, v166\n\t"
    "v_exp_f32 v167, v167\n\t"
    "v_cvt_pk_bf16_f32 v161, v162, v163\n\t"
    "v_cvt_pk_bf16_f32 v162, v164, v165\n\t"
    "v_cvt_pk_bf16_f32 v163, v166, v167\n\t"
    "v_mul_f32 v20, v242, v20\n\t"
    "v_mul_f32 v21, v242, v21\n\t"
    "v_mfma_f32_16x16x32_bf16 v[64:67], a[128:131], v[160:163], v[64:67]\n\t"
    "v_mul_f32 v22, v242, v22\n\t"
    "v_mul_f32 v23, v242, v23\n\t"
    "v_mfma_f32_16x16x32_bf16 v[68:71], a[132:135], v[160:163], v[68:71]\n\t"
    "v_cvt_pk_bf16_f32 v20, v20, v21\n\t"
    "v_cvt_pk_bf16_f32 v21, v22, v23\n\t"
    "ds_write_b64 v237, v[20:21] offset:0\n\t"
    "v_mfma_f32_16x16x32_bf16 v[72:75], a[136:139], v[160:163], v[72:75]\n\t"
    "v_mul_f32 v52, v243, v52\n\t"
    "v_mul_f32 v53, v243, v53\n\t"
    "v_mfma_f32_16x16x32_bf16 v[76:79], a[140:143], v[160:163], v[76:79]\n\t"
    "v_mul_f32 v54, v243, v54\n\t"
    "v_mul_f32 v55, v243, v55\n\t"
    "v_mfma_f32_16x16x32_bf16 v[80:83], a[144:147], v[160:163], v[80:83]\n\t"
    "v_cvt_pk_bf16_f32 v52, v52, v53\n\t"
    "v_cvt_pk_bf16_f32 v53, v54, v55\n\t"
    "ds_write_b64 v237, v[52:53] offset:2048\n\t"
    "v_mfma_f32_16x16x32_bf16 v[84:87], a[148:151], v[160:163], v[84:87]\n\t"
    "v_mul_f32 v24, v242, v24\n\t"
    "v_mul_f32 v25, v242, v25\n\t"
    "v_mfma_f32_16x16x32_bf16 v[88:91], a[152:155], v[160:163], v[88:91]\n\t"
    "v_mul_f32 v26, v242, v26\n\t"
    "v_mul_f32 v27, v242, v27\n\t"
    "v_mfma_f32_16x16x32_bf16 v[92:95], a[156:159], v[160:163], v[92:95]\n\t"
    "buffer_load_dwordx4 v251, %1, s40 offen offset:3072 nt lds\n\t"
    "v_cvt_pk_bf16_f32 v24, v24, v25\n\t"
    "v_cvt_pk_bf16_f32 v25, v26, v27\n\t"
    "ds_write_b64 v246, v[24:25] offset:0\n\t"
    "v_exp_f32 v168, v168\n\t"
    "v_exp_f32 v169, v169\n\t"
    "v_exp_f32 v170, v170\n\t"
    "v_exp_f32 v171, v171\n\t"
    "v_exp_f32 v172, v172\n\t"
    "v_exp_f32 v173, v173\n\t"
    "v_exp_f32 v174, v174\n\t"
    "v_exp_f32 v175, v175\n\t"
    "v_cvt_pk_bf16_f32 v168, v168, v169\n\t"
    "v_cvt_pk_bf16_f32 v169, v170, v171\n\t"
    "v_cvt_pk_bf16_f32 v170, v172, v173\n\t"
    "v_cvt_pk_bf16_f32 v171, v174, v175\n\t"
    "v_mul_f32 v56, v243, v56\n\t"
    "v_mul_f32 v57, v243, v57\n\t"
    "v_mfma_f32_16x16x32_bf16 v[96:99], a[128:131], v[168:171], v[96:99]\n\t"
    "v_mul_f32 v58, v243, v58\n\t"
    "v_mul_f32 v59, v243, v59\n\t"
    "v_mfma_f32_16x16x32_bf16 v[100:103], a[132:135], v[168:171], v[100:103]\n\t"
    "v_cvt_pk_bf16_f32 v56, v56, v57\n\t"
    "v_cvt_pk_bf16_f32 v57, v58, v59\n\t"
    "ds_write_b64 v246, v[56:57] offset:2048\n\t"
    "v_mfma_f32_16x16x32_bf16 v[104:107], a[136:139], v[168:171], v[104:107]\n\t"
    "v_mul_f32 v28, v242, v28\n\t"
    "v_mul_f32 v29, v242, v29\n\t"
    "v_mfma_f32_16x16x32_bf16 v[108:111], a[140:143], v[168:171], v[108:111]\n\t"
    "v_mul_f32 v30, v242, v30\n\t"
    "v_mul_f32 v31, v242, v31\n\t"
    "v_mfma_f32_16x16x32_bf16 v[112:115], a[144:147], v[168:171], v[112:115]\n\t"
    "v_cvt_pk_bf16_f32 v28, v28, v29\n\t"
    "v_cvt_pk_bf16_f32 v29, v30, v31\n\t"
    "ds_write_b64 v247, v[28:29] offset:0\n\t"
    "v_mfma_f32_16x16x32_bf16 v[116:119], a[148:151], v[168:171], v[116:119]\n\t"
    "v_mul_f32 v60, v243, v60\n\t"
    "v_mul_f32 v61, v243, v61\n\t"
    "v_mfma_f32_16x16x32_bf16 v[120:123], a[152:155], v[168:171], v[120:123]\n\t"
    "v_mul_f32 v62, v243, v62\n\t"
    "v_mul_f32 v63, v243, v63\n\t"
    "v_mfma_f32_16x16x32_bf16 v[124:127], a[156:159], v[168:171], v[124:127]\n\t"
    "v_cvt_pk_bf16_f32 v60, v60, v61\n\t"
    "v_cvt_pk_bf16_f32 v61, v62, v63\n\t"
    "ds_write_b64 v247, v[60:61] offset:2048\n\t"
    "v_mfma_f32_16x16x32_bf16 v[216:219], v[252:255], v[160:163], v[216:219]\n\t"
    "v_add_f32 v130, v194, v130\n\t"
    "v_add_f32 v131, v195, v131\n\t"
    "v_mfma_f32_16x16x32_bf16 v[220:223], v[252:255], v[168:171], v[220:223]\n\t"
    "v_exp_f32 v128, v128\n\t"
    "v_mfma_f32_16x16x32_bf16 v[160:163], a[96:99], a[32:35], v[200:203]\n\t"
    "v_exp_f32 v129, v129\n\t"
    "v_mfma_f32_16x16x32_bf16 v[160:163], a[100:103], a[36:39], v[160:163]\n\t"
    "ds_read_b128 a[176:179], v235 offset:0\n\t"
    "v_add_f32 v132, v192, v132\n\t"
    "v_mfma_f32_16x16x32_bf16 v[160:163], a[104:107], a[40:43], v[160:163]\n\t"
    "v_add_f32 v133, v193, v133\n\t"
    "v_add_f32 v134, v194, v134\n\t"
    "v_mfma_f32_16x16x32_bf16 v[160:163], a[108:111], a[44:47], v[160:163]\n\t"
    "ds_read2_b64 a[180:183], v235 offset0:129 offset1:128\n\t"
    "v_add_f32 v135, v195, v135\n\t"
    "v_mfma_f32_16x16x32_bf16 v[164:167], a[112:115], a[32:35], v[200:203]\n\t"
    "v_exp_f32 v130, v130\n\t"
    "v_mfma_f32_16x16x32_bf16 v[164:167], a[116:119], a[36:39], v[164:167]\n\t"
    "ds_read_b128 a[184:187], v235 offset:2048\n\t"
    "v_cvt_pk_bf16_f32 v128, v128, v129\n\t"
    "v_mfma_f32_16x16x32_bf16 v[164:167], a[120:123], a[40:43], v[164:167]\n\t"
    "v_exp_f32 v131, v131\n\t"
    "v_mfma_f32_16x16x32_bf16 v[164:167], a[124:127], a[44:47], v[164:167]\n\t"
    "ds_read2_b64 a[188:191], v245 offset0:129 offset1:128\n\t"
    "v_cvt_pk_bf16_f32 v129, v130, v131\n\t"
    "v_mfma_f32_16x16x32_bf16 v[168:171], a[96:99], a[48:51], v[204:207]\n\t"
    "v_exp_f32 v132, v132\n\t"
    "v_mfma_f32_16x16x32_bf16 v[168:171], a[100:103], a[52:55], v[168:171]\n\t"
    "v_exp_f32 v133, v133\n\t"
    "v_mfma_f32_16x16x32_bf16 v[168:171], a[104:107], a[56:59], v[168:171]\n\t"
    "v_exp_f32 v134, v134\n\t"
    "v_mfma_f32_16x16x32_bf16 v[168:171], a[108:111], a[60:63], v[168:171]\n\t"
    "v_exp_f32 v135, v135\n\t"
    "v_mfma_f32_16x16x32_bf16 v[172:175], a[112:115], a[48:51], v[204:207]\n\t"
    "v_cvt_pk_bf16_f32 v130, v132, v133\n\t"
    "v_cvt_pk_bf16_f32 v131, v134, v135\n\t"
    "v_mfma_f32_16x16x32_bf16 v[172:175], a[116:119], a[52:55], v[172:175]\n\t"
    "v_add_f32 v136, v196, v136\n\t"
    "v_add_f32 v137, v197, v137\n\t"
    "v_mfma_f32_16x16x32_bf16 v[172:175], a[120:123], a[56:59], v[172:175]\n\t"
    "v_add_f32 v138, v198, v138\n\t"
    "v_add_f32 v139, v199, v139\n\t"
    "v_mfma_f32_16x16x32_bf16 v[172:175], a[124:127], a[60:63], v[172:175]\n\t"
    "v_exp_f32 v136, v136\n\t"
    "v_mfma_f32_16x16x32_bf16 v[0:3], a[128:131], v[128:131], 0\n\t"
    "v_exp_f32 v137, v137\n\t"
    "v_mfma_f32_16x16x32_bf16 v[4:7], a[132:135], v[128:131], 0\n\t"
    "v_add_f32 v140, v196, v140\n\t"
    "v_add_f32 v141, v197, v141\n\t"
    "v_mfma_f32_16x16x32_bf16 v[8:11], a[136:139], v[128:131], 0\n\t"
    "v_add_f32 v142, v198, v142\n\t"
    "v_add_f32 v143, v199, v143\n\t"
    "v_mfma_f32_16x16x32_bf16 v[12:15], a[140:143], v[128:131], 0\n\t"
    "v_exp_f32 v138, v138\n\t"
    "v_mfma_f32_16x16x32_bf16 v[16:19], a[144:147], v[128:131], 0\n\t"
    "v_exp_f32 v139, v139\n\t"
    "v_mfma_f32_16x16x32_bf16 v[20:23], a[148:151], v[128:131], 0\n\t"
    "v_exp_f32 v140, v140\n\t"
    "v_mfma_f32_16x16x32_bf16 v[24:27], a[152:155], v[128:131], 0\n\t"
    "v_exp_f32 v141, v141\n\t"
    "v_mfma_f32_16x16x32_bf16 v[28:31], a[156:159], v[128:131], 0\n\t"
    "v_exp_f32 v142, v142\n\t"
    "v_exp_f32 v143, v143\n\t"
    "v_cvt_pk_bf16_f32 v136, v136, v137\n\t"
    "v_cvt_pk_bf16_f32 v137, v138, v139\n\t"
    "v_cvt_pk_bf16_f32 v138, v140, v141\n\t"
    "v_cvt_pk_bf16_f32 v139, v142, v143\n\t"
    "v_exp_f32 v144, v144\n\t"
    "s_nop 0\n\t"
    "v_mfma_f32_16x16x32_bf16 v[32:35], a[128:131], v[136:139], 0\n\t"
    "ds_read_b64_tr_b16 a[128:129], v226 offset:8192\n\t"
    "v_add_f32 v232, v229, v233\n\t"
    "v_mfma_f32_16x16x32_bf16 v[36:39], a[132:135], v[136:139], 0\n\t"
    "ds_read_b64_tr_b16 a[130:131], v226 offset:12288\n\t"
    "ds_read_b64_tr_b16 a[132:133], v225 offset:8192\n\t"
    "ds_read_b64_tr_b16 a[134:135], v225 offset:12288\n\t"
    "v_mfma_f32_16x16x32_bf16 v[40:43], a[136:139], v[136:139], 0\n\t"
    "ds_read_b64_tr_b16 a[136:137], v226 offset:8704\n\t"
    "ds_read_b64_tr_b16 a[138:139], v226 offset:12800\n\t"
    "v_mfma_f32_16x16x32_bf16 v[44:47], a[140:143], v[136:139], 0\n\t"
    "ds_read_b64_tr_b16 a[140:141], v225 offset:8704\n\t"
    "ds_read_b64_tr_b16 a[142:143], v225 offset:12800\n\t"
    "v_mfma_f32_16x16x32_bf16 v[48:51], a[144:147], v[136:139], 0\n\t"
    "ds_read_b64_tr_b16 a[144:145], v226 offset:9216\n\t"
    "ds_read_b64_tr_b16 a[146:147], v226 offset:13312\n\t"
    "v_mfma_f32_16x16x32_bf16 v[52:55], a[148:151], v[136:139], 0\n\t"
    "ds_read_b64_tr_b16 a[148:149], v225 offset:9216\n\t"
    "ds_read_b64_tr_b16 a[150:151], v225 offset:13312\n\t"
    "v_mfma_f32_16x16x32_bf16 v[56:59], a[152:155], v[136:139], 0\n\t"
    "ds_read_b64_tr_b16 a[152:153], v226 offset:9728\n\t"
    "ds_read_b64_tr_b16 a[154:155], v226 offset:13824\n\t"
    "v_mfma_f32_16x16x32_bf16 v[60:63], a[156:159], v[136:139], 0\n\t"
    "ds_read_b64_tr_b16 a[156:157], v225 offset:9728\n\t"
    "ds_read_b64_tr_b16 a[158:159], v225 offset:13824\n\t"
    "v_mfma_f32_16x16x32_bf16 v[208:211], v[252:255], v[128:131], 0\n\t"
    "v_exp_f32 v145, v145\n\t"
    "v_mfma_f32_16x16x32_bf16 v[212:215], v[252:255], v[136:139], 0\n\t"
    "v_exp_f32 v146, v146\n\t"
    "v_mfma_f32_16x16x32_bf16 v[128:131], a[96:99], a[64:67], v[192:195]\n\t"
    "v_exp_f32 v147, v147\n\t"
    "v_mfma_f32_16x16x32_bf16 v[128:131], a[100:103], a[68:71], v[128:131]\n\t"
    "v_exp_f32 v148, v148\n\t"
    "v_mfma_f32_16x16x32_bf16 v[128:131], a[104:107], a[72:75], v[128:131]\n\t"
    "v_exp_f32 v149, v149\n\t"
    "v_mfma_f32_16x16x32_bf16 v[128:131], a[108:111], a[76:79], v[128:131]\n\t"
    "v_exp_f32 v150, v150\n\t"
    "v_mfma_f32_16x16x32_bf16 v[132:135], a[112:115], a[64:67], v[192:195]\n\t"
    "v_exp_f32 v151, v151\n\t"
    "v_mfma_f32_16x16x32_bf16 v[132:135], a[116:119], a[68:71], v[132:135]\n\t"
    "v_cvt_pk_bf16_f32 v144, v144, v145\n\t"
    "v_cvt_pk_bf16_f32 v145, v146, v147\n\t"
    "v_mfma_f32_16x16x32_bf16 v[132:135], a[120:123], a[72:75], v[132:135]\n\t"
    "s_waitcnt lgkmcnt(0)\n\t"
    "v_cvt_pk_bf16_f32 v146, v148, v149\n\t"
    "v_cvt_pk_bf16_f32 v147, v150, v151\n\t"
    "s_waitcnt vmcnt(4)\n\t"
    "s_barrier\n\t"
    "s_add_u32 m0, %8, 0\n\t"
    "s_add_u32 s40, %9, 32768\n\t"
    "v_mfma_f32_16x16x32_bf16 v[132:135], a[124:127], a[76:79], v[132:135]\n\t"
    "v_exp_f32 v152, v152\n\t"
    "v_mfma_f32_16x16x32_bf16 v[136:139], a[96:99], a[80:83], v[196:199]\n\t"
    "ds_read_b128 a[96:99], v224 offset:8192\n\t"
    "v_mfma_f32_16x16x32_bf16 v[136:139], a[100:103], a[84:87], v[136:139]\n\t"
    "ds_read_b128 a[100:103], v224 offset:9216\n\t"
    "v_mfma_f32_16x16x32_bf16 v[136:139], a[104:107], a[88:91], v[136:139]\n\t"
    "buffer_load_dwordx4 v227, %3, s40 offen offset:0  lds\n\t"
    "v_mfma_f32_16x16x32_bf16 v[136:139], a[108:111], a[92:95], v[136:139]\n\t"
    "ds_read_b128 a[104:107], v224 offset:10240\n\t"
    "v_mfma_f32_16x16x32_bf16 v[140:143], a[112:115], a[80:83], v[196:199]\n\t"
    "ds_read_b128 a[108:111], v224 offset:11264\n\t"
    "v_mfma_f32_16x16x32_bf16 v[140:143], a[116:119], a[84:87], v[140:143]\n\t"
    "ds_read_b128 a[112:115], v224 offset:12288\n\t"
    "v_mfma_f32_16x16x32_bf16 v[140:143], a[120:123], a[88:91], v[140:143]\n\t"
    "ds_read_b128 a[116:119], v224 offset:13312\n\t"
    "v_mfma_f32_16x16x32_bf16 v[140:143], a[124:127], a[92:95], v[140:143]\n\t"
    "ds_read_b128 a[120:123], v224 offset:14336\n\t"
    "v_mfma_f32_16x16x32_bf16 v[0:3], a[128:131], v[144:147], v[0:3]\n\t"
    "buffer_load_dwordx4 v248, %3, s40 offen offset:1024  lds\n\t"
    "s_add_u32 m0, %8, 8192\n\t"
    "s_add_u32 s40, %9, 40960\n\t"
    "v_mfma_f32_16x16x32_bf16 v[4:7], a[132:135], v[144:147], v[4:7]\n\t"
    "ds_read_b128 a[124:127], v224 offset:15360\n\t"
    "v_mfma_f32_16x16x32_bf16 v[8:11], a[136:139], v[144:147], v[8:11]\n\t"
    "v_exp_f32 v153, v153\n\t"
    "v_mfma_f32_16x16x32_bf16 v[12:15], a[140:143], v[144:147], v[12:15]\n\t"
    "v_exp_f32 v154, v154\n\t"
    "v_mfma_f32_16x16x32_bf16 v[16:19], a[144:147], v[144:147], v[16:19]\n\t"
    "v_exp_f32 v155, v155\n\t"
    "v_mfma_f32_16x16x32_bf16 v[20:23], a[148:151], v[144:147], v[20:23]\n\t"
    "v_exp_f32 v156, v156\n\t"
    "v_mfma_f32_16x16x32_bf16 v[24:27], a[152:155], v[144:147], v[24:27]\n\t"
    "buffer_load_dwordx4 v227, %3, s40 offen offset:0  lds\n\t"
    "v_mfma_f32_16x16x32_bf16 v[28:31], a[156:159], v[144:147], v[28:31]\n\t"
    "buffer_load_dwordx4 v248, %3, s40 offen offset:1024  lds\n\t"
    "v_exp_f32 v157, v157\n\t"
    "v_exp_f32 v158, v158\n\t"
    "v_exp_f32 v159, v159\n\t"
    "v_cvt_pk_bf16_f32 v152, v152, v153\n\t"
    "v_cvt_pk_bf16_f32 v153, v154, v155\n\t"
    "v_cvt_pk_bf16_f32 v154, v156, v157\n\t"
    "v_cvt_pk_bf16_f32 v155, v158, v159\n\t"
    "v_exp_f32 v176, v176\n\t"
    "s_nop 0\n\t"
    "v_mfma_f32_16x16x32_bf16 v[32:35], a[128:131], v[152:155], v[32:35]\n\t"
    "v_exp_f32 v177, v177\n\t"
    "v_mfma_f32_16x16x32_bf16 v[36:39], a[132:135], v[152:155], v[36:39]\n\t"
    "v_exp_f32 v178, v178\n\t"
    "v_mfma_f32_16x16x32_bf16 v[40:43], a[136:139], v[152:155], v[40:43]\n\t"
    "v_exp_f32 v179, v179\n\t"
    "v_mfma_f32_16x16x32_bf16 v[44:47], a[140:143], v[152:155], v[44:47]\n\t"
    "v_exp_f32 v180, v180\n\t"
    "v_mfma_f32_16x16x32_bf16 v[48:51], a[144:147], v[152:155], v[48:51]\n\t"
    "v_exp_f32 v181, v181\n\t"
    "v_mfma_f32_16x16x32_bf16 v[52:55], a[148:151], v[152:155], v[52:55]\n\t"
    "v_exp_f32 v182, v182\n\t"
    "v_mfma_f32_16x16x32_bf16 v[56:59], a[152:155], v[152:155], v[56:59]\n\t"
    "v_exp_f32 v183, v183\n\t"
    "v_mfma_f32_16x16x32_bf16 v[60:63], a[156:159], v[152:155], v[60:63]\n\t"
    "v_cvt_pk_bf16_f32 v176, v176, v177\n\t"
    "v_cvt_pk_bf16_f32 v177, v178, v179\n\t"
    "v_mfma_f32_16x16x32_bf16 v[208:211], v[252:255], v[144:147], v[208:211]\n\t"
    "v_cvt_pk_bf16_f32 v178, v180, v181\n\t"
    "v_cvt_pk_bf16_f32 v179, v182, v183\n\t"
    "v_mfma_f32_16x16x32_bf16 v[212:215], v[252:255], v[152:155], v[212:215]\n\t"
    "v_exp_f32 v184, v184\n\t"
    "v_mfma_f32_16x16x32_bf16 v[64:67], a[128:131], v[176:179], v[64:67]\n\t"
    "v_exp_f32 v185, v185\n\t"
    "v_mfma_f32_16x16x32_bf16 v[68:71], a[132:135], v[176:179], v[68:71]\n\t"
    "v_exp_f32 v186, v186\n\t"
    "v_mfma_f32_16x16x32_bf16 v[72:75], a[136:139], v[176:179], v[72:75]\n\t"
    "v_exp_f32 v187, v187\n\t"
    "v_mfma_f32_16x16x32_bf16 v[76:79], a[140:143], v[176:179], v[76:79]\n\t"
    "v_exp_f32 v188, v188\n\t"
    "v_mfma_f32_16x16x32_bf16 v[80:83], a[144:147], v[176:179], v[80:83]\n\t"
    "v_exp_f32 v189, v189\n\t"
    "v_mfma_f32_16x16x32_bf16 v[84:87], a[148:151], v[176:179], v[84:87]\n\t"
    "v_exp_f32 v190, v190\n\t"
    "v_mfma_f32_16x16x32_bf16 v[88:91], a[152:155], v[176:179], v[88:91]\n\t"
    "ds_read_b128 a[0:3], v230 offset:0\n\t"
    "v_cvt_pk_bf16_f32 v184, v184, v185\n\t"
    "v_mfma_f32_16x16x32_bf16 v[92:95], a[156:159], v[176:179], v[92:95]\n\t"
    "ds_read_b128 a[4:7], v231 offset:0\n\t"
    "v_exp_f32 v191, v191\n\t"
    "v_cvt_pk_bf16_f32 v185, v186, v187\n\t"
    "v_cvt_pk_bf16_f32 v186, v188, v189\n\t"
    "v_cvt_pk_bf16_f32 v187, v190, v191\n\t"
    "v_exp_f32 v128, v128\n\t"
    "s_nop 0\n\t"
    "v_mfma_f32_16x16x32_bf16 v[96:99], a[128:131], v[184:187], v[96:99]\n\t"
    "ds_read_b128 a[16:19], v230 offset:2048\n\t"
    "v_mfma_f32_16x16x32_bf16 v[100:103], a[132:135], v[184:187], v[100:103]\n\t"
    "ds_read_b128 a[20:23], v231 offset:2048\n\t"
    "v_mfma_f32_16x16x32_bf16 v[104:107], a[136:139], v[184:187], v[104:107]\n\t"
    "s_add_u32 s40, %11, 0\n\t"
    "ds_read_b64_tr_b16 a[128:129], v226 offset:16384\n\t"
    "v_mfma_f32_16x16x32_bf16 v[108:111], a[140:143], v[184:187], v[108:111]\n\t"
    "ds_read_b64_tr_b16 a[130:131], v226 offset:20480\n\t"
    "v_mfma_f32_16x16x32_bf16 v[112:115], a[144:147], v[184:187], v[112:115]\n\t"
    "ds_read_b64_tr_b16 a[132:133], v225 offset:16384\n\t"
    "v_mfma_f32_16x16x32_bf16 v[116:119], a[148:151], v[184:187], v[116:119]\n\t"
    "ds_read_b64_tr_b16 a[134:135], v225 offset:20480\n\t"
    "v_mfma_f32_16x16x32_bf16 v[120:123], a[152:155], v[184:187], v[120:123]\n\t"
    "ds_read_b64_tr_b16 a[136:137], v226 offset:16896\n\t"
    "v_mfma_f32_16x16x32_bf16 v[124:127], a[156:159], v[184:187], v[124:127]\n\t"
    "ds_read_b64_tr_b16 a[138:139], v226 offset:20992\n\t"
    "v_mfma_f32_16x16x32_bf16 v[216:219], v[252:255], v[176:179], v[216:219]\n\t"
    "ds_read_b64_tr_b16 a[140:141], v225 offset:16896\n\t"
    "v_mfma_f32_16x16x32_bf16 v[220:223], v[252:255], v[184:187], v[220:223]\n\t"
    "s_waitcnt lgkmcnt(11)\n\t"
    "ds_read_b64_tr_b16 a[142:143], v225 offset:20992\n\t"
    "v_mfma_f32_16x16x32_bf16 v[144:147], a[96:99], a[64:67], v[192:195]\n\t"
    "ds_read_b64_tr_b16 a[144:145], v226 offset:17408\n\t"
    "v_mfma_f32_16x16x32_bf16 v[144:147], a[100:103], a[68:71], v[144:147]\n\t"
    "ds_read_b64_tr_b16 a[146:147], v226 offset:21504\n\t"
    "v_mfma_f32_16x16x32_bf16 v[144:147], a[104:107], a[72:75], v[144:147]\n\t"
    "ds_read_b64_tr_b16 a[148:149], v225 offset:17408\n\t"
    "v_mfma_f32_16x16x32_bf16 v[144:147], a[108:111], a[76:79], v[144:147]\n\t"
    "ds_read_b64_tr_b16 a[150:151], v225 offset:21504\n\t"
    "v_mfma_f32_16x16x32_bf16 v[148:151], a[112:115], a[64:67], v[192:195]\n\t"
    "ds_read_b64_tr_b16 a[152:153], v226 offset:17920\n\t"
    "v_mfma_f32_16x16x32_bf16 v[148:151], a[116:119], a[68:71], v[148:151]\n\t"
    "ds_read_b64_tr_b16 a[154:155], v226 offset:22016\n\t"
    "v_mfma_f32_16x16x32_bf16 v[148:151], a[120:123], a[72:75], v[148:151]\n\t"
    "ds_read_b64_tr_b16 a[156:157], v225 offset:17920\n\t"
    "v_mfma_f32_16x16x32_bf16 v[148:151], a[124:127], a[76:79], v[148:151]\n\t"
    "ds_read_b64_tr_b16 a[158:159], v225 offset:22016\n\t"
    "v_mfma_f32_16x16x32_bf16 v[152:155], a[96:99], a[80:83], v[196:199]\n\t"
    "v_exp_f32 v129, v129\n\t"
    "v_mfma_f32_16x16x32_bf16 v[152:155], a[100:103], a[84:87], v[152:155]\n\t"
    "v_exp_f32 v130, v130\n\t"
    "v_mfma_f32_16x16x32_bf16 v[152:155], a[104:107], a[88:91], v[152:155]\n\t"
    "v_exp_f32 v131, v131\n\t"
    "v_mfma_f32_16x16x32_bf16 v[152:155], a[108:111], a[92:95], v[152:155]\n\t"
    "v_exp_f32 v132, v132\n\t"
    "v_mfma_f32_16x16x32_bf16 v[156:159], a[112:115], a[80:83], v[196:199]\n\t"
    "v_exp_f32 v133, v133\n\t"
    "v_mfma_f32_16x16x32_bf16 v[156:159], a[116:119], a[84:87], v[156:159]\n\t"
    "v_exp_f32 v134, v134\n\t"
    "v_mfma_f32_16x16x32_bf16 v[156:159], a[120:123], a[88:91], v[156:159]\n\t"
    "v_exp_f32 v135, v135\n\t"
    "v_mfma_f32_16x16x32_bf16 v[156:159], a[124:127], a[92:95], v[156:159]\n\t"
    "v_cvt_pk_bf16_f32 v128, v128, v129\n\t"
    "v_cvt_pk_bf16_f32 v129, v130, v131\n\t"
    "v_mfma_f32_16x16x32_bf16 v[176:179], a[96:99], a[32:35], v[200:203]\n\t"
    "v_cvt_pk_bf16_f32 v130, v132, v133\n\t"
    "v_cvt_pk_bf16_f32 v131, v134, v135\n\t"
    "v_mfma_f32_16x16x32_bf16 v[176:179], a[100:103], a[36:39], v[176:179]\n\t"
    "buffer_store_dwordx4 a[160:163], v236, %4, s40 offen offset:0 sc1\n\t"
    "v_mfma_f32_16x16x32_bf16 v[176:179], a[104:107], a[40:43], v[176:179]\n\t"
    "v_exp_f32 v136, v136\n\t"
    "v_mfma_f32_16x16x32_bf16 v[176:179], a[108:111], a[44:47], v[176:179]\n\t"
    "v_exp_f32 v137, v137\n\t"
    "v_mfma_f32_16x16x32_bf16 v[180:183], a[112:115], a[32:35], v[200:203]\n\t"
    "v_exp_f32 v138, v138\n\t"
    "v_mfma_f32_16x16x32_bf16 v[180:183], a[116:119], a[36:39], v[180:183]\n\t"
    "v_exp_f32 v139, v139\n\t"
    "v_mfma_f32_16x16x32_bf16 v[180:183], a[120:123], a[40:43], v[180:183]\n\t"
    "v_exp_f32 v140, v140\n\t"
    "v_mfma_f32_16x16x32_bf16 v[180:183], a[124:127], a[44:47], v[180:183]\n\t"
    "v_exp_f32 v141, v141\n\t"
    "v_mfma_f32_16x16x32_bf16 v[184:187], a[96:99], a[48:51], v[204:207]\n\t"
    "ds_read_b128 a[96:99], v224 offset:16384\n\t"
    "buffer_store_dwordx4 a[164:167], v236, %4, s40 offen offset:2048 sc1\n\t"
    "s_add_u32 s40, %11, 4096\n\t"
    "v_mfma_f32_16x16x32_bf16 v[184:187], a[100:103], a[52:55], v[184:187]\n\t"
    "ds_read_b128 a[100:103], v224 offset:17408\n\t"
    "v_cvt_pk_bf16_f32 v136, v136, v137\n\t"
    "v_mfma_f32_16x16x32_bf16 v[184:187], a[104:107], a[56:59], v[184:187]\n\t"
    "ds_read_b128 a[104:107], v224 offset:18432\n\t"
    "v_cvt_pk_bf16_f32 v137, v138, v139\n\t"
    "v_mfma_f32_16x16x32_bf16 v[184:187], a[108:111], a[60:63], v[184:187]\n\t"
    "ds_read_b128 a[108:111], v224 offset:19456\n\t"
    "v_cvt_pk_bf16_f32 v138, v140, v141\n\t"
    "v_mfma_f32_16x16x32_bf16 v[188:191], a[112:115], a[48:51], v[204:207]\n\t"
    "ds_read_b128 a[112:115], v224 offset:20480\n\t"
    "v_mfma_f32_16x16x32_bf16 v[188:191], a[116:119], a[52:55], v[188:191]\n\t"
    "ds_read_b128 a[116:119], v224 offset:21504\n\t"
    "v_mfma_f32_16x16x32_bf16 v[188:191], a[120:123], a[56:59], v[188:191]\n\t"
    "ds_read_b128 a[120:123], v224 offset:22528\n\t"
    "v_mfma_f32_16x16x32_bf16 v[188:191], a[124:127], a[60:63], v[188:191]\n\t"
    "s_waitcnt lgkmcnt(7)\n\t"
    "ds_read_b128 a[124:127], v224 offset:23552\n\t"
    "v_mfma_f32_16x16x32_bf16 v[0:3], a[128:131], v[128:131], v[0:3]\n\t"
    "v_exp_f32 v142, v142\n\t"
    "v_mfma_f32_16x16x32_bf16 v[4:7], a[132:135], v[128:131], v[4:7]\n\t"
    "buffer_store_dwordx4 a[168:171], v236, %4, s40 offen offset:0 sc1\n\t"
    "v_mfma_f32_16x16x32_bf16 v[8:11], a[136:139], v[128:131], v[8:11]\n\t"
    "v_exp_f32 v143, v143\n\t"
    "v_mfma_f32_16x16x32_bf16 v[12:15], a[140:143], v[128:131], v[12:15]\n\t"
    "v_cvt_pk_bf16_f32 v139, v142, v143\n\t"
    "v_mfma_f32_16x16x32_bf16 v[16:19], a[144:147], v[128:131], v[16:19]\n\t"
    "v_exp_f32 v160, v160\n\t"
    "v_mfma_f32_16x16x32_bf16 v[20:23], a[148:151], v[128:131], v[20:23]\n\t"
    "v_exp_f32 v161, v161\n\t"
    "v_mfma_f32_16x16x32_bf16 v[24:27], a[152:155], v[128:131], v[24:27]\n\t"
    "v_exp_f32 v162, v162\n\t"
    "v_mfma_f32_16x16x32_bf16 v[28:31], a[156:159], v[128:131], v[28:31]\n\t"
    "v_exp_f32 v163, v163\n\t"
    "v_mfma_f32_16x16x32_bf16 v[32:35], a[128:131], v[136:139], v[32:35]\n\t"
    "v_exp_f32 v164, v164\n\t"
    "v_mfma_f32_16x16x32_bf16 v[36:39], a[132:135], v[136:139], v[36:39]\n\t"
    "buffer_store_dwordx4 a[172:175], v236, %4, s40 offen offset:2048 sc1\n\t"
    "v_mfma_f32_16x16x32_bf16 v[40:43], a[136:139], v[136:139], v[40:43]\n\t"
    "v_exp_f32 v165, v165\n\t"
    "v_mfma_f32_16x16x32_bf16 v[44:47], a[140:143], v[136:139], v[44:47]\n\t"
    "v_exp_f32 v166, v166\n\t"
    "v_mfma_f32_16x16x32_bf16 v[48:51], a[144:147], v[136:139], v[48:51]\n\t"
    "v_exp_f32 v167, v167\n\t"
    "v_mfma_f32_16x16x32_bf16 v[52:55], a[148:151], v[136:139], v[52:55]\n\t"
    "v_cvt_pk_bf16_f32 v160, v160, v161\n\t"
    "v_cvt_pk_bf16_f32 v161, v162, v163\n\t"
    "v_mfma_f32_16x16x32_bf16 v[56:59], a[152:155], v[136:139], v[56:59]\n\t"
    "v_cvt_pk_bf16_f32 v162, v164, v165\n\t"
    "v_cvt_pk_bf16_f32 v163, v166, v167\n\t"
    "v_mfma_f32_16x16x32_bf16 v[60:63], a[156:159], v[136:139], v[60:63]\n\t"
    "v_exp_f32 v168, v168\n\t"
    "v_mfma_f32_16x16x32_bf16 v[208:211], v[252:255], v[128:131], v[208:211]\n\t"
    "v_exp_f32 v169, v169\n\t"
    "v_mfma_f32_16x16x32_bf16 v[212:215], v[252:255], v[136:139], v[212:215]\n\t"
    "v_exp_f32 v170, v170\n\t"
    "v_mfma_f32_16x16x32_bf16 v[64:67], a[128:131], v[160:163], v[64:67]\n\t"
    "v_exp_f32 v171, v171\n\t"
    "v_mfma_f32_16x16x32_bf16 v[68:71], a[132:135], v[160:163], v[68:71]\n\t"
    "v_exp_f32 v172, v172\n\t"
    "v_mfma_f32_16x16x32_bf16 v[72:75], a[136:139], v[160:163], v[72:75]\n\t"
    "v_exp_f32 v173, v173\n\t"
    "v_mfma_f32_16x16x32_bf16 v[76:79], a[140:143], v[160:163], v[76:79]\n\t"
    "v_exp_f32 v174, v174\n\t"
    "v_mfma_f32_16x16x32_bf16 v[80:83], a[144:147], v[160:163], v[80:83]\n\t"
    "v_exp_f32 v175, v175\n\t"
    "v_mfma_f32_16x16x32_bf16 v[84:87], a[148:151], v[160:163], v[84:87]\n\t"
    "v_cvt_pk_bf16_f32 v168, v168, v169\n\t"
    "v_cvt_pk_bf16_f32 v169, v170, v171\n\t"
    "v_mfma_f32_16x16x32_bf16 v[88:91], a[152:155], v[160:163], v[88:91]\n\t"
    "v_cvt_pk_bf16_f32 v170, v172, v173\n\t"
    "v_cvt_pk_bf16_f32 v171, v174, v175\n\t"
    "v_mfma_f32_16x16x32_bf16 v[92:95], a[156:159], v[160:163], v[92:95]\n\t"
    "v_exp_f32 v144, v144\n\t"
    "v_mfma_f32_16x16x32_bf16 v[96:99], a[128:131], v[168:171], v[96:99]\n\t"
    "ds_read_b64_tr_b16 a[128:129], v226 offset:24576\n\t"
    "v_mfma_f32_16x16x32_bf16 v[100:103], a[132:135], v[168:171], v[100:103]\n\t"
    "ds_read_b64_tr_b16 a[130:131], v226 offset:28672\n\t"
    "v_mfma_f32_16x16x32_bf16 v[104:107], a[136:139], v[168:171], v[104:107]\n\t"
    "ds_read_b64_tr_b16 a[132:133], v225 offset:24576\n\t"
    "v_mfma_f32_16x16x32_bf16 v[108:111], a[140:143], v[168:171], v[108:111]\n\t"
    "ds_read_b64_tr_b16 a[134:135], v225 offset:28672\n\t"
    "v_mfma_f32_16x16x32_bf16 v[112:115], a[144:147], v[168:171], v[112:115]\n\t"
    "ds_read_b64_tr_b16 a[136:137], v226 offset:25088\n\t"
    "v_mfma_f32_16x16x32_bf16 v[116:119], a[148:151], v[168:171], v[116:119]\n\t"
    "ds_read_b64_tr_b16 a[138:139], v226 offset:29184\n\t"
    "v_mfma_f32_16x16x32_bf16 v[120:123], a[152:155], v[168:171], v[120:123]\n\t"
    "ds_read_b64_tr_b16 a[140:141], v225 offset:25088\n\t"
    "v_mfma_f32_16x16x32_bf16 v[124:127], a[156:159], v[168:171], v[124:127]\n\t"
    "ds_read_b64_tr_b16 a[142:143], v225 offset:29184\n\t"
    "v_mfma_f32_16x16x32_bf16 v[216:219], v[252:255], v[160:163], v[216:219]\n\t"
    "ds_read_b64_tr_b16 a[144:145], v226 offset:25600\n\t"
    "v_mfma_f32_16x16x32_bf16 v[220:223], v[252:255], v[168:171], v[220:223]\n\t"
    "s_waitcnt lgkmcnt(9)\n\t"
    "ds_read_b64_tr_b16 a[146:147], v226 offset:29696\n\t"
    "v_mfma_f32_16x16x32_bf16 v[128:131], a[96:99], a[64:67], v[192:195]\n\t"
    "ds_read_b64_tr_b16 a[148:149], v225 offset:25600\n\t"
    "v_mfma_f32_16x16x32_bf16 v[128:131], a[100:103], a[68:71], v[128:131]\n\t"
    "ds_read_b64_tr_b16 a[150:151], v225 offset:29696\n\t"
    "v_mfma_f32_16x16x32_bf16 v[128:131], a[104:107], a[72:75], v[128:131]\n\t"
    "ds_read_b64_tr_b16 a[152:153], v226 offset:26112\n\t"
    "v_mfma_f32_16x16x32_bf16 v[128:131], a[108:111], a[76:79], v[128:131]\n\t"
    "ds_read_b64_tr_b16 a[154:155], v226 offset:30208\n\t"
    "v_mfma_f32_16x16x32_bf16 v[132:135], a[112:115], a[64:67], v[192:195]\n\t"
    "ds_read_b64_tr_b16 a[156:157], v225 offset:26112\n\t"
    "v_mfma_f32_16x16x32_bf16 v[132:135], a[116:119], a[68:71], v[132:135]\n\t"
    "ds_read_b64_tr_b16 a[158:159], v225 offset:30208\n\t"
    "v_mfma_f32_16x16x32_bf16 v[132:135], a[120:123], a[72:75], v[132:135]\n\t"
    "v_exp_f32 v145, v145\n\t"
    "v_mfma_f32_16x16x32_bf16 v[132:135], a[124:127], a[76:79], v[132:135]\n\t"
    "v_exp_f32 v146, v146\n\t"
    "v_mfma_f32_16x16x32_bf16 v[136:139], a[96:99], a[80:83], v[196:199]\n\t"
    "v_exp_f32 v147, v147\n\t"
    "v_mfma_f32_16x16x32_bf16 v[136:139], a[100:103], a[84:87], v[136:139]\n\t"
    "v_exp_f32 v148, v148\n\t"
    "v_mfma_f32_16x16x32_bf16 v[136:139], a[104:107], a[88:91], v[136:139]\n\t"
    "v_exp_f32 v149, v149\n\t"
    "v_mfma_f32_16x16x32_bf16 v[136:139], a[108:111], a[92:95], v[136:139]\n\t"
    "v_exp_f32 v150, v150\n\t"
    "v_mfma_f32_16x16x32_bf16 v[140:143], a[112:115], a[80:83], v[196:199]\n\t"
    "v_exp_f32 v151, v151\n\t"
    "v_mfma_f32_16x16x32_bf16 v[140:143], a[116:119], a[84:87], v[140:143]\n\t"
    "v_cvt_pk_bf16_f32 v144, v144, v145\n\t"
    "v_cvt_pk_bf16_f32 v145, v146, v147\n\t"
    "v_mfma_f32_16x16x32_bf16 v[140:143], a[120:123], a[88:91], v[140:143]\n\t"
    "v_cvt_pk_bf16_f32 v146, v148, v149\n\t"
    "v_cvt_pk_bf16_f32 v147, v150, v151\n\t"
    "v_mfma_f32_16x16x32_bf16 v[140:143], a[124:127], a[92:95], v[140:143]\n\t"
    "s_waitcnt lgkmcnt(0)\n\t"
    "s_waitcnt vmcnt(4)\n\t"
    "s_barrier\n\t"
    "v_exp_f32 v152, v152\n\t"
    "v_mfma_f32_16x16x32_bf16 v[160:163], a[96:99], a[32:35], v[200:203]\n\t"
    "s_add_u32 m0, %8, 16384\n\t"
    "s_add_u32 s40, %9, 49152\n\t"
    "v_mfma_f32_16x16x32_bf16 v[160:163], a[100:103], a[36:39], v[160:163]\n\t"
    "v_exp_f32 v153, v153\n\t"
    "v_mfma_f32_16x16x32_bf16 v[160:163], a[104:107], a[40:43], v[160:163]\n\t"
    "v_exp_f32 v154, v154\n\t"
    "v_mfma_f32_16x16x32_bf16 v[160:163], a[108:111], a[44:47], v[160:163]\n\t"
    "v_exp_f32 v155, v155\n\t"
    "v_mfma_f32_16x16x32_bf16 v[164:167], a[112:115], a[32:35], v[200:203]\n\t"
    "v_exp_f32 v156, v156\n\t"
    "v_mfma_f32_16x16x32_bf16 v[164:167], a[116:119], a[36:39], v[164:167]\n\t"
    "v_exp_f32 v157, v157\n\t"
    "v_mfma_f32_16x16x32_bf16 v[164:167], a[120:123], a[40:43], v[164:167]\n\t"
    "v_exp_f32 v158, v158\n\t"
    "v_mfma_f32_16x16x32_bf16 v[164:167], a[124:127], a[44:47], v[164:167]\n\t"
    "v_exp_f32 v159, v159\n\t"
    "v_mfma_f32_16x16x32_bf16 v[168:171], a[96:99], a[48:51], v[204:207]\n\t"
    "ds_read_b128 a[96:99], v224 offset:24576\n\t"
    "v_cvt_pk_bf16_f32 v152, v152, v153\n\t"
    "v_mfma_f32_16x16x32_bf16 v[168:171], a[100:103], a[52:55], v[168:171]\n\t"
    "ds_read_b128 a[100:103], v224 offset:25600\n\t"
    "v_cvt_pk_bf16_f32 v153, v154, v155\n\t"
    "v_mfma_f32_16x16x32_bf16 v[168:171], a[104:107], a[56:59], v[168:171]\n\t"
    "buffer_load_dwordx4 v227, %3, s40 offen offset:0  lds\n\t"
    "v_mfma_f32_16x16x32_bf16 v[168:171], a[108:111], a[60:63], v[168:171]\n\t"
    "ds_read_b128 a[104:107], v224 offset:26624\n\t"
    "v_cvt_pk_bf16_f32 v154, v156, v157\n\t"
    "v_mfma_f32_16x16x32_bf16 v[172:175], a[112:115], a[48:51], v[204:207]\n\t"
    "ds_read_b128 a[108:111], v224 offset:27648\n\t"
    "v_cvt_pk_bf16_f32 v155, v158, v159\n\t"
    "v_mfma_f32_16x16x32_bf16 v[172:175], a[116:119], a[52:55], v[172:175]\n\t"
    "ds_read_b128 a[112:115], v224 offset:28672\n\t"
    "v_mfma_f32_16x16x32_bf16 v[172:175], a[120:123], a[56:59], v[172:175]\n\t"
    "ds_read_b128 a[116:119], v224 offset:29696\n\t"
    "v_mfma_f32_16x16x32_bf16 v[172:175], a[124:127], a[60:63], v[172:175]\n\t"
    "ds_read_b128 a[120:123], v224 offset:30720\n\t"
    "v_mfma_f32_16x16x32_bf16 v[0:3], a[128:131], v[144:147], v[0:3]\n\t"
    "buffer_load_dwordx4 v248, %3, s40 offen offset:1024  lds\n\t"
    "s_add_u32 m0, %8, 24576\n\t"
    "s_add_u32 s40, %9, 57344\n\t"
    "v_mfma_f32_16x16x32_bf16 v[4:7], a[132:135], v[144:147], v[4:7]\n\t"
    "ds_read_b128 a[124:127], v224 offset:31744\n\t"
    "v_mfma_f32_16x16x32_bf16 v[8:11], a[136:139], v[144:147], v[8:11]\n\t"
    "v_exp_f32 v176, v176\n\t"
    "v_mfma_f32_16x16x32_bf16 v[12:15], a[140:143], v[144:147], v[12:15]\n\t"
    "v_exp_f32 v177, v177\n\t"
    "v_mfma_f32_16x16x32_bf16 v[16:19], a[144:147], v[144:147], v[16:19]\n\t"
    "v_exp_f32 v178, v178\n\t"
    "v_mfma_f32_16x16x32_bf16 v[20:23], a[148:151], v[144:147], v[20:23]\n\t"
    "v_exp_f32 v179, v179\n\t"
    "v_mfma_f32_16x16x32_bf16 v[24:27], a[152:155], v[144:147], v[24:27]\n\t"
    "buffer_load_dwordx4 v227, %3, s40 offen offset:0  lds\n\t"
    "v_mfma_f32_16x16x32_bf16 v[28:31], a[156:159], v[144:147], v[28:31]\n\t"
    "v_exp_f32 v180, v180\n\t"
    "v_mfma_f32_16x16x32_bf16 v[32:35], a[128:131], v[152:155], v[32:35]\n\t"
    "v_exp_f32 v181, v181\n\t"
    "v_mfma_f32_16x16x32_bf16 v[36:39], a[132:135], v[152:155], v[36:39]\n\t"
    "v_exp_f32 v182, v182\n\t"
    "v_mfma_f32_16x16x32_bf16 v[40:43], a[136:139], v[152:155], v[40:43]\n\t"
    "v_exp_f32 v183, v183\n\t"
    "v_mfma_f32_16x16x32_bf16 v[44:47], a[140:143], v[152:155], v[44:47]\n\t"
    "v_cvt_pk_bf16_f32 v176, v176, v177\n\t"
    "v_cvt_pk_bf16_f32 v177, v178, v179\n\t"
    "v_mfma_f32_16x16x32_bf16 v[48:51], a[144:147], v[152:155], v[48:51]\n\t"
    "buffer_load_dwordx4 v248, %3, s40 offen offset:1024  lds\n\t"
    "v_mfma_f32_16x16x32_bf16 v[52:55], a[148:151], v[152:155], v[52:55]\n\t"
    "v_cvt_pk_bf16_f32 v178, v180, v181\n\t"
    "v_cvt_pk_bf16_f32 v179, v182, v183\n\t"
    "v_mfma_f32_16x16x32_bf16 v[56:59], a[152:155], v[152:155], v[56:59]\n\t"
    "v_exp_f32 v184, v184\n\t"
    "v_mfma_f32_16x16x32_bf16 v[60:63], a[156:159], v[152:155], v[60:63]\n\t"
    "v_exp_f32 v185, v185\n\t"
    "v_mfma_f32_16x16x32_bf16 v[208:211], v[252:255], v[144:147], v[208:211]\n\t"
    "ds_read_b128 a[8:11], v230 offset:4096\n\t"
    "v_cvt_pk_bf16_f32 v184, v184, v185\n\t"
    "v_mfma_f32_16x16x32_bf16 v[212:215], v[252:255], v[152:155], v[212:215]\n\t"
    "ds_read_b128 a[12:15], v231 offset:4096\n\t"
    "v_mfma_f32_16x16x32_bf16 v[64:67], a[128:131], v[176:179], v[64:67]\n\t"
    "ds_read_b128 a[24:27], v230 offset:6144\n\t"
    "v_mfma_f32_16x16x32_bf16 v[68:71], a[132:135], v[176:179], v[68:71]\n\t"
    "ds_read_b128 a[28:31], v231 offset:6144\n\t"
    "v_mfma_f32_16x16x32_bf16 v[72:75], a[136:139], v[176:179], v[72:75]\n\t"
    "v_exp_f32 v186, v186\n\t"
    "v_mfma_f32_16x16x32_bf16 v[76:79], a[140:143], v[176:179], v[76:79]\n\t"
    "v_exp_f32 v187, v187\n\t"
    "v_mfma_f32_16x16x32_bf16 v[80:83], a[144:147], v[176:179], v[80:83]\n\t"
    "v_exp_f32 v188, v188\n\t"
    "v_mfma_f32_16x16x32_bf16 v[84:87], a[148:151], v[176:179], v[84:87]\n\t"
    "v_exp_f32 v189, v189\n\t"
    "v_mfma_f32_16x16x32_bf16 v[88:91], a[152:155], v[176:179], v[88:91]\n\t"
    "v_exp_f32 v190, v190\n\t"
    "v_mfma_f32_16x16x32_bf16 v[92:95], a[156:159], v[176:179], v[92:95]\n\t"
    "v_exp_f32 v191, v191\n\t"
    "v_cvt_pk_bf16_f32 v185, v186, v187\n\t"
    "v_cvt_pk_bf16_f32 v186, v188, v189\n\t"
    "v_cvt_pk_bf16_f32 v187, v190, v191\n\t"
    "v_exp_f32 v128, v128\n\t"
    "s_nop 0\n\t"
    "v_mfma_f32_16x16x32_bf16 v[96:99], a[128:131], v[184:187], v[96:99]\n\t"
    "ds_read_b64_tr_b16 a[128:129], v226 offset:0\n\t"
    "v_mfma_f32_16x16x32_bf16 v[100:103], a[132:135], v[184:187], v[100:103]\n\t"
    "ds_read_b64_tr_b16 a[130:131], v226 offset:4096\n\t"
    "v_mfma_f32_16x16x32_bf16 v[104:107], a[136:139], v[184:187], v[104:107]\n\t"
    "s_add_u32 m0, %10, 0\n\t"
    "s_add_u32 s40, %11, 131072\n\t"
    "ds_read_b64_tr_b16 a[132:133], v225 offset:0\n\t"
    "v_mfma_f32_16x16x32_bf16 v[108:111], a[140:143], v[184:187], v[108:111]\n\t"
    "ds_read_b64_tr_b16 a[134:135], v225 offset:4096\n\t"
    "v_mfma_f32_16x16x32_bf16 v[112:115], a[144:147], v[184:187], v[112:115]\n\t"
    "ds_read_b64_tr_b16 a[136:137], v226 offset:512\n\t"
    "v_mfma_f32_16x16x32_bf16 v[116:119], a[148:151], v[184:187], v[116:119]\n\t"
    "ds_read_b64_tr_b16 a[138:139], v226 offset:4608\n\t"
    "v_mfma_f32_16x16x32_bf16 v[120:123], a[152:155], v[184:187], v[120:123]\n\t"
    "ds_read_b64_tr_b16 a[140:141], v225 offset:512\n\t"
    "v_mfma_f32_16x16x32_bf16 v[124:127], a[156:159], v[184:187], v[124:127]\n\t"
    "ds_read_b64_tr_b16 a[142:143], v225 offset:4608\n\t"
    "v_mfma_f32_16x16x32_bf16 v[216:219], v[252:255], v[176:179], v[216:219]\n\t"
    "ds_read_b64_tr_b16 a[144:145], v226 offset:1024\n\t"
    "v_mfma_f32_16x16x32_bf16 v[220:223], v[252:255], v[184:187], v[220:223]\n\t"
    "s_waitcnt lgkmcnt(9)\n\t"
    "ds_read_b64_tr_b16 a[146:147], v226 offset:5120\n\t"
    "v_mfma_f32_16x16x32_bf16 v[144:147], a[96:99], a[64:67], v[192:195]\n\t"
    "ds_read_b64_tr_b16 a[148:149], v225 offset:1024\n\t"
    "v_mfma_f32_16x16x32_bf16 v[144:147], a[100:103], a[68:71], v[144:147]\n\t"
    "ds_read_b64_tr_b16 a[150:151], v225 offset:5120\n\t"
    "v_mfma_f32_16x16x32_bf16 v[144:147], a[104:107], a[72:75], v[144:147]\n\t"
    "ds_read_b64_tr_b16 a[152:153], v226 offset:1536\n\t"
    "v_mfma_f32_16x16x32_bf16 v[144:147], a[108:111], a[76:79], v[144:147]\n\t"
    "ds_read_b64_tr_b16 a[154:155], v226 offset:5632\n\t"
    "v_mfma_f32_16x16x32_bf16 v[148:151], a[112:115], a[64:67], v[192:195]\n\t"
    "ds_read_b64_tr_b16 a[156:157], v225 offset:1536\n\t"
    "v_mfma_f32_16x16x32_bf16 v[148:151], a[116:119], a[68:71], v[148:151]\n\t"
    "ds_read_b64_tr_b16 a[158:159], v225 offset:5632\n\t"
    "v_mfma_f32_16x16x32_bf16 v[148:151], a[120:123], a[72:75], v[148:151]\n\t"
    "v_exp_f32 v129, v129\n\t"
    "v_mfma_f32_16x16x32_bf16 v[148:151], a[124:127], a[76:79], v[148:151]\n\t"
    "v_exp_f32 v130, v130\n\t"
    "v_mfma_f32_16x16x32_bf16 v[152:155], a[96:99], a[80:83], v[196:199]\n\t"
    "v_exp_f32 v131, v131\n\t"
    "v_mfma_f32_16x16x32_bf16 v[152:155], a[100:103], a[84:87], v[152:155]\n\t"
    "v_exp_f32 v132, v132\n\t"
    "v_mfma_f32_16x16x32_bf16 v[152:155], a[104:107], a[88:91], v[152:155]\n\t"
    "v_exp_f32 v133, v133\n\t"
    "v_mfma_f32_16x16x32_bf16 v[152:155], a[108:111], a[92:95], v[152:155]\n\t"
    "v_exp_f32 v134, v134\n\t"
    "v_mfma_f32_16x16x32_bf16 v[156:159], a[112:115], a[80:83], v[196:199]\n\t"
    "v_exp_f32 v135, v135\n\t"
    "v_mfma_f32_16x16x32_bf16 v[156:159], a[116:119], a[84:87], v[156:159]\n\t"
    "v_cvt_pk_bf16_f32 v128, v128, v129\n\t"
    "v_cvt_pk_bf16_f32 v129, v130, v131\n\t"
    "v_mfma_f32_16x16x32_bf16 v[156:159], a[120:123], a[88:91], v[156:159]\n\t"
    "v_cvt_pk_bf16_f32 v130, v132, v133\n\t"
    "v_cvt_pk_bf16_f32 v131, v134, v135\n\t"
    "v_mfma_f32_16x16x32_bf16 v[156:159], a[124:127], a[92:95], v[156:159]\n\t"
    "v_exp_f32 v136, v136\n\t"
    "v_mfma_f32_16x16x32_bf16 v[176:179], a[96:99], a[32:35], v[200:203]\n\t"
    "v_exp_f32 v137, v137\n\t"
    "v_mfma_f32_16x16x32_bf16 v[176:179], a[100:103], a[36:39], v[176:179]\n\t"
    "buffer_load_dwordx4 v228, %1, s40 offen offset:0 nt lds\n\t"
    "v_mfma_f32_16x16x32_bf16 v[176:179], a[104:107], a[40:43], v[176:179]\n\t"
    "v_exp_f32 v138, v138\n\t"
    "v_mfma_f32_16x16x32_bf16 v[176:179], a[108:111], a[44:47], v[176:179]\n\t"
    "v_exp_f32 v139, v139\n\t"
    "v_mfma_f32_16x16x32_bf16 v[180:183], a[112:115], a[32:35], v[200:203]\n\t"
    "v_exp_f32 v140, v140\n\t"
    "v_mfma_f32_16x16x32_bf16 v[180:183], a[116:119], a[36:39], v[180:183]\n\t"
    "v_exp_f32 v141, v141\n\t"
    "v_mfma_f32_16x16x32_bf16 v[180:183], a[120:123], a[40:43], v[180:183]\n\t"
    "v_exp_f32 v142, v142\n\t"
    "v_mfma_f32_16x16x32_bf16 v[180:183], a[124:127], a[44:47], v[180:183]\n\t"
    "v_exp_f32 v143, v143\n\t"
    "v_mfma_f32_16x16x32_bf16 v[184:187], a[96:99], a[48:51], v[204:207]\n\t"
    "ds_read_b128 a[96:99], v224 offset:32768\n\t"
    "buffer_load_dwordx4 v249, %1, s40 offen offset:1024 nt lds\n\t"
    "v_mfma_f32_16x16x32_bf16 v[184:187], a[100:103], a[52:55], v[184:187]\n\t"
    "ds_read_b128 a[100:103], v224 offset:33792\n\t"
    "v_cvt_pk_bf16_f32 v136, v136, v137\n\t"
    "v_mfma_f32_16x16x32_bf16 v[184:187], a[104:107], a[56:59], v[184:187]\n\t"
    "ds_read_b128 a[104:107], v224 offset:34816\n\t"
    "v_cvt_pk_bf16_f32 v137, v138, v139\n\t"
    "v_mfma_f32_16x16x32_bf16 v[184:187], a[108:111], a[60:63], v[184:187]\n\t"
    "ds_read_b128 a[108:111], v224 offset:35840\n\t"
    "v_cvt_pk_bf16_f32 v138, v140, v141\n\t"
    "v_mfma_f32_16x16x32_bf16 v[188:191], a[112:115], a[48:51], v[204:207]\n\t"
    "ds_read_b128 a[112:115], v224 offset:36864\n\t"
    "v_cvt_pk_bf16_f32 v139, v142, v143\n\t"
    "v_mfma_f32_16x16x32_bf16 v[188:191], a[116:119], a[52:55], v[188:191]\n\t"
    "ds_read_b128 a[116:119], v224 offset:37888\n\t"
    "v_mfma_f32_16x16x32_bf16 v[188:191], a[120:123], a[56:59], v[188:191]\n\t"
    "ds_read_b128 a[120:123], v224 offset:38912\n\t"
    "v_mfma_f32_16x16x32_bf16 v[188:191], a[124:127], a[60:63], v[188:191]\n\t"
    "s_waitcnt lgkmcnt(7)\n\t"
    "ds_read_b128 a[124:127], v224 offset:39936\n\t"
    "v_mfma_f32_16x16x32_bf16 v[0:3], a[128:131], v[128:131], v[0:3]\n\t"
    "v_exp_f32 v160, v160\n\t"
    "v_mfma_f32_16x16x32_bf16 v[4:7], a[132:135], v[128:131], v[4:7]\n\t"
    "buffer_load_dwordx4 v250, %1, s40 offen offset:2048 nt lds\n\t"
    "v_mfma_f32_16x16x32_bf16 v[8:11], a[136:139], v[128:131], v[8:11]\n\t"
    "v_exp_f32 v161, v161\n\t"
    "v_mfma_f32_16x16x32_bf16 v[12:15], a[140:143], v[128:131], v[12:15]\n\t"
    "v_exp_f32 v162, v162\n\t"
    "v_mfma_f32_16x16x32_bf16 v[16:19], a[144:147], v[128:131], v[16:19]\n\t"
    "v_exp_f32 v163, v163\n\t"
    "v_mfma_f32_16x16x32_bf16 v[20:23], a[148:151], v[128:131], v[20:23]\n\t"
    "v_exp_f32 v164, v164\n\t"
    "v_mfma_f32_16x16x32_bf16 v[24:27], a[152:155], v[128:131], v[24:27]\n\t"
    "v_exp_f32 v165, v165\n\t"
    "v_mfma_f32_16x16x32_bf16 v[28:31], a[156:159], v[128:131], v[28:31]\n\t"
    "v_exp_f32 v166, v166\n\t"
    "v_mfma_f32_16x16x32_bf16 v[32:35], a[128:131], v[136:139], v[32:35]\n\t"
    "v_exp_f32 v167, v167\n\t"
    "v_mfma_f32_16x16x32_bf16 v[36:39], a[132:135], v[136:139], v[36:39]\n\t"
    "buffer_load_dwordx4 v251, %1, s40 offen offset:3072 nt lds\n\t"
    "s_add_u32 s40, %11, 128\n\t"
    "v_mfma_f32_16x16x32_bf16 v[40:43], a[136:139], v[136:139], v[40:43]\n\t"
    "v_cvt_pk_bf16_f32 v160, v160, v161\n\t"
    "v_cvt_pk_bf16_f32 v161, v162, v163\n\t"
    "v_mfma_f32_16x16x32_bf16 v[44:47], a[140:143], v[136:139], v[44:47]\n\t"
    "v_cvt_pk_bf16_f32 v162, v164, v165\n\t"
    "v_cvt_pk_bf16_f32 v163, v166, v167\n\t"
    "v_mfma_f32_16x16x32_bf16 v[48:51], a[144:147], v[136:139], v[48:51]\n\t"
    "v_exp_f32 v168, v168\n\t"
    "v_mfma_f32_16x16x32_bf16 v[52:55], a[148:151], v[136:139], v[52:55]\n\t"
    "v_exp_f32 v169, v169\n\t"
    "v_mfma_f32_16x16x32_bf16 v[56:59], a[152:155], v[136:139], v[56:59]\n\t"
    "v_exp_f32 v170, v170\n\t"
    "v_mfma_f32_16x16x32_bf16 v[60:63], a[156:159], v[136:139], v[60:63]\n\t"
    "v_exp_f32 v171, v171\n\t"
    "v_mfma_f32_16x16x32_bf16 v[208:211], v[252:255], v[128:131], v[208:211]\n\t"
    "v_exp_f32 v172, v172\n\t"
    "v_mfma_f32_16x16x32_bf16 v[212:215], v[252:255], v[136:139], v[212:215]\n\t"
    "buffer_store_dwordx4 a[176:179], v236, %4, s40 offen offset:0 sc1\n\t"
    "v_mfma_f32_16x16x32_bf16 v[64:67], a[128:131], v[160:163], v[64:67]\n\t"
    "v_exp_f32 v173, v173\n\t"
    "v_mfma_f32_16x16x32_bf16 v[68:71], a[132:135], v[160:163], v[68:71]\n\t"
    "v_exp_f32 v174, v174\n\t"
    "v_mfma_f32_16x16x32_bf16 v[72:75], a[136:139], v[160:163], v[72:75]\n\t"
    "v_exp_f32 v175, v175\n\t"
    "v_mfma_f32_16x16x32_bf16 v[76:79], a[140:143], v[160:163], v[76:79]\n\t"
    "v_cvt_pk_bf16_f32 v168, v168, v169\n\t"
    "v_cvt_pk_bf16_f32 v169, v170, v171\n\t"
    "v_mfma_f32_16x16x32_bf16 v[80:83], a[144:147], v[160:163], v[80:83]\n\t"
    "v_cvt_pk_bf16_f32 v170, v172, v173\n\t"
    "v_cvt_pk_bf16_f32 v171, v174, v175\n\t"
    "v_mfma_f32_16x16x32_bf16 v[84:87], a[148:151], v[160:163], v[84:87]\n\t"
    "v_exp_f32 v144, v144\n\t"
    "v_mfma_f32_16x16x32_bf16 v[88:91], a[152:155], v[160:163], v[88:91]\n\t"
    "v_exp_f32 v145, v145\n\t"
    "v_mfma_f32_16x16x32_bf16 v[92:95], a[156:159], v[160:163], v[92:95]\n\t"
    "buffer_store_dwordx4 a[180:183], v236, %4, s40 offen offset:2048 sc1\n\t"
    "s_add_u32 s40, %11, 4224\n\t"
    "v_mfma_f32_16x16x32_bf16 v[96:99], a[128:131], v[168:171], v[96:99]\n\t"
    "ds_read_b64_tr_b16 a[128:129], v226 offset:8192\n\t"
    "v_cvt_pk_bf16_f32 v144, v144, v145\n\t"
    "v_mfma_f32_16x16x32_bf16 v[100:103], a[132:135], v[168:171], v[100:103]\n\t"
    "ds_read_b64_tr_b16 a[130:131], v226 offset:12288\n\t"
    "v_mfma_f32_16x16x32_bf16 v[104:107], a[136:139], v[168:171], v[104:107]\n\t"
    "ds_read_b64_tr_b16 a[132:133], v225 offset:8192\n\t"
    "v_mfma_f32_16x16x32_bf16 v[108:111], a[140:143], v[168:171], v[108:111]\n\t"
    "ds_read_b64_tr_b16 a[134:135], v225 offset:12288\n\t"
    "v_mfma_f32_16x16x32_bf16 v[112:115], a[144:147], v[168:171], v[112:115]\n\t"
    "ds_read_b64_tr_b16 a[136:137], v226 offset:8704\n\t"
    "v_mfma_f32_16x16x32_bf16 v[116:119], a[148:151], v[168:171], v[116:119]\n\t"
    "ds_read_b64_tr_b16 a[138:139], v226 offset:12800\n\t"
    "v_mfma_f32_16x16x32_bf16 v[120:123], a[152:155], v[168:171], v[120:123]\n\t"
    "ds_read_b64_tr_b16 a[140:141], v225 offset:8704\n\t"
    "buffer_store_dwordx4 a[184:187], v236, %4, s40 offen offset:0 sc1\n\t"
    "v_mfma_f32_16x16x32_bf16 v[124:127], a[156:159], v[168:171], v[124:127]\n\t"
    "ds_read_b64_tr_b16 a[142:143], v225 offset:12800\n\t"
    "v_mfma_f32_16x16x32_bf16 v[216:219], v[252:255], v[160:163], v[216:219]\n\t"
    "ds_read_b64_tr_b16 a[144:145], v226 offset:9216\n\t"
    "v_mfma_f32_16x16x32_bf16 v[220:223], v[252:255], v[168:171], v[220:223]\n\t"
    "s_waitcnt lgkmcnt(9)\n\t"
    "ds_read_b64_tr_b16 a[146:147], v226 offset:13312\n\t"
    "v_mfma_f32_16x16x32_bf16 v[128:131], a[96:99], a[64:67], v[192:195]\n\t"
    "ds_read_b64_tr_b16 a[148:149], v225 offset:9216\n\t"
    "v_mfma_f32_16x16x32_bf16 v[128:131], a[100:103], a[68:71], v[128:131]\n\t"
    "ds_read_b64_tr_b16 a[150:151], v225 offset:13312\n\t"
    "v_mfma_f32_16x16x32_bf16 v[128:131], a[104:107], a[72:75], v[128:131]\n\t"
    "ds_read_b64_tr_b16 a[152:153], v226 offset:9728\n\t"
    "v_mfma_f32_16x16x32_bf16 v[128:131], a[108:111], a[76:79], v[128:131]\n\t"
    "ds_read_b64_tr_b16 a[154:155], v226 offset:13824\n\t"
    "v_mfma_f32_16x16x32_bf16 v[132:135], a[112:115], a[64:67], v[192:195]\n\t"
    "ds_read_b64_tr_b16 a[156:157], v225 offset:9728\n\t"
    "buffer_store_dwordx4 a[188:191], v236, %4, s40 offen offset:2048 sc1\n\t"
    "v_mfma_f32_16x16x32_bf16 v[132:135], a[116:119], a[68:71], v[132:135]\n\t"
    "ds_read_b64_tr_b16 a[158:159], v225 offset:13824\n\t"
    "v_mfma_f32_16x16x32_bf16 v[132:135], a[120:123], a[72:75], v[132:135]\n\t"
    "v_exp_f32 v146, v146\n\t"
    "v_mfma_f32_16x16x32_bf16 v[132:135], a[124:127], a[76:79], v[132:135]\n\t"
    "v_exp_f32 v147, v147\n\t"
    "v_mfma_f32_16x16x32_bf16 v[136:139], a[96:99], a[80:83], v[196:199]\n\t"
    "v_exp_f32 v148, v148\n\t"
    "v_mfma_f32_16x16x32_bf16 v[136:139], a[100:103], a[84:87], v[136:139]\n\t"
    "v_exp_f32 v149, v149\n\t"
    "v_mfma_f32_16x16x32_bf16 v[136:139], a[104:107], a[88:91], v[136:139]\n\t"
    "v_exp_f32 v150, v150\n\t"
    "v_mfma_f32_16x16x32_bf16 v[136:139], a[108:111], a[92:95], v[136:139]\n\t"
    "v_exp_f32 v151, v151\n\t"
    "v_mfma_f32_16x16x32_bf16 v[140:143], a[112:115], a[80:83], v[196:199]\n\t"
    "v_cvt_pk_bf16_f32 v145, v146, v147\n\t"
    "v_cvt_pk_bf16_f32 v146, v148, v149\n\t"
    "v_mfma_f32_16x16x32_bf16 v[140:143], a[116:119], a[84:87], v[140:143]\n\t"
    "v_cvt_pk_bf16_f32 v147, v150, v151\n\t"
    "v_mfma_f32_16x16x32_bf16 v[140:143], a[120:123], a[88:91], v[140:143]\n\t"
    "v_exp_f32 v152, v152\n\t"
    "v_mfma_f32_16x16x32_bf16 v[140:143], a[124:127], a[92:95], v[140:143]\n\t"
    "s_waitcnt lgkmcnt(0)\n\t"
    "s_waitcnt vmcnt(8)\n\t"
    "s_barrier\n\t"
    "v_exp_f32 v153, v153\n\t"
    "v_mfma_f32_16x16x32_bf16 v[160:163], a[96:99], a[0:3], 0\n\t"
    "s_add_u32 m0, %8, 0\n\t"
    "s_add_u32 s40, %9, 65536\n\t"
    "v_cvt_pk_bf16_f32 v152, v152, v153\n\t"
    "v_mfma_f32_16x16x32_bf16 v[160:163], a[100:103], a[4:7], v[160:163]\n\t"
    "v_exp_f32 v154, v154\n\t"
    "v_mfma_f32_16x16x32_bf16 v[160:163], a[104:107], a[8:11], v[160:163]\n\t"
    "v_exp_f32 v155, v155\n\t"
    "v_mfma_f32_16x16x32_bf16 v[160:163], a[108:111], a[12:15], v[160:163]\n\t"
    "v_exp_f32 v156, v156\n\t"
    "v_mfma_f32_16x16x32_bf16 v[164:167], a[112:115], a[0:3], 0\n\t"
    "v_exp_f32 v157, v157\n\t"
    "v_mfma_f32_16x16x32_bf16 v[164:167], a[116:119], a[4:7], v[164:167]\n\t"
    "v_exp_f32 v158, v158\n\t"
    "v_mfma_f32_16x16x32_bf16 v[164:167], a[120:123], a[8:11], v[164:167]\n\t"
    "v_exp_f32 v159, v159\n\t"
    "v_mfma_f32_16x16x32_bf16 v[164:167], a[124:127], a[12:15], v[164:167]\n\t"
    "v_cvt_pk_bf16_f32 v153, v154, v155\n\t"
    "v_cvt_pk_bf16_f32 v154, v156, v157\n\t"
    "v_mfma_f32_16x16x32_bf16 v[168:171], a[96:99], a[16:19], 0\n\t"
    "ds_read_b128 a[96:99], v224 offset:40960\n\t"
    "v_cvt_pk_bf16_f32 v155, v158, v159\n\t"
    "v_mfma_f32_16x16x32_bf16 v[168:171], a[100:103], a[20:23], v[168:171]\n\t"
    "ds_read_b128 a[100:103], v224 offset:41984\n\t"
    "v_max3_f32 v229, v160, v161, v162\n\t"
    "v_mfma_f32_16x16x32_bf16 v[168:171], a[104:107], a[24:27], v[168:171]\n\t"
    "buffer_load_dwordx4 v227, %3, s40 offen offset:0  lds\n\t"
    "v_mfma_f32_16x16x32_bf16 v[168:171], a[108:111], a[28:31], v[168:171]\n\t"
    "ds_read_b128 a[104:107], v224 offset:43008\n\t"
    "v_max3_f32 v229, v229, v166, v167\n\t"
    "v_mfma_f32_16x16x32_bf16 v[172:175], a[112:115], a[16:19], 0\n\t"
    "ds_read_b128 a[108:111], v224 offset:44032\n\t"
    "v_mfma_f32_16x16x32_bf16 v[172:175], a[116:119], a[20:23], v[172:175]\n\t"
    "ds_read_b128 a[112:115], v224 offset:45056\n\t"
    "v_mfma_f32_16x16x32_bf16 v[172:175], a[120:123], a[24:27], v[172:175]\n\t"
    "ds_read_b128 a[116:119], v224 offset:46080\n\t"
    "v_max3_f32 v233, v168, v169, v170\n\t"
    "v_mfma_f32_16x16x32_bf16 v[172:175], a[124:127], a[28:31], v[172:175]\n\t"
    "ds_read_b128 a[120:123], v224 offset:47104\n\t"
    "v_mfma_f32_16x16x32_bf16 v[0:3], a[128:131], v[144:147], v[0:3]\n\t"
    "buffer_load_dwordx4 v248, %3, s40 offen offset:1024  lds\n\t"
    "s_add_u32 m0, %8, 8192\n\t"
    "s_add_u32 s40, %9, 73728\n\t"
    "v_mfma_f32_16x16x32_bf16 v[4:7], a[132:135], v[144:147], v[4:7]\n\t"
    "ds_read_b128 a[124:127], v224 offset:48128\n\t"
    "v_mfma_f32_16x16x32_bf16 v[8:11], a[136:139], v[144:147], v[8:11]\n\t"
    "v_exp_f32 v176, v176\n\t"
    "v_mfma_f32_16x16x32_bf16 v[12:15], a[140:143], v[144:147], v[12:15]\n\t"
    "v_exp_f32 v177, v177\n\t"
    "v_mfma_f32_16x16x32_bf16 v[16:19], a[144:147], v[144:147], v[16:19]\n\t"
    "v_exp_f32 v178, v178\n\t"
    "v_mfma_f32_16x16x32_bf16 v[20:23], a[148:151], v[144:147], v[20:23]\n\t"
    "v_exp_f32 v179, v179\n\t"
    "v_mfma_f32_16x16x32_bf16 v[24:27], a[152:155], v[144:147], v[24:27]\n\t"
    "buffer_load_dwordx4 v227, %3, s40 offen offset:0  lds\n\t"
    "v_mfma_f32_16x16x32_bf16 v[28:31], a[156:159], v[144:147], v[28:31]\n\t"
    "v_exp_f32 v180, v180\n\t"
    "v_mfma_f32_16x16x32_bf16 v[32:35], a[128:131], v[152:155], v[32:35]\n\t"
    "v_exp_f32 v181, v181\n\t"
    "v_mfma_f32_16x16x32_bf16 v[36:39], a[132:135], v[152:155], v[36:39]\n\t"
    "v_exp_f32 v182, v182\n\t"
    "v_mfma_f32_16x16x32_bf16 v[40:43], a[136:139], v[152:155], v[40:43]\n\t"
    "v_exp_f32 v183, v183\n\t"
    "v_mfma_f32_16x16x32_bf16 v[44:47], a[140:143], v[152:155], v[44:47]\n\t"
    "v_cvt_pk_bf16_f32 v176, v176, v177\n\t"
    "v_cvt_pk_bf16_f32 v177, v178, v179\n\t"
    "v_mfma_f32_16x16x32_bf16 v[48:51], a[144:147], v[152:155], v[48:51]\n\t"
    "buffer_load_dwordx4 v248, %3, s40 offen offset:1024  lds\n\t"
    "v_mfma_f32_16x16x32_bf16 v[52:55], a[148:151], v[152:155], v[52:55]\n\t"
    "v_exp_f32 v184, v184\n\t"
    "v_mfma_f32_16x16x32_bf16 v[56:59], a[152:155], v[152:155], v[56:59]\n\t"
    "v_exp_f32 v185, v185\n\t"
    "v_mfma_f32_16x16x32_bf16 v[60:63], a[156:159], v[152:155], v[60:63]\n\t"
    "v_cvt_pk_bf16_f32 v178, v180, v181\n\t"
    "v_cvt_pk_bf16_f32 v179, v182, v183\n\t"
    "v_mfma_f32_16x16x32_bf16 v[208:211], v[252:255], v[144:147], v[208:211]\n\t"
    "v_exp_f32 v186, v186\n\t"
    "v_mfma_f32_16x16x32_bf16 v[212:215], v[252:255], v[152:155], v[212:215]\n\t"
    "v_exp_f32 v187, v187\n\t"
    "v_mfma_f32_16x16x32_bf16 v[216:219], v[252:255], v[176:179], v[216:219]\n\t"
    "v_exp_f32 v188, v188\n\t"
    "v_exp_f32 v189, v189\n\t"
    "v_exp_f32 v190, v190\n\t"
    "v_exp_f32 v191, v191\n\t"
    "v_cvt_pk_bf16_f32 v184, v184, v185\n\t"
    "v_cvt_pk_bf16_f32 v185, v186, v187\n\t"
    "v_cvt_pk_bf16_f32 v186, v188, v189\n\t"
    "v_cvt_pk_bf16_f32 v187, v190, v191\n\t"
    "v_max3_f32 v241, v171, v172, v173\n\t"
    "v_max3_f32 v233, v233, v174, v175\n\t"
    "v_mfma_f32_16x16x32_bf16 v[220:223], v[252:255], v[184:187], v[220:223]\n\t"
    "v_max_f32 v233, v233, v241\n\t"
    "v_mov_b32 v241, v233\n\t"
    "v_mfma_f32_16x16x32_bf16 v[64:67], a[128:131], v[176:179], v[64:67]\n\t"
    "v_exp_f32 v128, v128\n\t"
    "v_mfma_f32_16x16x32_bf16 v[68:71], a[132:135], v[176:179], v[68:71]\n\t"
    "v_permlane16_swap_b32 v233, v241\n\t"
    "v_max_f32 v233, v233, v241\n\t"
    "v_mfma_f32_16x16x32_bf16 v[72:75], a[136:139], v[176:179], v[72:75]\n\t"
    "v_mov_b32 v241, v233\n\t"
    "v_mfma_f32_16x16x32_bf16 v[76:79], a[140:143], v[176:179], v[76:79]\n\t"
    "v_exp_f32 v129, v129\n\t"
    "v_mfma_f32_16x16x32_bf16 v[80:83], a[144:147], v[176:179], v[80:83]\n\t"
    "v_permlane32_swap_b32 v233, v241\n\t"
    "v_min_f32_e64 v204, -v233, -v241\n\t"
    "v_mfma_f32_16x16x32_bf16 v[84:87], a[148:151], v[176:179], v[84:87]\n\t"
    "v_min_f32_e64 v205, -v233, -v241\n\t"
    "v_min_f32_e64 v206, -v233, -v241\n\t"
    "v_mfma_f32_16x16x32_bf16 v[88:91], a[152:155], v[176:179], v[88:91]\n\t"
    "v_min_f32_e64 v207, -v233, -v241\n\t"
    "v_mov_b32 v233, v220\n\t"
    "v_mfma_f32_16x16x32_bf16 v[92:95], a[156:159], v[176:179], v[92:95]\n\t"
    "v_rcp_f32 v243, v233\n\t"
    "v_mfma_f32_16x16x32_bf16 v[96:99], a[128:131], v[184:187], v[96:99]\n\t"
    "s_add_u32 m0, %10, 4096\n\t"
    "s_add_u32 s40, %11, 131200\n\t"
    "ds_read_b64_tr_b16 a[128:129], v226 offset:16384\n\t"
    "v_mfma_f32_16x16x32_bf16 v[100:103], a[132:135], v[184:187], v[100:103]\n\t"
    "ds_read_b64_tr_b16 a[130:131], v226 offset:20480\n\t"
    "v_cvt_pk_bf16_f32 v128, v128, v129\n\t"
    "v_mfma_f32_16x16x32_bf16 v[104:107], a[136:139], v[184:187], v[104:107]\n\t"
    "ds_read_b64_tr_b16 a[132:133], v225 offset:16384\n\t"
    "v_add_f32 v168, v204, v168\n\t"
    "v_mfma_f32_16x16x32_bf16 v[108:111], a[140:143], v[184:187], v[108:111]\n\t"
    "ds_read_b64_tr_b16 a[134:135], v225 offset:20480\n\t"
    "v_mul_f32 v96, v243, v96\n\t"
    "v_mfma_f32_16x16x32_bf16 v[112:115], a[144:147], v[184:187], v[112:115]\n\t"
    "ds_read_b64_tr_b16 a[136:137], v226 offset:16896\n\t"
    "v_mul_f32 v97, v243, v97\n\t"
    "v_mfma_f32_16x16x32_bf16 v[116:119], a[148:151], v[184:187], v[116:119]\n\t"
    "ds_read_b64_tr_b16 a[138:139], v226 offset:20992\n\t"
    "v_mul_f32 v98, v243, v98\n\t"
    "v_mfma_f32_16x16x32_bf16 v[120:123], a[152:155], v[184:187], v[120:123]\n\t"
    "ds_read_b64_tr_b16 a[140:141], v225 offset:16896\n\t"
    "v_mul_f32 v99, v243, v99\n\t"
    "v_mfma_f32_16x16x32_bf16 v[124:127], a[156:159], v[184:187], v[124:127]\n\t"
    "s_waitcnt lgkmcnt(7)\n\t"
    "ds_read_b64_tr_b16 a[142:143], v225 offset:20992\n\t"
    "v_cvt_pk_bf16_f32 v96, v96, v97\n\t"
    "v_mfma_f32_16x16x32_bf16 v[144:147], a[96:99], a[64:67], v[192:195]\n\t"
    "ds_read_b64_tr_b16 a[144:145], v226 offset:17408\n\t"
    "v_cvt_pk_bf16_f32 v97, v98, v99\n\t"
    "v_mfma_f32_16x16x32_bf16 v[144:147], a[100:103], a[68:71], v[144:147]\n\t"
    "ds_read_b64_tr_b16 a[146:147], v226 offset:21504\n\t"
    "v_mul_f32 v100, v243, v100\n\t"
    "v_mfma_f32_16x16x32_bf16 v[144:147], a[104:107], a[72:75], v[144:147]\n\t"
    "ds_read_b64_tr_b16 a[148:149], v225 offset:17408\n\t"
    "v_mul_f32 v101, v243, v101\n\t"
    "v_mfma_f32_16x16x32_bf16 v[144:147], a[108:111], a[76:79], v[144:147]\n\t"
    "ds_read_b64_tr_b16 a[150:151], v225 offset:21504\n\t"
    "v_mul_f32 v102, v243, v102\n\t"
    "v_mfma_f32_16x16x32_bf16 v[148:151], a[112:115], a[64:67], v[192:195]\n\t"
    "ds_read_b64_tr_b16 a[152:153], v226 offset:17920\n\t"
    "v_mul_f32 v103, v243, v103\n\t"
    "v_mfma_f32_16x16x32_bf16 v[148:151], a[116:119], a[68:71], v[148:151]\n\t"
    "ds_read_b64_tr_b16 a[154:155], v226 offset:22016\n\t"
    "v_cvt_pk_bf16_f32 v100, v100, v101\n\t"
    "v_mfma_f32_16x16x32_bf16 v[148:151], a[120:123], a[72:75], v[148:151]\n\t"
    "ds_read_b64_tr_b16 a[156:157], v225 offset:17920\n\t"
    "v_cvt_pk_bf16_f32 v101, v102, v103\n\t"
    "v_mfma_f32_16x16x32_bf16 v[148:151], a[124:127], a[76:79], v[148:151]\n\t"
    "ds_read_b64_tr_b16 a[158:159], v225 offset:22016\n\t"
    "v_mul_f32 v104, v243, v104\n\t"
    "v_mfma_f32_16x16x32_bf16 v[152:155], a[96:99], a[80:83], v[196:199]\n\t"
    "v_mul_f32 v105, v243, v105\n\t"
    "v_mul_f32 v106, v243, v106\n\t"
    "v_mfma_f32_16x16x32_bf16 v[152:155], a[100:103], a[84:87], v[152:155]\n\t"
    "v_mul_f32 v107, v243, v107\n\t"
    "v_cvt_pk_bf16_f32 v104, v104, v105\n\t"
    "v_mfma_f32_16x16x32_bf16 v[152:155], a[104:107], a[88:91], v[152:155]\n\t"
    "v_cvt_pk_bf16_f32 v105, v106, v107\n\t"
    "v_mul_f32 v108, v243, v108\n\t"
    "v_mfma_f32_16x16x32_bf16 v[152:155], a[108:111], a[92:95], v[152:155]\n\t"
    "v_mul_f32 v109, v243, v109\n\t"
    "v_mul_f32 v110, v243, v110\n\t"
    "v_mfma_f32_16x16x32_bf16 v[156:159], a[112:115], a[80:83], v[196:199]\n\t"
    "v_mul_f32 v111, v243, v111\n\t"
    "v_cvt_pk_bf16_f32 v108, v108, v109\n\t"
    "v_mfma_f32_16x16x32_bf16 v[156:159], a[116:119], a[84:87], v[156:159]\n\t"
    "v_cvt_pk_bf16_f32 v109, v110, v111\n\t"
    "v_mul_f32 v112, v243, v112\n\t"
    "v_mfma_f32_16x16x32_bf16 v[156:159], a[120:123], a[88:91], v[156:159]\n\t"
    "v_exp_f32 v130, v130\n\t"
    "v_mfma_f32_16x16x32_bf16 v[156:159], a[124:127], a[92:95], v[156:159]\n\t"
    "buffer_load_dwordx4 v228, %1, s40 offen offset:0 nt lds\n\t"
    "buffer_load_dwordx4 v249, %1, s40 offen offset:1024 nt lds\n\t"
    "buffer_load_dwordx4 v250, %1, s40 offen offset:2048 nt lds\n\t"
    "v_cmp_ngt_f32 vcc, s44, v232\n\t"
    "s_cmp_lg_u64 vcc, 0\n\t"
    "s_cselect_b32 s45, 1, 0\n\t"
    "s_or_b32 s41, s41, s45\n\t"
    "v_max3_f32 v232, v163, v164, v165\n\t"
    "v_max_f32 v229, v229, v232\n\t"
    "v_mov_b32 v232, v229\n\t"
    "v_exp_f32 v131, v131\n\t"
    "v_exp_f32 v132, v132\n\t"
    "v_permlane16_swap_b32 v229, v232\n\t"
    "v_max_f32 v229, v229, v232\n\t"
    "v_mov_b32 v232, v229\n\t"
    "v_exp_f32 v133, v133\n\t"
    "v_exp_f32 v134, v134\n\t"
    "v_permlane32_swap_b32 v229, v232\n\t"
    "v_min_f32_e64 v200, -v229, -v232\n\t"
    "v_min_f32_e64 v201, -v229, -v232\n\t"
    "v_min_f32_e64 v202, -v229, -v232\n\t"
    "v_min_f32_e64 v203, -v229, -v232\n\t"
    "v_mov_b32 v229, v216\n\t"
    "v_cvt_pk_bf16_f32 v129, v130, v131\n\t"
    "v_mfma_f32_16x16x32_bf16 v[176:179], a[96:99], a[0:3], v[200:203]\n\t"
    "v_rcp_f32 v242, v229\n\t"
    "v_mfma_f32_16x16x32_bf16 v[176:179], a[100:103], a[4:7], v[176:179]\n\t"
    "v_mul_f32 v64, v242, v64\n\t"
    "v_mul_f32 v65, v242, v65\n\t"
    "v_mfma_f32_16x16x32_bf16 v[176:179], a[104:107], a[8:11], v[176:179]\n\t"
    "v_mul_f32 v66, v242, v66\n\t"
    "v_mul_f32 v67, v242, v67\n\t"
    "v_mfma_f32_16x16x32_bf16 v[176:179], a[108:111], a[12:15], v[176:179]\n\t"
    "v_cvt_pk_bf16_f32 v64, v64, v65\n\t"
    "v_cvt_pk_bf16_f32 v65, v66, v67\n\t"
    "ds_write_b64 v234, v[64:65] offset:0\n\t"
    "v_mfma_f32_16x16x32_bf16 v[180:183], a[112:115], a[0:3], v[200:203]\n\t"
    "v_mul_f32 v68, v242, v68\n\t"
    "v_mul_f32 v69, v242, v69\n\t"
    "v_mfma_f32_16x16x32_bf16 v[180:183], a[116:119], a[4:7], v[180:183]\n\t"
    "ds_write_b64 v234, v[96:97] offset:2048\n\t"
    "v_mfma_f32_16x16x32_bf16 v[180:183], a[120:123], a[8:11], v[180:183]\n\t"
    "v_mul_f32 v70, v242, v70\n\t"
    "v_mul_f32 v71, v242, v71\n\t"
    "v_mfma_f32_16x16x32_bf16 v[180:183], a[124:127], a[12:15], v[180:183]\n\t"
    "v_cvt_pk_bf16_f32 v68, v68, v69\n\t"
    "v_cvt_pk_bf16_f32 v69, v70, v71\n\t"
    "ds_write_b64 v237, v[68:69] offset:0\n\t"
    "v_mfma_f32_16x16x32_bf16 v[184:187], a[96:99], a[16:19], v[204:207]\n\t"
    "ds_read_b128 a[96:99], v224 offset:49152\n\t"
    "v_mul_f32 v72, v242, v72\n\t"
    "v_mfma_f32_16x16x32_bf16 v[184:187], a[100:103], a[20:23], v[184:187]\n\t"
    "ds_write_b64 v237, v[100:101] offset:2048\n\t"
    "v_mfma_f32_16x16x32_bf16 v[184:187], a[104:107], a[24:27], v[184:187]\n\t"
    "ds_read_b128 a[100:103], v224 offset:50176\n\t"
    "v_mul_f32 v73, v242, v73\n\t"
    "v_mfma_f32_16x16x32_bf16 v[184:187], a[108:111], a[28:31], v[184:187]\n\t"
    "ds_read_b128 a[104:107], v224 offset:51200\n\t"
    "v_mul_f32 v74, v242, v74\n\t"
    "v_mfma_f32_16x16x32_bf16 v[188:191], a[112:115], a[16:19], v[204:207]\n\t"
    "ds_read_b128 a[108:111], v224 offset:52224\n\t"
    "v_mul_f32 v75, v242, v75\n\t"
    "v_mfma_f32_16x16x32_bf16 v[188:191], a[116:119], a[20:23], v[188:191]\n\t"
    "ds_read_b128 a[112:115], v224 offset:53248\n\t"
    "v_cvt_pk_bf16_f32 v72, v72, v73\n\t"
    "v_mfma_f32_16x16x32_bf16 v[188:191], a[120:123], a[24:27], v[188:191]\n\t"
    "ds_read_b128 a[116:119], v224 offset:54272\n\t"
    "v_cvt_pk_bf16_f32 v73, v74, v75\n\t"
    "ds_write_b64 v246, v[72:73] offset:0\n\t"
    "v_mfma_f32_16x16x32_bf16 v[188:191], a[124:127], a[28:31], v[188:191]\n\t"
    "s_waitcnt lgkmcnt(10)\n\t"
    "ds_write_b64 v246, v[104:105] offset:2048\n\t"
    "v_mul_f32 v76, v242, v76\n\t"
    "v_mul_f32 v77, v242, v77\n\t"
    "v_mul_f32 v78, v242, v78\n\t"
    "v_mul_f32 v79, v242, v79\n\t"
    "v_cvt_pk_bf16_f32 v76, v76, v77\n\t"
    "v_cvt_pk_bf16_f32 v77, v78, v79\n\t"
    "ds_write_b64 v247, v[76:77] offset:0\n\t"
    "ds_write_b64 v247, v[108:109] offset:2048\n\t"
    "v_exp_f32 v135, v135\n\t"
    "ds_read_b128 a[160:163], v235 offset:0\n\t"
    "v_cvt_pk_bf16_f32 v130, v132, v133\n\t"
    "v_cvt_pk_bf16_f32 v131, v134, v135\n\t"
    "v_exp_f32 v136, v136\n\t"
    "s_nop 0\n\t"
    "v_mfma_f32_16x16x32_bf16 v[0:3], a[128:131], v[128:131], v[0:3]\n\t"
    "ds_read_b128 a[120:123], v224 offset:55296\n\t"
    "v_mul_f32 v80, v242, v80\n\t"
    "v_mfma_f32_16x16x32_bf16 v[4:7], a[132:135], v[128:131], v[4:7]\n\t"
    "ds_read_b128 a[124:127], v224 offset:56320\n\t"
    "v_mul_f32 v81, v242, v81\n\t"
    "v_mfma_f32_16x16x32_bf16 v[8:11], a[136:139], v[128:131], v[8:11]\n\t"
    "v_exp_f32 v137, v137\n\t"
    "v_mfma_f32_16x16x32_bf16 v[12:15], a[140:143], v[128:131], v[12:15]\n\t"
    "ds_read2_b64 a[164:167], v235 offset0:129 offset1:128\n\t"
    "v_cvt_pk_bf16_f32 v136, v136, v137\n\t"
    "v_mfma_f32_16x16x32_bf16 v[16:19], a[144:147], v[128:131], v[16:19]\n\t"
    "v_exp_f32 v138, v138\n\t"
    "v_mfma_f32_16x16x32_bf16 v[20:23], a[148:151], v[128:131], v[20:23]\n\t"
    "ds_read_b128 a[168:171], v235 offset:2048\n\t"
    "v_mul_f32 v82, v242, v82\n\t"
    "v_mfma_f32_16x16x32_bf16 v[24:27], a[152:155], v[128:131], v[24:27]\n\t"
    "v_exp_f32 v139, v139\n\t"
    "v_mfma_f32_16x16x32_bf16 v[28:31], a[156:159], v[128:131], v[28:31]\n\t"
    "buffer_load_dwordx4 v251, %1, s40 offen offset:3072 nt lds\n\t"
    "ds_read2_b64 a[172:175], v245 offset0:129 offset1:128\n\t"
    "v_exp_f32 v140, v140\n\t"
    "v_exp_f32 v141, v141\n\t"
    "v_exp_f32 v142, v142\n\t"
    "v_exp_f32 v143, v143\n\t"
    "v_cvt_pk_bf16_f32 v137, v138, v139\n\t"
    "v_cvt_pk_bf16_f32 v138, v140, v141\n\t"
    "v_cvt_pk_bf16_f32 v139, v142, v143\n\t"
    "v_mul_f32 v83, v242, v83\n\t"
    "v_cvt_pk_bf16_f32 v80, v80, v81\n\t"
    "v_mfma_f32_16x16x32_bf16 v[32:35], a[128:131], v[136:139], v[32:35]\n\t"
    "v_cvt_pk_bf16_f32 v81, v82, v83\n\t"
    "v_mul_f32 v113, v243, v113\n\t"
    "v_mfma_f32_16x16x32_bf16 v[36:39], a[132:135], v[136:139], v[36:39]\n\t"
    "v_mul_f32 v114, v243, v114\n\t"
    "v_mul_f32 v115, v243, v115\n\t"
    "v_mfma_f32_16x16x32_bf16 v[40:43], a[136:139], v[136:139], v[40:43]\n\t"
    "v_cvt_pk_bf16_f32 v112, v112, v113\n\t"
    "v_cvt_pk_bf16_f32 v113, v114, v115\n\t"
    "v_mfma_f32_16x16x32_bf16 v[44:47], a[140:143], v[136:139], v[44:47]\n\t"
    "v_mul_f32 v84, v242, v84\n\t"
    "v_mul_f32 v85, v242, v85\n\t"
    "v_mfma_f32_16x16x32_bf16 v[48:51], a[144:147], v[136:139], v[48:51]\n\t"
    "v_mul_f32 v86, v242, v86\n\t"
    "v_mul_f32 v87, v242, v87\n\t"
    "v_mfma_f32_16x16x32_bf16 v[52:55], a[148:151], v[136:139], v[52:55]\n\t"
    "ds_write_b64 v234, v[80:81] offset:0\n\t"
    "v_mfma_f32_16x16x32_bf16 v[56:59], a[152:155], v[136:139], v[56:59]\n\t"
    "v_cvt_pk_bf16_f32 v84, v84, v85\n\t"
    "v_cvt_pk_bf16_f32 v85, v86, v87\n\t"
    "v_mfma_f32_16x16x32_bf16 v[60:63], a[156:159], v[136:139], v[60:63]\n\t"
    "ds_write_b64 v234, v[112:113] offset:2048\n\t"
    "v_mfma_f32_16x16x32_bf16 v[208:211], v[252:255], v[128:131], v[208:211]\n\t"
    "v_mul_f32 v116, v243, v116\n\t"
    "v_mul_f32 v117, v243, v117\n\t"
    "v_mfma_f32_16x16x32_bf16 v[212:215], v[252:255], v[136:139], v[212:215]\n\t"
    "s_waitcnt lgkmcnt(3)\n\t"
    "ds_write_b64 v237, v[84:85] offset:0\n\t"
    "v_mfma_f32_16x16x32_bf16 v[128:131], a[96:99], a[64:67], v[192:195]\n\t"
    "v_mul_f32 v118, v243, v118\n\t"
    "v_mul_f32 v119, v243, v119\n\t"
    "v_mfma_f32_16x16x32_bf16 v[128:131], a[100:103], a[68:71], v[128:131]\n\t"
    "v_cvt_pk_bf16_f32 v116, v116, v117\n\t"
    "v_cvt_pk_bf16_f32 v117, v118, v119\n\t"
    "ds_write_b64 v237, v[116:117] offset:2048\n\t"
    "v_mfma_f32_16x16x32_bf16 v[128:131], a[104:107], a[72:75], v[128:131]\n\t"
    "v_mul_f32 v88, v242, v88\n\t"
    "v_mul_f32 v89, v242, v89\n\t"
    "v_mfma_f32_16x16x32_bf16 v[128:131], a[108:111], a[76:79], v[128:131]\n\t"
    "v_mul_f32 v90, v242, v90\n\t"
    "v_mul_f32 v91, v242, v91\n\t"
    "v_mfma_f32_16x16x32_bf16 v[132:135], a[112:115], a[64:67], v[192:195]\n\t"
    "v_cvt_pk_bf16_f32 v88, v88, v89\n\t"
    "v_cvt_pk_bf16_f32 v89, v90, v91\n\t"
    "ds_write_b64 v246, v[88:89] offset:0\n\t"
    "v_mfma_f32_16x16x32_bf16 v[132:135], a[116:119], a[68:71], v[132:135]\n\t"
    "v_mul_f32 v120, v243, v120\n\t"
    "v_mul_f32 v121, v243, v121\n\t"
    "v_mfma_f32_16x16x32_bf16 v[132:135], a[120:123], a[72:75], v[132:135]\n\t"
    "v_mul_f32 v122, v243, v122\n\t"
    "v_mul_f32 v123, v243, v123\n\t"
    "v_mfma_f32_16x16x32_bf16 v[132:135], a[124:127], a[76:79], v[132:135]\n\t"
    "v_cvt_pk_bf16_f32 v120, v120, v121\n\t"
    "v_cvt_pk_bf16_f32 v121, v122, v123\n\t"
    "ds_write_b64 v246, v[120:121] offset:2048\n\t"
    "v_mfma_f32_16x16x32_bf16 v[136:139], a[96:99], a[80:83], v[196:199]\n\t"
    "v_mul_f32 v92, v242, v92\n\t"
    "v_mul_f32 v93, v242, v93\n\t"
    "v_mfma_f32_16x16x32_bf16 v[136:139], a[100:103], a[84:87], v[136:139]\n\t"
    "v_mul_f32 v94, v242, v94\n\t"
    "v_mul_f32 v95, v242, v95\n\t"
    "v_mfma_f32_16x16x32_bf16 v[136:139], a[104:107], a[88:91], v[136:139]\n\t"
    "v_cvt_pk_bf16_f32 v92, v92, v93\n\t"
    "v_cvt_pk_bf16_f32 v93, v94, v95\n\t"
    "ds_write_b64 v247, v[92:93] offset:0\n\t"
    "v_mfma_f32_16x16x32_bf16 v[136:139], a[108:111], a[92:95], v[136:139]\n\t"
    "v_mul_f32 v124, v243, v124\n\t"
    "v_mul_f32 v125, v243, v125\n\t"
    "v_mfma_f32_16x16x32_bf16 v[140:143], a[112:115], a[80:83], v[196:199]\n\t"
    "v_mul_f32 v126, v243, v126\n\t"
    "v_mul_f32 v127, v243, v127\n\t"
    "v_mfma_f32_16x16x32_bf16 v[140:143], a[116:119], a[84:87], v[140:143]\n\t"
    "v_cvt_pk_bf16_f32 v124, v124, v125\n\t"
    "v_cvt_pk_bf16_f32 v125, v126, v127\n\t"
    "ds_write_b64 v247, v[124:125] offset:2048\n\t"
    "v_mfma_f32_16x16x32_bf16 v[140:143], a[120:123], a[88:91], v[140:143]\n\t"
    "v_add_f32 v160, v200, v160\n\t"
    "v_add_f32 v161, v201, v161\n\t"
    "v_mfma_f32_16x16x32_bf16 v[140:143], a[124:127], a[92:95], v[140:143]\n\t"
    "ds_read_b128 a[176:179], v235 offset:0\n\t"
    "ds_read2_b64 a[180:183], v235 offset0:129 offset1:128\n\t"
    "ds_read_b128 a[184:187], v235 offset:2048\n\t"
    "v_add_f32 v162, v202, v162\n\t"
    "v_add_f32 v163, v203, v163\n\t"
    "v_exp_f32 v160, v160\n\t"
    "v_exp_f32 v161, v161\n\t"
    "v_add_f32 v164, v200, v164\n\t"
    "v_add_f32 v165, v201, v165\n\t"
    "v_add_f32 v166, v202, v166\n\t"
    "v_add_f32 v167, v203, v167\n\t"
    "v_exp_f32 v162, v162\n\t"
    "v_exp_f32 v163, v163\n\t"
    "v_exp_f32 v164, v164\n\t"
    "v_exp_f32 v165, v165\n\t"
    "v_exp_f32 v166, v166\n\t"
    "v_exp_f32 v167, v167\n\t"
    "v_cvt_pk_bf16_f32 v160, v160, v161\n\t"
    "v_cvt_pk_bf16_f32 v161, v162, v163\n\t"
    "v_cvt_pk_bf16_f32 v162, v164, v165\n\t"
    "v_cvt_pk_bf16_f32 v163, v166, v167\n\t"
    "v_add_f32 v169, v205, v169\n\t"
    "v_add_f32 v170, v206, v170\n\t"
    "v_mfma_f32_16x16x32_bf16 v[64:67], a[128:131], v[160:163], 0\n\t"
    "v_add_f32 v171, v207, v171\n\t"
    "v_add_f32 v172, v204, v172\n\t"
    "v_mfma_f32_16x16x32_bf16 v[68:71], a[132:135], v[160:163], 0\n\t"
    "ds_read2_b64 a[188:191], v245 offset0:129 offset1:128\n\t"
    "v_add_f32 v173, v205, v173\n\t"
    "v_mfma_f32_16x16x32_bf16 v[72:75], a[136:139], v[160:163], 0\n\t"
    "v_exp_f32 v168, v168\n\t"
    "v_mfma_f32_16x16x32_bf16 v[76:79], a[140:143], v[160:163], 0\n\t"
    "v_exp_f32 v169, v169\n\t"
    "v_mfma_f32_16x16x32_bf16 v[80:83], a[144:147], v[160:163], 0\n\t"
    "v_add_f32 v174, v206, v174\n\t"
    "v_add_f32 v175, v207, v175\n\t"
    "v_mfma_f32_16x16x32_bf16 v[84:87], a[148:151], v[160:163], 0\n\t"
    "v_exp_f32 v170, v170\n\t"
    "v_mfma_f32_16x16x32_bf16 v[88:91], a[152:155], v[160:163], 0\n\t"
    "v_exp_f32 v171, v171\n\t"
    "v_mfma_f32_16x16x32_bf16 v[92:95], a[156:159], v[160:163], 0\n\t"
    "v_exp_f32 v172, v172\n\t"
    "v_exp_f32 v173, v173\n\t"
    "v_exp_f32 v174, v174\n\t"
    "v_exp_f32 v175, v175\n\t"
    "v_cvt_pk_bf16_f32 v168, v168, v169\n\t"
    "v_cvt_pk_bf16_f32 v169, v170, v171\n\t"
    "v_cvt_pk_bf16_f32 v170, v172, v173\n\t"
    "v_cvt_pk_bf16_f32 v171, v174, v175\n\t"
    "v_exp_f32 v144, v144\n\t"
    "s_nop 0\n\t"
    "v_mfma_f32_16x16x32_bf16 v[96:99], a[128:131], v[168:171], 0\n\t"
    "ds_read_b64_tr_b16 a[128:129], v226 offset:24576\n\t"
    "v_add_f32 v232, v229, v233\n\t"
    "v_mfma_f32_16x16x32_bf16 v[100:103], a[132:135], v[168:171], 0\n\t"
    "ds_read_b64_tr_b16 a[130:131], v226 offset:28672\n\t"
    "ds_read_b64_tr_b16 a[132:133], v225 offset:24576\n\t"
    "ds_read_b64_tr_b16 a[134:135], v225 offset:28672\n\t"
    "v_mfma_f32_16x16x32_bf16 v[104:107], a[136:139], v[168:171], 0\n\t"
    "ds_read_b64_tr_b16 a[136:137], v226 offset:25088\n\t"
    "ds_read_b64_tr_b16 a[138:139], v226 offset:29184\n\t"
    "v_mfma_f32_16x16x32_bf16 v[108:111], a[140:143], v[168:171], 0\n\t"
    "ds_read_b64_tr_b16 a[140:141], v225 offset:25088\n\t"
    "ds_read_b64_tr_b16 a[142:143], v225 offset:29184\n\t"
    "v_mfma_f32_16x16x32_bf16 v[112:115], a[144:147], v[168:171], 0\n\t"
    "ds_read_b64_tr_b16 a[144:145], v226 offset:25600\n\t"
    "ds_read_b64_tr_b16 a[146:147], v226 offset:29696\n\t"
    "v_mfma_f32_16x16x32_bf16 v[116:119], a[148:151], v[168:171], 0\n\t"
    "ds_read_b64_tr_b16 a[148:149], v225 offset:25600\n\t"
    "ds_read_b64_tr_b16 a[150:151], v225 offset:29696\n\t"
    "v_mfma_f32_16x16x32_bf16 v[120:123], a[152:155], v[168:171], 0\n\t"
    "ds_read_b64_tr_b16 a[152:153], v226 offset:26112\n\t"
    "ds_read_b64_tr_b16 a[154:155], v226 offset:30208\n\t"
    "v_mfma_f32_16x16x32_bf16 v[124:127], a[156:159], v[168:171], 0\n\t"
    "ds_read_b64_tr_b16 a[156:157], v225 offset:26112\n\t"
    "ds_read_b64_tr_b16 a[158:159], v225 offset:30208\n\t"
    "v_mfma_f32_16x16x32_bf16 v[216:219], v[252:255], v[160:163], 0\n\t"
    "v_exp_f32 v145, v145\n\t"
    "v_mfma_f32_16x16x32_bf16 v[220:223], v[252:255], v[168:171], 0\n\t"
    "v_exp_f32 v146, v146\n\t"
    "v_mfma_f32_16x16x32_bf16 v[160:163], a[96:99], a[0:3], v[200:203]\n\t"
    "v_exp_f32 v147, v147\n\t"
    "v_mfma_f32_16x16x32_bf16 v[160:163], a[100:103], a[4:7], v[160:163]\n\t"
    "v_exp_f32 v148, v148\n\t"
    "v_mfma_f32_16x16x32_bf16 v[160:163], a[104:107], a[8:11], v[160:163]\n\t"
    "v_exp_f32 v149, v149\n\t"
    "v_mfma_f32_16x16x32_bf16 v[160:163], a[108:111], a[12:15], v[160:163]\n\t"
    "v_exp_f32 v150, v150\n\t"
    "v_mfma_f32_16x16x32_bf16 v[164:167], a[112:115], a[0:3], v[200:203]\n\t"
    "v_exp_f32 v151, v151\n\t"
    "v_mfma_f32_16x16x32_bf16 v[164:167], a[116:119], a[4:7], v[164:167]\n\t"
    "v_cvt_pk_bf16_f32 v144, v144, v145\n\t"
    "v_cvt_pk_bf16_f32 v145, v146, v147\n\t"
    "v_mfma_f32_16x16x32_bf16 v[164:167], a[120:123], a[8:11], v[164:167]\n\t"
    "s_waitcnt lgkmcnt(0)\n\t"
    "v_cvt_pk_bf16_f32 v146, v148, v149\n\t"
    "v_cvt_pk_bf16_f32 v147, v150, v151\n\t"
    "s_waitcnt vmcnt(4)\n\t"
    "s_barrier\n\t"
    "s_add_u32 m0, %8, 16384\n\t"
    "s_add_u32 s40, %9, 81920\n\t"
    "v_mfma_f32_16x16x32_bf16 v[164:167], a[124:127], a[12:15], v[164:167]\n\t"
    "v_exp_f32 v152, v152\n\t"
    "v_mfma_f32_16x16x32_bf16 v[168:171], a[96:99], a[16:19], v[204:207]\n\t"
    "ds_read_b128 a[96:99], v224 offset:57344\n\t"
    "v_mfma_f32_16x16x32_bf16 v[168:171], a[100:103], a[20:23], v[168:171]\n\t"
    "ds_read_b128 a[100:103], v224 offset:58368\n\t"
    "v_mfma_f32_16x16x32_bf16 v[168:171], a[104:107], a[24:27], v[168:171]\n\t"
    "buffer_load_dwordx4 v227, %3, s40 offen offset:0  lds\n\t"
    "v_mfma_f32_16x16x32_bf16 v[168:171], a[108:111], a[28:31], v[168:171]\n\t"
    "ds_read_b128 a[104:107], v224 offset:59392\n\t"
    "v_mfma_f32_16x16x32_bf16 v[172:175], a[112:115], a[16:19], v[204:207]\n\t"
    "ds_read_b128 a[108:111], v224 offset:60416\n\t"
    "v_mfma_f32_16x16x32_bf16 v[172:175], a[116:119], a[20:23], v[172:175]\n\t"
    "ds_read_b128 a[112:115], v224 offset:61440\n\t"
    "v_mfma_f32_16x16x32_bf16 v[172:175], a[120:123], a[24:27], v[172:175]\n\t"
    "ds_read_b128 a[116:119], v224 offset:62464\n\t"
    "v_mfma_f32_16x16x32_bf16 v[172:175], a[124:127], a[28:31], v[172:175]\n\t"
    "ds_read_b128 a[120:123], v224 offset:63488\n\t"
    "v_mfma_f32_16x16x32_bf16 v[0:3], a[128:131], v[144:147], v[0:3]\n\t"
    "buffer_load_dwordx4 v248, %3, s40 offen offset:1024  lds\n\t"
    "s_add_u32 m0, %8, 24576\n\t"
    "s_add_u32 s40, %9, 90112\n\t"
    "v_mfma_f32_16x16x32_bf16 v[4:7], a[132:135], v[144:147], v[4:7]\n\t"
    "ds_read_b128 a[124:127], v224 offset:64512\n\t"
    "v_mfma_f32_16x16x32_bf16 v[8:11], a[136:139], v[144:147], v[8:11]\n\t"
    "v_exp_f32 v153, v153\n\t"
    "v_mfma_f32_16x16x32_bf16 v[12:15], a[140:143], v[144:147], v[12:15]\n\t"
    "v_exp_f32 v154, v154\n\t"
    "v_mfma_f32_16x16x32_bf16 v[16:19], a[144:147], v[144:147], v[16:19]\n\t"
    "v_exp_f32 v155, v155\n\t"
    "v_mfma_f32_16x16x32_bf16 v[20:23], a[148:151], v[144:147], v[20:23]\n\t"
    "v_exp_f32 v156, v156\n\t"
    "v_mfma_f32_16x16x32_bf16 v[24:27], a[152:155], v[144:147], v[24:27]\n\t"
    "buffer_load_dwordx4 v227, %3, s40 offen offset:0  lds\n\t"
    "v_mfma_f32_16x16x32_bf16 v[28:31], a[156:159], v[144:147], v[28:31]\n\t"
    "buffer_load_dwordx4 v248, %3, s40 offen offset:1024  lds\n\t"
    "v_exp_f32 v157, v157\n\t"
    "v_exp_f32 v158, v158\n\t"
    "v_exp_f32 v159, v159\n\t"
    "v_cvt_pk_bf16_f32 v152, v152, v153\n\t"
    "v_cvt_pk_bf16_f32 v153, v154, v155\n\t"
    "v_cvt_pk_bf16_f32 v154, v156, v157\n\t"
    "v_cvt_pk_bf16_f32 v155, v158, v159\n\t"
    "v_exp_f32 v176, v176\n\t"
    "s_nop 0\n\t"
    "v_mfma_f32_16x16x32_bf16 v[32:35], a[128:131], v[152:155], v[32:35]\n\t"
    "v_exp_f32 v177, v177\n\t"
    "v_mfma_f32_16x16x32_bf16 v[36:39], a[132:135], v[152:155], v[36:39]\n\t"
    "v_exp_f32 v178, v178\n\t"
    "v_mfma_f32_16x16x32_bf16 v[40:43], a[136:139], v[152:155], v[40:43]\n\t"
    "v_exp_f32 v179, v179\n\t"
    "v_mfma_f32_16x16x32_bf16 v[44:47], a[140:143], v[152:155], v[44:47]\n\t"
    "v_exp_f32 v180, v180\n\t"
    "v_mfma_f32_16x16x32_bf16 v[48:51], a[144:147], v[152:155], v[48:51]\n\t"
    "v_exp_f32 v181, v181\n\t"
    "v_mfma_f32_16x16x32_bf16 v[52:55], a[148:151], v[152:155], v[52:55]\n\t"
    "v_exp_f32 v182, v182\n\t"
    "v_mfma_f32_16x16x32_bf16 v[56:59], a[152:155], v[152:155], v[56:59]\n\t"
    "v_exp_f32 v183, v183\n\t"
    "v_mfma_f32_16x16x32_bf16 v[60:63], a[156:159], v[152:155], v[60:63]\n\t"
    "v_cvt_pk_bf16_f32 v176, v176, v177\n\t"
    "v_cvt_pk_bf16_f32 v177, v178, v179\n\t"
    "v_mfma_f32_16x16x32_bf16 v[208:211], v[252:255], v[144:147], v[208:211]\n\t"
    "v_cvt_pk_bf16_f32 v178, v180, v181\n\t"
    "v_cvt_pk_bf16_f32 v179, v182, v183\n\t"
    "v_mfma_f32_16x16x32_bf16 v[212:215], v[252:255], v[152:155], v[212:215]\n\t"
    "v_exp_f32 v184, v184\n\t"
    "v_mfma_f32_16x16x32_bf16 v[64:67], a[128:131], v[176:179], v[64:67]\n\t"
    "v_exp_f32 v185, v185\n\t"
    "v_mfma_f32_16x16x32_bf16 v[68:71], a[132:135], v[176:179], v[68:71]\n\t"
    "v_exp_f32 v186, v186\n\t"
    "v_mfma_f32_16x16x32_bf16 v[72:75], a[136:139], v[176:179], v[72:75]\n\t"
    "v_exp_f32 v187, v187\n\t"
    "v_mfma_f32_16x16x32_bf16 v[76:79], a[140:143], v[176:179], v[76:79]\n\t"
    "v_exp_f32 v188, v188\n\t"
    "v_mfma_f32_16x16x32_bf16 v[80:83], a[144:147], v[176:179], v[80:83]\n\t"
    "v_exp_f32 v189, v189\n\t"
    "v_mfma_f32_16x16x32_bf16 v[84:87], a[148:151], v[176:179], v[84:87]\n\t"
    "v_exp_f32 v190, v190\n\t"
    "v_mfma_f32_16x16x32_bf16 v[88:91], a[152:155], v[176:179], v[88:91]\n\t"
    "ds_read_b128 a[32:35], v230 offset:0\n\t"
    "v_cvt_pk_bf16_f32 v184, v184, v185\n\t"
    "v_mfma_f32_16x16x32_bf16 v[92:95], a[156:159], v[176:179], v[92:95]\n\t"
    "ds_read_b128 a[36:39], v231 offset:0\n\t"
    "v_exp_f32 v191, v191\n\t"
    "v_cvt_pk_bf16_f32 v185, v186, v187\n\t"
    "v_cvt_pk_bf16_f32 v186, v188, v189\n\t"
    "v_cvt_pk_bf16_f32 v187, v190, v191\n\t"
    "v_exp_f32 v128, v128\n\t"
    "s_nop 0\n\t"
    "v_mfma_f32_16x16x32_bf16 v[96:99], a[128:131], v[184:187], v[96:99]\n\t"
    "ds_read_b128 a[48:51], v230 offset:2048\n\t"
    "v_mfma_f32_16x16x32_bf16 v[100:103], a[132:135], v[184:187], v[100:103]\n\t"
    "ds_read_b128 a[52:55], v231 offset:2048\n\t"
    "v_mfma_f32_16x16x32_bf16 v[104:107], a[136:139], v[184:187], v[104:107]\n\t"
    "s_add_u32 s40, %11, 8192\n\t"
    "ds_read_b64_tr_b16 a[128:129], v226 offset:0\n\t"
    "v_mfma_f32_16x16x32_bf16 v[108:111], a[140:143], v[184:187], v[108:111]\n\t"
    "ds_read_b64_tr_b16 a[130:131], v226 offset:4096\n\t"
    "v_mfma_f32_16x16x32_bf16 v[112:115], a[144:147], v[184:187], v[112:115]\n\t"
    "ds_read_b64_tr_b16 a[132:133], v225 offset:0\n\t"
    "v_mfma_f32_16x16x32_bf16 v[116:119], a[148:151], v[184:187], v[116:119]\n\t"
    "ds_read_b64_tr_b16 a[134:135], v225 offset:4096\n\t"
    "v_mfma_f32_16x16x32_bf16 v[120:123], a[152:155], v[184:187], v[120:123]\n\t"
    "ds_read_b64_tr_b16 a[136:137], v226 offset:512\n\t"
    "v_mfma_f32_16x16x32_bf16 v[124:127], a[156:159], v[184:187], v[124:127]\n\t"
    "ds_read_b64_tr_b16 a[138:139], v226 offset:4608\n\t"
    "v_mfma_f32_16x16x32_bf16 v[216:219], v[252:255], v[176:179], v[216:219]\n\t"
    "ds_read_b64_tr_b16 a[140:141], v225 offset:512\n\t"
    "v_mfma_f32_16x16x32_bf16 v[220:223], v[252:255], v[184:187], v[220:223]\n\t"
    "s_waitcnt lgkmcnt(11)\n\t"
    "ds_read_b64_tr_b16 a[142:143], v225 offset:4608\n\t"
    "v_mfma_f32_16x16x32_bf16 v[144:147], a[96:99], a[64:67], v[192:195]\n\t"
    "ds_read_b64_tr_b16 a[144:145], v226 offset:1024\n\t"
    "v_mfma_f32_16x16x32_bf16 v[144:147], a[100:103], a[68:71], v[144:147]\n\t"
    "ds_read_b64_tr_b16 a[146:147], v226 offset:5120\n\t"
    "v_mfma_f32_16x16x32_bf16 v[144:147], a[104:107], a[72:75], v[144:147]\n\t"
    "ds_read_b64_tr_b16 a[148:149], v225 offset:1024\n\t"
    "v_mfma_f32_16x16x32_bf16 v[144:147], a[108:111], a[76:79], v[144:147]\n\t"
    "ds_read_b64_tr_b16 a[150:151], v225 offset:5120\n\t"
    "v_mfma_f32_16x16x32_bf16 v[148:151], a[112:115], a[64:67], v[192:195]\n\t"
    "ds_read_b64_tr_b16 a[152:153], v226 offset:1536\n\t"
    "v_mfma_f32_16x16x32_bf16 v[148:151], a[116:119], a[68:71], v[148:151]\n\t"
    "ds_read_b64_tr_b16 a[154:155], v226 offset:5632\n\t"
    "v_mfma_f32_16x16x32_bf16 v[148:151], a[120:123], a[72:75], v[148:151]\n\t"
    "ds_read_b64_tr_b16 a[156:157], v225 offset:1536\n\t"
    "v_mfma_f32_16x16x32_bf16 v[148:151], a[124:127], a[76:79], v[148:151]\n\t"
    "ds_read_b64_tr_b16 a[158:159], v225 offset:5632\n\t"
    "v_mfma_f32_16x16x32_bf16 v[152:155], a[96:99], a[80:83], v[196:199]\n\t"
    "v_exp_f32 v129, v129\n\t"
    "v_mfma_f32_16x16x32_bf16 v[152:155], a[100:103], a[84:87], v[152:155]\n\t"
    "v_exp_f32 v130, v130\n\t"
    "v_mfma_f32_16x16x32_bf16 v[152:155], a[104:107], a[88:91], v[152:155]\n\t"
    "v_exp_f32 v131, v131\n\t"
    "v_mfma_f32_16x16x32_bf16 v[152:155], a[108:111], a[92:95], v[152:155]\n\t"
    "v_exp_f32 v132, v132\n\t"
    "v_mfma_f32_16x16x32_bf16 v[156:159], a[112:115], a[80:83], v[196:199]\n\t"
    "v_exp_f32 v133, v133\n\t"
    "v_mfma_f32_16x16x32_bf16 v[156:159], a[116:119], a[84:87], v[156:159]\n\t"
    "v_exp_f32 v134, v134\n\t"
    "v_mfma_f32_16x16x32_bf16 v[156:159], a[120:123], a[88:91], v[156:159]\n\t"
    "v_exp_f32 v135, v135\n\t"
    "v_mfma_f32_16x16x32_bf16 v[156:159], a[124:127], a[92:95], v[156:159]\n\t"
    "v_cvt_pk_bf16_f32 v128, v128, v129\n\t"
    "v_cvt_pk_bf16_f32 v129, v130, v131\n\t"
    "v_mfma_f32_16x16x32_bf16 v[176:179], a[96:99], a[0:3], v[200:203]\n\t"
    "v_cvt_pk_bf16_f32 v130, v132, v133\n\t"
    "v_cvt_pk_bf16_f32 v131, v134, v135\n\t"
    "v_mfma_f32_16x16x32_bf16 v[176:179], a[100:103], a[4:7], v[176:179]\n\t"
    "buffer_store_dwordx4 a[160:163], v236, %4, s40 offen offset:0 sc1\n\t"
    "v_mfma_f32_16x16x32_bf16 v[176:179], a[104:107], a[8:11], v[176:179]\n\t"
    "v_exp_f32 v136, v136\n\t"
    "v_mfma_f32_16x16x32_bf16 v[176:179], a[108:111], a[12:15], v[176:179]\n\t"
    "v_exp_f32 v137, v137\n\t"
    "v_mfma_f32_16x16x32_bf16 v[180:183], a[112:115], a[0:3], v[200:203]\n\t"
    "v_exp_f32 v138, v138\n\t"
    "v_mfma_f32_16x16x32_bf16 v[180:183], a[116:119], a[4:7], v[180:183]\n\t"
    "v_exp_f32 v139, v139\n\t"
    "v_mfma_f32_16x16x32_bf16 v[180:183], a[120:123], a[8:11], v[180:183]\n\t"
    "v_exp_f32 v140, v140\n\t"
    "v_mfma_f32_16x16x32_bf16 v[180:183], a[124:127], a[12:15], v[180:183]\n\t"
    "v_exp_f32 v141, v141\n\t"
    "v_mfma_f32_16x16x32_bf16 v[184:187], a[96:99], a[16:19], v[204:207]\n\t"
    "ds_read_b128 a[96:99], v244 offset:0\n\t"
    "buffer_store_dwordx4 a[164:167], v236, %4, s40 offen offset:2048 sc1\n\t"
    "s_add_u32 s40, %11, 12288\n\t"
    "v_mfma_f32_16x16x32_bf16 v[184:187], a[100:103], a[20:23], v[184:187]\n\t"
    "ds_read_b128 a[100:103], v244 offset:1024\n\t"
    "v_cvt_pk_bf16_f32 v136, v136, v137\n\t"
    "v_mfma_f32_16x16x32_bf16 v[184:187], a[104:107], a[24:27], v[184:187]\n\t"
    "ds_read_b128 a[104:107], v244 offset:2048\n\t"
    "v_cvt_pk_bf16_f32 v137, v138, v139\n\t"
    "v_mfma_f32_16x16x32_bf16 v[184:187], a[108:111], a[28:31], v[184:187]\n\t"
    "ds_read_b128 a[108:111], v244 offset:3072\n\t"
    "v_cvt_pk_bf16_f32 v138, v140, v141\n\t"
    "v_mfma_f32_16x16x32_bf16 v[188:191], a[112:115], a[16:19], v[204:207]\n\t"
    "ds_read_b128 a[112:115], v244 offset:4096\n\t"
    "v_mfma_f32_16x16x32_bf16 v[188:191], a[116:119], a[20:23], v[188:191]\n\t"
    "ds_read_b128 a[116:119], v244 offset:5120\n\t"
    "v_mfma_f32_16x16x32_bf16 v[188:191], a[120:123], a[24:27], v[188:191]\n\t"
    "ds_read_b128 a[120:123], v244 offset:6144\n\t"
    "v_mfma_f32_16x16x32_bf16 v[188:191], a[124:127], a[28:31], v[188:191]\n\t"
    "s_waitcnt lgkmcnt(7)\n\t"
    "ds_read_b128 a[124:127], v244 offset:7168\n\t"
    "v_mfma_f32_16x16x32_bf16 v[0:3], a[128:131], v[128:131], v[0:3]\n\t"
    "v_exp_f32 v142, v142\n\t"
    "v_mfma_f32_16x16x32_bf16 v[4:7], a[132:135], v[128:131], v[4:7]\n\t"
    "buffer_store_dwordx4 a[168:171], v236, %4, s40 offen offset:0 sc1\n\t"
    "v_mfma_f32_16x16x32_bf16 v[8:11], a[136:139], v[128:131], v[8:11]\n\t"
    "v_exp_f32 v143, v143\n\t"
    "v_mfma_f32_16x16x32_bf16 v[12:15], a[140:143], v[128:131], v[12:15]\n\t"
    "v_cvt_pk_bf16_f32 v139, v142, v143\n\t"
    "v_mfma_f32_16x16x32_bf16 v[16:19], a[144:147], v[128:131], v[16:19]\n\t"
    "v_exp_f32 v160, v160\n\t"
    "v_mfma_f32_16x16x32_bf16 v[20:23], a[148:151], v[128:131], v[20:23]\n\t"
    "v_exp_f32 v161, v161\n\t"
    "v_mfma_f32_16x16x32_bf16 v[24:27], a[152:155], v[128:131], v[24:27]\n\t"
    "v_exp_f32 v162, v162\n\t"
    "v_mfma_f32_16x16x32_bf16 v[28:31], a[156:159], v[128:131], v[28:31]\n\t"
    "v_exp_f32 v163, v163\n\t"
    "v_mfma_f32_16x16x32_bf16 v[32:35], a[128:131], v[136:139], v[32:35]\n\t"
    "v_exp_f32 v164, v164\n\t"
    "v_mfma_f32_16x16x32_bf16 v[36:39], a[132:135], v[136:139], v[36:39]\n\t"
    "buffer_store_dwordx4 a[172:175], v236, %4, s40 offen offset:2048 sc1\n\t"
    "v_mfma_f32_16x16x32_bf16 v[40:43], a[136:139], v[136:139], v[40:43]\n\t"
    "v_exp_f32 v165, v165\n\t"
    "v_mfma_f32_16x16x32_bf16 v[44:47], a[140:143], v[136:139], v[44:47]\n\t"
    "v_exp_f32 v166, v166\n\t"
    "v_mfma_f32_16x16x32_bf16 v[48:51], a[144:147], v[136:139], v[48:51]\n\t"
    "v_exp_f32 v167, v167\n\t"
    "v_mfma_f32_16x16x32_bf16 v[52:55], a[148:151], v[136:139], v[52:55]\n\t"
    "v_cvt_pk_bf16_f32 v160, v160, v161\n\t"
    "v_cvt_pk_bf16_f32 v161, v162, v163\n\t"
    "v_mfma_f32_16x16x32_bf16 v[56:59], a[152:155], v[136:139], v[56:59]\n\t"
    "v_cvt_pk_bf16_f32 v162, v164, v165\n\t"
    "v_cvt_pk_bf16_f32 v163, v166, v167\n\t"
    "v_mfma_f32_16x16x32_bf16 v[60:63], a[156:159], v[136:139], v[60:63]\n\t"
    "v_exp_f32 v168, v168\n\t"
    "v_mfma_f32_16x16x32_bf16 v[208:211], v[252:255], v[128:131], v[208:211]\n\t"
    "v_exp_f32 v169, v169\n\t"
    "v_mfma_f32_16x16x32_bf16 v[212:215], v[252:255], v[136:139], v[212:215]\n\t"
    "v_exp_f32 v170, v170\n\t"
    "v_mfma_f32_16x16x32_bf16 v[64:67], a[128:131], v[160:163], v[64:67]\n\t"
    "v_exp_f32 v171, v171\n\t"
    "v_mfma_f32_16x16x32_bf16 v[68:71], a[132:135], v[160:163], v[68:71]\n\t"
    "v_exp_f32 v172, v172\n\t"
    "v_mfma_f32_16x16x32_bf16 v[72:75], a[136:139], v[160:163], v[72:75]\n\t"
    "v_exp_f32 v173, v173\n\t"
    "v_mfma_f32_16x16x32_bf16 v[76:79], a[140:143], v[160:163], v[76:79]\n\t"
    "v_exp_f32 v174, v174\n\t"
    "v_mfma_f32_16x16x32_bf16 v[80:83], a[144:147], v[160:163], v[80:83]\n\t"
    "v_exp_f32 v175, v175\n\t"
    "v_mfma_f32_16x16x32_bf16 v[84:87], a[148:151], v[160:163], v[84:87]\n\t"
    "v_cvt_pk_bf16_f32 v168, v168, v169\n\t"
    "v_cvt_pk_bf16_f32 v169, v170, v171\n\t"
    "v_mfma_f32_16x16x32_bf16 v[88:91], a[152:155], v[160:163], v[88:91]\n\t"
    "v_cvt_pk_bf16_f32 v170, v172, v173\n\t"
    "v_cvt_pk_bf16_f32 v171, v174, v175\n\t"
    "v_mfma_f32_16x16x32_bf16 v[92:95], a[156:159], v[160:163], v[92:95]\n\t"
    "v_exp_f32 v144, v144\n\t"
    "v_mfma_f32_16x16x32_bf16 v[96:99], a[128:131], v[168:171], v[96:99]\n\t"
    "ds_read_b64_tr_b16 a[128:129], v226 offset:8192\n\t"
    "v_mfma_f32_16x16x32_bf16 v[100:103], a[132:135], v[168:171], v[100:103]\n\t"
    "ds_read_b64_tr_b16 a[130:131], v226 offset:12288\n\t"
    "v_mfma_f32_16x16x32_bf16 v[104:107], a[136:139], v[168:171], v[104:107]\n\t"
    "ds_read_b64_tr_b16 a[132:133], v225 offset:8192\n\t"
    "v_mfma_f32_16x16x32_bf16 v[108:111], a[140:143], v[168:171], v[108:111]\n\t"
    "ds_read_b64_tr_b16 a[134:135], v225 offset:12288\n\t"
    "v_mfma_f32_16x16x32_bf16 v[112:115], a[144:147], v[168:171], v[112:115]\n\t"
    "ds_read_b64_tr_b16 a[136:137], v226 offset:8704\n\t"
    "v_mfma_f32_16x16x32_bf16 v[116:119], a[148:151], v[168:171], v[116:119]\n\t"
    "ds_read_b64_tr_b16 a[138:139], v226 offset:12800\n\t"
    "v_mfma_f32_16x16x32_bf16 v[120:123], a[152:155], v[168:171], v[120:123]\n\t"
    "ds_read_b64_tr_b16 a[140:141], v225 offset:8704\n\t"
    "v_mfma_f32_16x16x32_bf16 v[124:127], a[156:159], v[168:171], v[124:127]\n\t"
    "ds_read_b64_tr_b16 a[142:143], v225 offset:12800\n\t"
    "v_mfma_f32_16x16x32_bf16 v[216:219], v[252:255], v[160:163], v[216:219]\n\t"
    "ds_read_b64_tr_b16 a[144:145], v226 offset:9216\n\t"
    "v_mfma_f32_16x16x32_bf16 v[220:223], v[252:255], v[168:171], v[220:223]\n\t"
    "s_waitcnt lgkmcnt(9)\n\t"
    "ds_read_b64_tr_b16 a[146:147], v226 offset:13312\n\t"
    "v_mfma_f32_16x16x32_bf16 v[128:131], a[96:99], a[64:67], v[192:195]\n\t"
    "ds_read_b64_tr_b16 a[148:149], v225 offset:9216\n\t"
    "v_mfma_f32_16x16x32_bf16 v[128:131], a[100:103], a[68:71], v[128:131]\n\t"
    "ds_read_b64_tr_b16 a[150:151], v225 offset:13312\n\t"
    "v_mfma_f32_16x16x32_bf16 v[128:131], a[104:107], a[72:75], v[128:131]\n\t"
    "ds_read_b64_tr_b16 a[152:153], v226 offset:9728\n\t"
    "v_mfma_f32_16x16x32_bf16 v[128:131], a[108:111], a[76:79], v[128:131]\n\t"
    "ds_read_b64_tr_b16 a[154:155], v226 offset:13824\n\t"
    "v_mfma_f32_16x16x32_bf16 v[132:135], a[112:115], a[64:67], v[192:195]\n\t"
    "ds_read_b64_tr_b16 a[156:157], v225 offset:9728\n\t"
    "v_mfma_f32_16x16x32_bf16 v[132:135], a[116:119], a[68:71], v[132:135]\n\t"
    "ds_read_b64_tr_b16 a[158:159], v225 offset:13824\n\t"
    "v_mfma_f32_16x16x32_bf16 v[132:135], a[120:123], a[72:75], v[132:135]\n\t"
    "v_exp_f32 v145, v145\n\t"
    "v_mfma_f32_16x16x32_bf16 v[132:135], a[124:127], a[76:79], v[132:135]\n\t"
    "v_exp_f32 v146, v146\n\t"
    "v_mfma_f32_16x16x32_bf16 v[136:139], a[96:99], a[80:83], v[196:199]\n\t"
    "v_exp_f32 v147, v147\n\t"
    "v_mfma_f32_16x16x32_bf16 v[136:139], a[100:103], a[84:87], v[136:139]\n\t"
    "v_exp_f32 v148, v148\n\t"
    "v_mfma_f32_16x16x32_bf16 v[136:139], a[104:107], a[88:91], v[136:139]\n\t"
    "v_exp_f32 v149, v149\n\t"
    "v_mfma_f32_16x16x32_bf16 v[136:139], a[108:111], a[92:95], v[136:139]\n\t"
    "v_exp_f32 v150, v150\n\t"
    "v_mfma_f32_16x16x32_bf16 v[140:143], a[112:115], a[80:83], v[196:199]\n\t"
    "v_exp_f32 v151, v151\n\t"
    "v_mfma_f32_16x16x32_bf16 v[140:143], a[116:119], a[84:87], v[140:143]\n\t"
    "v_cvt_pk_bf16_f32 v144, v144, v145\n\t"
    "v_cvt_pk_bf16_f32 v145, v146, v147\n\t"
    "v_mfma_f32_16x16x32_bf16 v[140:143], a[120:123], a[88:91], v[140:143]\n\t"
    "v_cvt_pk_bf16_f32 v146, v148, v149\n\t"
    "v_cvt_pk_bf16_f32 v147, v150, v151\n\t"
    "v_mfma_f32_16x16x32_bf16 v[140:143], a[124:127], a[92:95], v[140:143]\n\t"
    "s_waitcnt lgkmcnt(0)\n\t"
    "s_waitcnt vmcnt(4)\n\t"
    "s_barrier\n\t"
    "v_exp_f32 v152, v152\n\t"
    "v_mfma_f32_16x16x32_bf16 v[160:163], a[96:99], a[0:3], v[200:203]\n\t"
    "s_add_u32 m0, %8, 0\n\t"
    "s_add_u32 s40, %9, 0\n\t"
    "v_mfma_f32_16x16x32_bf16 v[160:163], a[100:103], a[4:7], v[160:163]\n\t"
    "v_exp_f32 v153, v153\n\t"
    "v_mfma_f32_16x16x32_bf16 v[160:163], a[104:107], a[8:11], v[160:163]\n\t"
    "v_exp_f32 v154, v154\n\t"
    "v_mfma_f32_16x16x32_bf16 v[160:163], a[108:111], a[12:15], v[160:163]\n\t"
    "v_exp_f32 v155, v155\n\t"
    "v_mfma_f32_16x16x32_bf16 v[164:167], a[112:115], a[0:3], v[200:203]\n\t"
    "v_exp_f32 v156, v156\n\t"
    "v_mfma_f32_16x16x32_bf16 v[164:167], a[116:119], a[4:7], v[164:167]\n\t"
    "v_exp_f32 v157, v157\n\t"
    "v_mfma_f32_16x16x32_bf16 v[164:167], a[120:123], a[8:11], v[164:167]\n\t"
    "v_exp_f32 v158, v158\n\t"
    "v_mfma_f32_16x16x32_bf16 v[164:167], a[124:127], a[12:15], v[164:167]\n\t"
    "v_exp_f32 v159, v159\n\t"
    "v_mfma_f32_16x16x32_bf16 v[168:171], a[96:99], a[16:19], v[204:207]\n\t"
    "ds_read_b128 a[96:99], v244 offset:8192\n\t"
    "v_cvt_pk_bf16_f32 v152, v152, v153\n\t"
    "v_mfma_f32_16x16x32_bf16 v[168:171], a[100:103], a[20:23], v[168:171]\n\t"
    "ds_read_b128 a[100:103], v244 offset:9216\n\t"
    "v_cvt_pk_bf16_f32 v153, v154, v155\n\t"
    "v_mfma_f32_16x16x32_bf16 v[168:171], a[104:107], a[24:27], v[168:171]\n\t"
    "buffer_load_dwordx4 v227, %3, s40 offen offset:0  lds\n\t"
    "v_mfma_f32_16x16x32_bf16 v[168:171], a[108:111], a[28:31], v[168:171]\n\t"
    "ds_read_b128 a[104:107], v244 offset:10240\n\t"
    "v_cvt_pk_bf16_f32 v154, v156, v157\n\t"
    "v_mfma_f32_16x16x32_bf16 v[172:175], a[112:115], a[16:19], v[204:207]\n\t"
    "ds_read_b128 a[108:111], v244 offset:11264\n\t"
    "v_cvt_pk_bf16_f32 v155, v158, v159\n\t"
    "v_mfma_f32_16x16x32_bf16 v[172:175], a[116:119], a[20:23], v[172:175]\n\t"
    "ds_read_b128 a[112:115], v244 offset:12288\n\t"
    "v_mfma_f32_16x16x32_bf16 v[172:175], a[120:123], a[24:27], v[172:175]\n\t"
    "ds_read_b128 a[116:119], v244 offset:13312\n\t"
    "v_mfma_f32_16x16x32_bf16 v[172:175], a[124:127], a[28:31], v[172:175]\n\t"
    "ds_read_b128 a[120:123], v244 offset:14336\n\t"
    "v_mfma_f32_16x16x32_bf16 v[0:3], a[128:131], v[144:147], v[0:3]\n\t"
    "buffer_load_dwordx4 v248, %3, s40 offen offset:1024  lds\n\t"
    "s_add_u32 m0, %8, 8192\n\t"
    "s_add_u32 s40, %9, 8192\n\t"
    "v_mfma_f32_16x16x32_bf16 v[4:7], a[132:135], v[144:147], v[4:7]\n\t"
    "ds_read_b128 a[124:127], v244 offset:15360\n\t"
    "v_mfma_f32_16x16x32_bf16 v[8:11], a[136:139], v[144:147], v[8:11]\n\t"
    "v_exp_f32 v176, v176\n\t"
    "v_mfma_f32_16x16x32_bf16 v[12:15], a[140:143], v[144:147], v[12:15]\n\t"
    "v_exp_f32 v177, v177\n\t"
    "v_mfma_f32_16x16x32_bf16 v[16:19], a[144:147], v[144:147], v[16:19]\n\t"
    "v_exp_f32 v178, v178\n\t"
    "v_mfma_f32_16x16x32_bf16 v[20:23], a[148:151], v[144:147], v[20:23]\n\t"
    "v_exp_f32 v179, v179\n\t"
    "v_mfma_f32_16x16x32_bf16 v[24:27], a[152:155], v[144:147], v[24:27]\n\t"
    "buffer_load_dwordx4 v227, %3, s40 offen offset:0  lds\n\t"
    "v_mfma_f32_16x16x32_bf16 v[28:31], a[156:159], v[144:147], v[28:31]\n\t"
    "v_exp_f32 v180, v180\n\t"
    "v_mfma_f32_16x16x32_bf16 v[32:35], a[128:131], v[152:155], v[32:35]\n\t"
    "v_exp_f32 v181, v181\n\t"
    "v_mfma_f32_16x16x32_bf16 v[36:39], a[132:135], v[152:155], v[36:39]\n\t"
    "v_exp_f32 v182, v182\n\t"
    "v_mfma_f32_16x16x32_bf16 v[40:43], a[136:139], v[152:155], v[40:43]\n\t"
    "v_exp_f32 v183, v183\n\t"
    "v_mfma_f32_16x16x32_bf16 v[44:47], a[140:143], v[152:155], v[44:47]\n\t"
    "v_cvt_pk_bf16_f32 v176, v176, v177\n\t"
    "v_cvt_pk_bf16_f32 v177, v178, v179\n\t"
    "v_mfma_f32_16x16x32_bf16 v[48:51], a[144:147], v[152:155], v[48:51]\n\t"
    "buffer_load_dwordx4 v248, %3, s40 offen offset:1024  lds\n\t"
    "v_mfma_f32_16x16x32_bf16 v[52:55], a[148:151], v[152:155], v[52:55]\n\t"
    "v_cvt_pk_bf16_f32 v178, v180, v181\n\t"
    "v_cvt_pk_bf16_f32 v179, v182, v183\n\t"
    "v_mfma_f32_16x16x32_bf16 v[56:59], a[152:155], v[152:155], v[56:59]\n\t"
    "v_exp_f32 v184, v184\n\t"
    "v_mfma_f32_16x16x32_bf16 v[60:63], a[156:159], v[152:155], v[60:63]\n\t"
    "v_exp_f32 v185, v185\n\t"
    "v_mfma_f32_16x16x32_bf16 v[208:211], v[252:255], v[144:147], v[208:211]\n\t"
    "ds_read_b128 a[40:43], v230 offset:4096\n\t"
    "v_cvt_pk_bf16_f32 v184, v184, v185\n\t"
    "v_mfma_f32_16x16x32_bf16 v[212:215], v[252:255], v[152:155], v[212:215]\n\t"
    "ds_read_b128 a[44:47], v231 offset:4096\n\t"
    "v_mfma_f32_16x16x32_bf16 v[64:67], a[128:131], v[176:179], v[64:67]\n\t"
    "ds_read_b128 a[56:59], v230 offset:6144\n\t"
    "v_mfma_f32_16x16x32_bf16 v[68:71], a[132:135], v[176:179], v[68:71]\n\t"
    "ds_read_b128 a[60:63], v231 offset:6144\n\t"
    "v_mfma_f32_16x16x32_bf16 v[72:75], a[136:139], v[176:179], v[72:75]\n\t"
    "v_exp_f32 v186, v186\n\t"
    "v_mfma_f32_16x16x32_bf16 v[76:79], a[140:143], v[176:179], v[76:79]\n\t"
    "v_exp_f32 v187, v187\n\t"
    "v_mfma_f32_16x16x32_bf16 v[80:83], a[144:147], v[176:179], v[80:83]\n\t"
    "v_exp_f32 v188, v188\n\t"
    "v_mfma_f32_16x16x32_bf16 v[84:87], a[148:151], v[176:179], v[84:87]\n\t"
    "v_exp_f32 v189, v189\n\t"
    "v_mfma_f32_16x16x32_bf16 v[88:91], a[152:155], v[176:179], v[88:91]\n\t"
    "v_exp_f32 v190, v190\n\t"
    "v_mfma_f32_16x16x32_bf16 v[92:95], a[156:159], v[176:179], v[92:95]\n\t"
    "v_exp_f32 v191, v191\n\t"
    "v_cvt_pk_bf16_f32 v185, v186, v187\n\t"
    "v_cvt_pk_bf16_f32 v186, v188, v189\n\t"
    "v_cvt_pk_bf16_f32 v187, v190, v191\n\t"
    "v_exp_f32 v128, v128\n\t"
    "s_nop 0\n\t"
    "v_mfma_f32_16x16x32_bf16 v[96:99], a[128:131], v[184:187], v[96:99]\n\t"
    "ds_read_b64_tr_b16 a[128:129], v226 offset:16384\n\t"
    "v_mfma_f32_16x16x32_bf16 v[100:103], a[132:135], v[184:187], v[100:103]\n\t"
    "ds_read_b64_tr_b16 a[130:131], v226 offset:20480\n\t"
    "v_mfma_f32_16x16x32_bf16 v[104:107], a[136:139], v[184:187], v[104:107]\n\t"
    "s_add_u32 m0, %10, 0\n\t"
    "s_add_u32 s40, %11, 139264\n\t"
    "ds_read_b64_tr_b16 a[132:133], v225 offset:16384\n\t"
    "v_mfma_f32_16x16x32_bf16 v[108:111], a[140:143], v[184:187], v[108:111]\n\t"
    "ds_read_b64_tr_b16 a[134:135], v225 offset:20480\n\t"
    "v_mfma_f32_16x16x32_bf16 v[112:115], a[144:147], v[184:187], v[112:115]\n\t"
    "ds_read_b64_tr_b16 a[136:137], v226 offset:16896\n\t"
    "v_mfma_f32_16x16x32_bf16 v[116:119], a[148:151], v[184:187], v[116:119]\n\t"
    "ds_read_b64_tr_b16 a[138:139], v226 offset:20992\n\t"
    "v_mfma_f32_16x16x32_bf16 v[120:123], a[152:155], v[184:187], v[120:123]\n\t"
    "ds_read_b64_tr_b16 a[140:141], v225 offset:16896\n\t"
    "v_mfma_f32_16x16x32_bf16 v[124:127], a[156:159], v[184:187], v[124:127]\n\t"
    "ds_read_b64_tr_b16 a[142:143], v225 offset:20992\n\t"
    "v_mfma_f32_16x16x32_bf16 v[216:219], v[252:255], v[176:179], v[216:219]\n\t"
    "ds_read_b64_tr_b16 a[144:145], v226 offset:17408\n\t"
    "v_mfma_f32_16x16x32_bf16 v[220:223], v[252:255], v[184:187], v[220:223]\n\t"
    "s_waitcnt lgkmcnt(9)\n\t"
    "ds_read_b64_tr_b16 a[146:147], v226 offset:21504\n\t"
    "v_mfma_f32_16x16x32_bf16 v[144:147], a[96:99], a[64:67], v[192:195]\n\t"
    "ds_read_b64_tr_b16 a[148:149], v225 offset:17408\n\t"
    "v_mfma_f32_16x16x32_bf16 v[144:147], a[100:103], a[68:71], v[144:147]\n\t"
    "ds_read_b64_tr_b16 a[150:151], v225 offset:21504\n\t"
    "v_mfma_f32_16x16x32_bf16 v[144:147], a[104:107], a[72:75], v[144:147]\n\t"
    "ds_read_b64_tr_b16 a[152:153], v226 offset:17920\n\t"
    "v_mfma_f32_16x16x32_bf16 v[144:147], a[108:111], a[76:79], v[144:147]\n\t"
    "ds_read_b64_tr_b16 a[154:155], v226 offset:22016\n\t"
    "v_mfma_f32_16x16x32_bf16 v[148:151], a[112:115], a[64:67], v[192:195]\n\t"
    "ds_read_b64_tr_b16 a[156:157], v225 offset:17920\n\t"
    "v_mfma_f32_16x16x32_bf16 v[148:151], a[116:119], a[68:71], v[148:151]\n\t"
    "ds_read_b64_tr_b16 a[158:159], v225 offset:22016\n\t"
    "v_mfma_f32_16x16x32_bf16 v[148:151], a[120:123], a[72:75], v[148:151]\n\t"
    "v_exp_f32 v129, v129\n\t"
    "v_mfma_f32_16x16x32_bf16 v[148:151], a[124:127], a[76:79], v[148:151]\n\t"
    "v_exp_f32 v130, v130\n\t"
    "v_mfma_f32_16x16x32_bf16 v[152:155], a[96:99], a[80:83], v[196:199]\n\t"
    "v_exp_f32 v131, v131\n\t"
    "v_mfma_f32_16x16x32_bf16 v[152:155], a[100:103], a[84:87], v[152:155]\n\t"
    "v_exp_f32 v132, v132\n\t"
    "v_mfma_f32_16x16x32_bf16 v[152:155], a[104:107], a[88:91], v[152:155]\n\t"
    "v_exp_f32 v133, v133\n\t"
    "v_mfma_f32_16x16x32_bf16 v[152:155], a[108:111], a[92:95], v[152:155]\n\t"
    "v_exp_f32 v134, v134\n\t"
    "v_mfma_f32_16x16x32_bf16 v[156:159], a[112:115], a[80:83], v[196:199]\n\t"
    "v_exp_f32 v135, v135\n\t"
    "v_mfma_f32_16x16x32_bf16 v[156:159], a[116:119], a[84:87], v[156:159]\n\t"
    "v_cvt_pk_bf16_f32 v128, v128, v129\n\t"
    "v_cvt_pk_bf16_f32 v129, v130, v131\n\t"
    "v_mfma_f32_16x16x32_bf16 v[156:159], a[120:123], a[88:91], v[156:159]\n\t"
    "v_cvt_pk_bf16_f32 v130, v132, v133\n\t"
    "v_cvt_pk_bf16_f32 v131, v134, v135\n\t"
    "v_mfma_f32_16x16x32_bf16 v[156:159], a[124:127], a[92:95], v[156:159]\n\t"
    "v_exp_f32 v136, v136\n\t"
    "v_mfma_f32_16x16x32_bf16 v[176:179], a[96:99], a[0:3], v[200:203]\n\t"
    "v_exp_f32 v137, v137\n\t"
    "v_mfma_f32_16x16x32_bf16 v[176:179], a[100:103], a[4:7], v[176:179]\n\t"
    "buffer_load_dwordx4 v228, %1, s40 offen offset:0 nt lds\n\t"
    "v_mfma_f32_16x16x32_bf16 v[176:179], a[104:107], a[8:11], v[176:179]\n\t"
    "v_exp_f32 v138, v138\n\t"
    "v_mfma_f32_16x16x32_bf16 v[176:179], a[108:111], a[12:15], v[176:179]\n\t"
    "v_exp_f32 v139, v139\n\t"
    "v_mfma_f32_16x16x32_bf16 v[180:183], a[112:115], a[0:3], v[200:203]\n\t"
    "v_exp_f32 v140, v140\n\t"
    "v_mfma_f32_16x16x32_bf16 v[180:183], a[116:119], a[4:7], v[180:183]\n\t"
    "v_exp_f32 v141, v141\n\t"
    "v_mfma_f32_16x16x32_bf16 v[180:183], a[120:123], a[8:11], v[180:183]\n\t"
    "v_exp_f32 v142, v142\n\t"
    "v_mfma_f32_16x16x32_bf16 v[180:183], a[124:127], a[12:15], v[180:183]\n\t"
    "v_exp_f32 v143, v143\n\t"
    "v_mfma_f32_16x16x32_bf16 v[184:187], a[96:99], a[16:19], v[204:207]\n\t"
    "v_cvt_pk_bf16_f32 v136, v136, v137\n\t"
    "v_cvt_pk_bf16_f32 v137, v138, v139\n\t"
    "v_mfma_f32_16x16x32_bf16 v[184:187], a[100:103], a[20:23], v[184:187]\n\t"
    "buffer_load_dwordx4 v249, %1, s40 offen offset:1024 nt lds\n\t"
    "v_mfma_f32_16x16x32_bf16 v[184:187], a[104:107], a[24:27], v[184:187]\n\t"
    "v_cvt_pk_bf16_f32 v138, v140, v141\n\t"
    "v_cvt_pk_bf16_f32 v139, v142, v143\n\t"
    "v_mfma_f32_16x16x32_bf16 v[184:187], a[108:111], a[28:31], v[184:187]\n\t"
    "v_exp_f32 v160, v160\n\t"
    "v_mfma_f32_16x16x32_bf16 v[188:191], a[112:115], a[16:19], v[204:207]\n\t"
    "v_exp_f32 v161, v161\n\t"
    "v_mfma_f32_16x16x32_bf16 v[188:191], a[116:119], a[20:23], v[188:191]\n\t"
    "v_exp_f32 v162, v162\n\t"
    "v_mfma_f32_16x16x32_bf16 v[188:191], a[120:123], a[24:27], v[188:191]\n\t"
    "v_exp_f32 v163, v163\n\t"
    "v_mfma_f32_16x16x32_bf16 v[188:191], a[124:127], a[28:31], v[188:191]\n\t"
    "s_waitcnt lgkmcnt(0)\n\t"
    "v_exp_f32 v164, v164\n\t"
    "v_mfma_f32_16x16x32_bf16 v[0:3], a[128:131], v[128:131], v[0:3]\n\t"
    "v_exp_f32 v165, v165\n\t"
    "v_mfma_f32_16x16x32_bf16 v[4:7], a[132:135], v[128:131], v[4:7]\n\t"
    "buffer_load_dwordx4 v250, %1, s40 offen offset:2048 nt lds\n\t"
    "v_mfma_f32_16x16x32_bf16 v[8:11], a[136:139], v[128:131], v[8:11]\n\t"
    "v_exp_f32 v166, v166\n\t"
    "v_mfma_f32_16x16x32_bf16 v[12:15], a[140:143], v[128:131], v[12:15]\n\t"
    "v_exp_f32 v167, v167\n\t"
    "v_mfma_f32_16x16x32_bf16 v[16:19], a[144:147], v[128:131], v[16:19]\n\t"
    "v_cvt_pk_bf16_f32 v160, v160, v161\n\t"
    "v_cvt_pk_bf16_f32 v161, v162, v163\n\t"
    "v_mfma_f32_16x16x32_bf16 v[20:23], a[148:151], v[128:131], v[20:23]\n\t"
    "v_cvt_pk_bf16_f32 v162, v164, v165\n\t"
    "v_cvt_pk_bf16_f32 v163, v166, v167\n\t"
    "v_mfma_f32_16x16x32_bf16 v[24:27], a[152:155], v[128:131], v[24:27]\n\t"
    "v_exp_f32 v168, v168\n\t"
    "v_mfma_f32_16x16x32_bf16 v[28:31], a[156:159], v[128:131], v[28:31]\n\t"
    "v_exp_f32 v169, v169\n\t"
    "v_mfma_f32_16x16x32_bf16 v[32:35], a[128:131], v[136:139], v[32:35]\n\t"
    "v_exp_f32 v170, v170\n\t"
    "v_mfma_f32_16x16x32_bf16 v[36:39], a[132:135], v[136:139], v[36:39]\n\t"
    "buffer_load_dwordx4 v251, %1, s40 offen offset:3072 nt lds\n\t"
    "s_add_u32 s40, %11, 8320\n\t"
    "v_mfma_f32_16x16x32_bf16 v[40:43], a[136:139], v[136:139], v[40:43]\n\t"
    "v_exp_f32 v171, v171\n\t"
    "v_mfma_f32_16x16x32_bf16 v[44:47], a[140:143], v[136:139], v[44:47]\n\t"
    "v_exp_f32 v172, v172\n\t"
    "v_mfma_f32_16x16x32_bf16 v[48:51], a[144:147], v[136:139], v[48:51]\n\t"
    "v_exp_f32 v173, v173\n\t"
    "v_mfma_f32_16x16x32_bf16 v[52:55], a[148:151], v[136:139], v[52:55]\n\t"
    "v_exp_f32 v174, v174\n\t"
    "v_mfma_f32_16x16x32_bf16 v[56:59], a[152:155], v[136:139], v[56:59]\n\t"
    "v_exp_f32 v175, v175\n\t"
    "v_mfma_f32_16x16x32_bf16 v[60:63], a[156:159], v[136:139], v[60:63]\n\t"
    "v_cvt_pk_bf16_f32 v168, v168, v169\n\t"
    "v_cvt_pk_bf16_f32 v169, v170, v171\n\t"
    "v_mfma_f32_16x16x32_bf16 v[208:211], v[252:255], v[128:131], v[208:211]\n\t"
    "v_cvt_pk_bf16_f32 v170, v172, v173\n\t"
    "v_cvt_pk_bf16_f32 v171, v174, v175\n\t"
    "v_mfma_f32_16x16x32_bf16 v[212:215], v[252:255], v[136:139], v[212:215]\n\t"
    "buffer_store_dwordx4 a[176:179], v236, %4, s40 offen offset:0 sc1\n\t"
    "v_mfma_f32_16x16x32_bf16 v[64:67], a[128:131], v[160:163], v[64:67]\n\t"
    "v_exp_f32 v144, v144\n\t"
    "v_mfma_f32_16x16x32_bf16 v[68:71], a[132:135], v[160:163], v[68:71]\n\t"
    "v_exp_f32 v145, v145\n\t"
    "v_mfma_f32_16x16x32_bf16 v[72:75], a[136:139], v[160:163], v[72:75]\n\t"
    "v_exp_f32 v146, v146\n\t"
    "v_mfma_f32_16x16x32_bf16 v[76:79], a[140:143], v[160:163], v[76:79]\n\t"
    "v_exp_f32 v147, v147\n\t"
    "v_mfma_f32_16x16x32_bf16 v[80:83], a[144:147], v[160:163], v[80:83]\n\t"
    "v_exp_f32 v148, v148\n\t"
    "v_mfma_f32_16x16x32_bf16 v[84:87], a[148:151], v[160:163], v[84:87]\n\t"
    "v_exp_f32 v149, v149\n\t"
    "v_mfma_f32_16x16x32_bf16 v[88:91], a[152:155], v[160:163], v[88:91]\n\t"
    "v_exp_f32 v150, v150\n\t"
    "v_mfma_f32_16x16x32_bf16 v[92:95], a[156:159], v[160:163], v[92:95]\n\t"
    "buffer_store_dwordx4 a[180:183], v236, %4, s40 offen offset:2048 sc1\n\t"
    "s_add_u32 s40, %11, 12416\n\t"
    "v_mfma_f32_16x16x32_bf16 v[96:99], a[128:131], v[168:171], v[96:99]\n\t"
    "ds_read_b64_tr_b16 a[128:129], v226 offset:24576\n\t"
    "v_cvt_pk_bf16_f32 v144, v144, v145\n\t"
    "v_mfma_f32_16x16x32_bf16 v[100:103], a[132:135], v[168:171], v[100:103]\n\t"
    "ds_read_b64_tr_b16 a[130:131], v226 offset:28672\n\t"
    "v_cvt_pk_bf16_f32 v145, v146, v147\n\t"
    "v_mfma_f32_16x16x32_bf16 v[104:107], a[136:139], v[168:171], v[104:107]\n\t"
    "ds_read_b64_tr_b16 a[132:133], v225 offset:24576\n\t"
    "v_cvt_pk_bf16_f32 v146, v148, v149\n\t"
    "v_mfma_f32_16x16x32_bf16 v[108:111], a[140:143], v[168:171], v[108:111]\n\t"
    "ds_read_b64_tr_b16 a[134:135], v225 offset:28672\n\t"
    "v_mfma_f32_16x16x32_bf16 v[112:115], a[144:147], v[168:171], v[112:115]\n\t"
    "ds_read_b64_tr_b16 a[136:137], v226 offset:25088\n\t"
    "v_mfma_f32_16x16x32_bf16 v[116:119], a[148:151], v[168:171], v[116:119]\n\t"
    "ds_read_b64_tr_b16 a[138:139], v226 offset:29184\n\t"
    "v_mfma_f32_16x16x32_bf16 v[120:123], a[152:155], v[168:171], v[120:123]\n\t"
    "ds_read_b64_tr_b16 a[140:141], v225 offset:25088\n\t"
    "buffer_store_dwordx4 a[184:187], v236, %4, s40 offen offset:0 sc1\n\t"
    "v_mfma_f32_16x16x32_bf16 v[124:127], a[156:159], v[168:171], v[124:127]\n\t"
    "ds_read_b64_tr_b16 a[142:143], v225 offset:29184\n\t"
    "v_mfma_f32_16x16x32_bf16 v[216:219], v[252:255], v[160:163], v[216:219]\n\t"
    "ds_read_b64_tr_b16 a[144:145], v226 offset:25600\n\t"
    "v_mfma_f32_16x16x32_bf16 v[220:223], v[252:255], v[168:171], v[220:223]\n\t"
    "ds_read_b64_tr_b16 a[146:147], v226 offset:29696\n\t"
    "v_mfma_f32_16x16x32_bf16 v[128:131], a[192:195], a[32:35], 0\n\t"
    "ds_read_b64_tr_b16 a[148:149], v225 offset:25600\n\t"
    "v_mfma_f32_16x16x32_bf16 v[128:131], a[196:199], a[36:39], v[128:131]\n\t"
    "ds_read_b64_tr_b16 a[150:151], v225 offset:29696\n\t"
    "v_mfma_f32_16x16x32_bf16 v[128:131], a[200:203], a[40:43], v[128:131]\n\t"
    "ds_read_b64_tr_b16 a[152:153], v226 offset:26112\n\t"
    "v_mfma_f32_16x16x32_bf16 v[128:131], a[204:207], a[44:47], v[128:131]\n\t"
    "ds_read_b64_tr_b16 a[154:155], v226 offset:30208\n\t"
    "v_mfma_f32_16x16x32_bf16 v[132:135], a[208:211], a[32:35], 0\n\t"
    "ds_read_b64_tr_b16 a[156:157], v225 offset:26112\n\t"
    "buffer_store_dwordx4 a[188:191], v236, %4, s40 offen offset:2048 sc1\n\t"
    "v_mfma_f32_16x16x32_bf16 v[132:135], a[212:215], a[36:39], v[132:135]\n\t"
    "ds_read_b64_tr_b16 a[158:159], v225 offset:30208\n\t"
    "v_mfma_f32_16x16x32_bf16 v[132:135], a[216:219], a[40:43], v[132:135]\n\t"
    "v_exp_f32 v151, v151\n\t"
    "v_mfma_f32_16x16x32_bf16 v[132:135], a[220:223], a[44:47], v[132:135]\n\t"
    "v_exp_f32 v152, v152\n\t"
    "v_mfma_f32_16x16x32_bf16 v[136:139], a[192:195], a[48:51], 0\n\t"
    "v_exp_f32 v153, v153\n\t"
    "v_mfma_f32_16x16x32_bf16 v[136:139], a[196:199], a[52:55], v[136:139]\n\t"
    "v_cvt_pk_bf16_f32 v147, v150, v151\n\t"
    "v_cvt_pk_bf16_f32 v152, v152, v153\n\t"
    "v_mfma_f32_16x16x32_bf16 v[136:139], a[200:203], a[56:59], v[136:139]\n\t"
    "v_exp_f32 v154, v154\n\t"
    "v_mfma_f32_16x16x32_bf16 v[136:139], a[204:207], a[60:63], v[136:139]\n\t"
    "v_exp_f32 v155, v155\n\t"
    "v_mfma_f32_16x16x32_bf16 v[140:143], a[208:211], a[48:51], 0\n\t"
    "v_exp_f32 v156, v156\n\t"
    "v_mfma_f32_16x16x32_bf16 v[140:143], a[212:215], a[52:55], v[140:143]\n\t"
    "v_exp_f32 v157, v157\n\t"
    "v_mfma_f32_16x16x32_bf16 v[140:143], a[216:219], a[56:59], v[140:143]\n\t"
    "v_exp_f32 v158, v158\n\t"
    "v_mfma_f32_16x16x32_bf16 v[140:143], a[220:223], a[60:63], v[140:143]\n\t"
    "s_waitcnt lgkmcnt(0)\n\t"
    "s_waitcnt vmcnt(8)\n\t"
    "s_barrier\n\t"
    "v_exp_f32 v159, v159\n\t"
    "v_mfma_f32_16x16x32_bf16 v[160:163], a[192:195], a[0:3], v[200:203]\n\t"
    "s_add_u32 m0, %8, 16384\n\t"
    "s_add_u32 s40, %9, 16384\n\t"
    "v_cvt_pk_bf16_f32 v153, v154, v155\n\t"
    "v_mfma_f32_16x16x32_bf16 v[160:163], a[196:199], a[4:7], v[160:163]\n\t"
    "v_cvt_pk_bf16_f32 v154, v156, v157\n\t"
    "v_cvt_pk_bf16_f32 v155, v158, v159\n\t"
    "v_mfma_f32_16x16x32_bf16 v[160:163], a[200:203], a[8:11], v[160:163]\n\t"
    "v_exp_f32 v176, v176\n\t"
    "v_mfma_f32_16x16x32_bf16 v[160:163], a[204:207], a[12:15], v[160:163]\n\t"
    "v_exp_f32 v177, v177\n\t"
    "v_mfma_f32_16x16x32_bf16 v[164:167], a[208:211], a[0:3], v[200:203]\n\t"
    "v_exp_f32 v178, v178\n\t"
    "v_mfma_f32_16x16x32_bf16 v[164:167], a[212:215], a[4:7], v[164:167]\n\t"
    "v_exp_f32 v179, v179\n\t"
    "v_mfma_f32_16x16x32_bf16 v[164:167], a[216:219], a[8:11], v[164:167]\n\t"
    "v_exp_f32 v180, v180\n\t"
    "v_mfma_f32_16x16x32_bf16 v[164:167], a[220:223], a[12:15], v[164:167]\n\t"
    "v_exp_f32 v181, v181\n\t"
    "v_mfma_f32_16x16x32_bf16 v[168:171], a[192:195], a[16:19], v[204:207]\n\t"
    "v_exp_f32 v182, v182\n\t"
    "v_mfma_f32_16x16x32_bf16 v[168:171], a[196:199], a[20:23], v[168:171]\n\t"
    "v_exp_f32 v183, v183\n\t"
    "v_mfma_f32_16x16x32_bf16 v[168:171], a[200:203], a[24:27], v[168:171]\n\t"
    "buffer_load_dwordx4 v227, %3, s40 offen offset:0  lds\n\t"
    "v_mfma_f32_16x16x32_bf16 v[168:171], a[204:207], a[28:31], v[168:171]\n\t"
    "v_cvt_pk_bf16_f32 v176, v176, v177\n\t"
    "v_cvt_pk_bf16_f32 v177, v178, v179\n\t"
    "v_mfma_f32_16x16x32_bf16 v[172:175], a[208:211], a[16:19], v[204:207]\n\t"
    "v_cvt_pk_bf16_f32 v178, v180, v181\n\t"
    "v_cvt_pk_bf16_f32 v179, v182, v183\n\t"
    "v_mfma_f32_16x16x32_bf16 v[172:175], a[212:215], a[20:23], v[172:175]\n\t"
    "v_max3_f32 v229, v128, v129, v130\n\t"
    "v_max3_f32 v229, v229, v134, v135\n\t"
    "v_mfma_f32_16x16x32_bf16 v[172:175], a[216:219], a[24:27], v[172:175]\n\t"
    "v_max3_f32 v233, v136, v137, v138\n\t"
    "v_max3_f32 v241, v139, v140, v141\n\t"
    "v_mfma_f32_16x16x32_bf16 v[172:175], a[220:223], a[28:31], v[172:175]\n\t"
    "v_max3_f32 v233, v233, v142, v143\n\t"
    "v_max_f32 v233, v233, v241\n\t"
    "v_mfma_f32_16x16x32_bf16 v[208:211], v[252:255], v[144:147], v[208:211]\n\t"
    "buffer_load_dwordx4 v248, %3, s40 offen offset:1024  lds\n\t"
    "s_add_u32 m0, %8, 24576\n\t"
    "s_add_u32 s40, %9, 24576\n\t"
    "v_mfma_f32_16x16x32_bf16 v[212:215], v[252:255], v[152:155], v[212:215]\n\t"
    "v_mov_b32 v241, v233\n\t"
    "v_mfma_f32_16x16x32_bf16 v[0:3], a[128:131], v[144:147], v[0:3]\n\t"
    "ds_read_b128 a[96:99], v224 offset:0\n\t"
    "v_permlane16_swap_b32 v233, v241\n\t"
    "v_mfma_f32_16x16x32_bf16 v[4:7], a[132:135], v[144:147], v[4:7]\n\t"
    "ds_read_b128 a[100:103], v224 offset:1024\n\t"
    "v_max_f32 v233, v233, v241\n\t"
    "v_mfma_f32_16x16x32_bf16 v[8:11], a[136:139], v[144:147], v[8:11]\n\t"
    "ds_read_b128 a[104:107], v224 offset:2048\n\t"
    "v_mov_b32 v241, v233\n\t"
    "v_mfma_f32_16x16x32_bf16 v[12:15], a[140:143], v[144:147], v[12:15]\n\t"
    "ds_read_b128 a[108:111], v224 offset:3072\n\t"
    "v_permlane32_swap_b32 v233, v241\n\t"
    "v_mfma_f32_16x16x32_bf16 v[16:19], a[144:147], v[144:147], v[16:19]\n\t"
    "buffer_load_dwordx4 v227, %3, s40 offen offset:0  lds\n\t"
    "v_mfma_f32_16x16x32_bf16 v[20:23], a[148:151], v[144:147], v[20:23]\n\t"
    "ds_read_b128 a[112:115], v224 offset:4096\n\t"
    "v_min_f32_e64 v196, -v233, -v241\n\t"
    "v_mfma_f32_16x16x32_bf16 v[24:27], a[152:155], v[144:147], v[24:27]\n\t"
    "ds_read_b128 a[116:119], v224 offset:5120\n\t"
    "v_min_f32_e64 v197, -v233, -v241\n\t"
    "v_mfma_f32_16x16x32_bf16 v[28:31], a[156:159], v[144:147], v[28:31]\n\t"
    "ds_read_b128 a[120:123], v224 offset:6144\n\t"
    "v_min_f32_e64 v198, -v233, -v241\n\t"
    "v_mfma_f32_16x16x32_bf16 v[32:35], a[128:131], v[152:155], v[32:35]\n\t"
    "ds_read_b128 a[124:127], v224 offset:7168\n\t"
    "v_min_f32_e64 v199, -v233, -v241\n\t"
    "v_mfma_f32_16x16x32_bf16 v[36:39], a[132:135], v[152:155], v[36:39]\n\t"
    "v_exp_f32 v184, v184\n\t"
    "v_mfma_f32_16x16x32_bf16 v[40:43], a[136:139], v[152:155], v[40:43]\n\t"
    "buffer_load_dwordx4 v248, %3, s40 offen offset:1024  lds\n\t"
    "v_mfma_f32_16x16x32_bf16 v[44:47], a[140:143], v[152:155], v[44:47]\n\t"
    "v_exp_f32 v185, v185\n\t"
    "v_mfma_f32_16x16x32_bf16 v[48:51], a[144:147], v[152:155], v[48:51]\n\t"
    "v_exp_f32 v186, v186\n\t"
    "v_mfma_f32_16x16x32_bf16 v[52:55], a[148:151], v[152:155], v[52:55]\n\t"
    "v_exp_f32 v187, v187\n\t"
    "v_mfma_f32_16x16x32_bf16 v[56:59], a[152:155], v[152:155], v[56:59]\n\t"
    "v_exp_f32 v188, v188\n\t"
    "v_mfma_f32_16x16x32_bf16 v[60:63], a[156:159], v[152:155], v[60:63]\n\t"
    "v_exp_f32 v189, v189\n\t"
    "v_mfma_f32_16x16x32_bf16 v[64:67], a[128:131], v[176:179], v[64:67]\n\t"
    "v_exp_f32 v190, v190\n\t"
    "v_mfma_f32_16x16x32_bf16 v[68:71], a[132:135], v[176:179], v[68:71]\n\t"
    "v_exp_f32 v191, v191\n\t"
    "v_mfma_f32_16x16x32_bf16 v[72:75], a[136:139], v[176:179], v[72:75]\n\t"
    "v_cvt_pk_bf16_f32 v184, v184, v185\n\t"
    "v_cvt_pk_bf16_f32 v185, v186, v187\n\t"
    "v_mfma_f32_16x16x32_bf16 v[76:79], a[140:143], v[176:179], v[76:79]\n\t"
    "v_mov_b32 v233, v212\n\t"
    "v_cvt_pk_bf16_f32 v186, v188, v189\n\t"
    "v_mfma_f32_16x16x32_bf16 v[80:83], a[144:147], v[176:179], v[80:83]\n\t"
    "v_cvt_pk_bf16_f32 v187, v190, v191\n\t"
    "v_add_f32 v136, v196, v136\n\t"
    "v_mfma_f32_16x16x32_bf16 v[84:87], a[148:151], v[176:179], v[84:87]\n\t"
    "v_rcp_f32 v243, v233\n\t"
    "v_mfma_f32_16x16x32_bf16 v[88:91], a[152:155], v[176:179], v[88:91]\n\t"
    "v_mul_f32 v32, v243, v32\n\t"
    "v_mul_f32 v33, v243, v33\n\t"
    "v_mfma_f32_16x16x32_bf16 v[92:95], a[156:159], v[176:179], v[92:95]\n\t"
    "v_mul_f32 v34, v243, v34\n\t"
    "v_mul_f32 v35, v243, v35\n\t"
    "v_mfma_f32_16x16x32_bf16 v[96:99], a[128:131], v[184:187], v[96:99]\n\t"
    "ds_read_b64_tr_b16 a[128:129], v226 offset:0\n\t"
    "v_cvt_pk_bf16_f32 v32, v32, v33\n\t"
    "v_mfma_f32_16x16x32_bf16 v[100:103], a[132:135], v[184:187], v[100:103]\n\t"
    "ds_read_b64_tr_b16 a[130:131], v226 offset:4096\n\t"
    "v_cvt_pk_bf16_f32 v33, v34, v35\n\t"
    "v_mfma_f32_16x16x32_bf16 v[104:107], a[136:139], v[184:187], v[104:107]\n\t"
    "s_add_u32 m0, %10, 4096\n\t"
    "s_add_u32 s40, %11, 139392\n\t"
    "ds_read_b64_tr_b16 a[132:133], v225 offset:0\n\t"
    "v_mfma_f32_16x16x32_bf16 v[108:111], a[140:143], v[184:187], v[108:111]\n\t"
    "ds_read_b64_tr_b16 a[134:135], v225 offset:4096\n\t"
    "v_mul_f32 v36, v243, v36\n\t"
    "v_mfma_f32_16x16x32_bf16 v[112:115], a[144:147], v[184:187], v[112:115]\n\t"
    "ds_read_b64_tr_b16 a[136:137], v226 offset:512\n\t"
    "v_mul_f32 v37, v243, v37\n\t"
    "v_mfma_f32_16x16x32_bf16 v[116:119], a[148:151], v[184:187], v[116:119]\n\t"
    "ds_read_b64_tr_b16 a[138:139], v226 offset:4608\n\t"
    "v_mul_f32 v38, v243, v38\n\t"
    "v_mfma_f32_16x16x32_bf16 v[120:123], a[152:155], v[184:187], v[120:123]\n\t"
    "ds_read_b64_tr_b16 a[140:141], v225 offset:512\n\t"
    "v_mul_f32 v39, v243, v39\n\t"
    "v_mfma_f32_16x16x32_bf16 v[124:127], a[156:159], v[184:187], v[124:127]\n\t"
    "ds_read_b64_tr_b16 a[142:143], v225 offset:4608\n\t"
    "v_cvt_pk_bf16_f32 v36, v36, v37\n\t"
    "v_mfma_f32_16x16x32_bf16 v[216:219], v[252:255], v[176:179], v[216:219]\n\t"
    "ds_read_b64_tr_b16 a[144:145], v226 offset:1024\n\t"
    "v_cvt_pk_bf16_f32 v37, v38, v39\n\t"
    "v_mfma_f32_16x16x32_bf16 v[220:223], v[252:255], v[184:187], v[220:223]\n\t"
    "buffer_load_dwordx4 v228, %1, s40 offen offset:0 nt lds\n\t"
    "v_cmp_ngt_f32 vcc, s44, v232\n\t"
    "s_cmp_lg_u64 vcc, 0\n\t"
    "s_cselect_b32 s45, 2, 0\n\t"
    "s_or_b32 s41, s41, s45\n\t"
    "v_max3_f32 v232, v131, v132, v133\n\t"
    "v_max_f32 v229, v229, v232\n\t"
    "v_mov_b32 v232, v229\n\t"
    "v_mul_f32 v40, v243, v40\n\t"
    "v_mul_f32 v41, v243, v41\n\t"
    "v_permlane16_swap_b32 v229, v232\n\t"
    "v_max_f32 v229, v229, v232\n\t"
    "v_mov_b32 v232, v229\n\t"
    "v_mul_f32 v42, v243, v42\n\t"
    "v_mul_f32 v43, v243, v43\n\t"
    "v_permlane32_swap_b32 v229, v232\n\t"
    "v_min_f32_e64 v192, -v229, -v232\n\t"
    "v_min_f32_e64 v193, -v229, -v232\n\t"
    "v_min_f32_e64 v194, -v229, -v232\n\t"
    "v_min_f32_e64 v195, -v229, -v232\n\t"
    "v_mov_b32 v229, v208\n\t"
    "v_cvt_pk_bf16_f32 v40, v40, v41\n\t"
    "v_mfma_f32_16x16x32_bf16 v[144:147], a[224:227], a[32:35], v[192:195]\n\t"
    "ds_read_b64_tr_b16 a[146:147], v226 offset:5120\n\t"
    "v_cvt_pk_bf16_f32 v41, v42, v43\n\t"
    "v_mfma_f32_16x16x32_bf16 v[144:147], a[228:231], a[36:39], v[144:147]\n\t"
    "ds_read_b64_tr_b16 a[148:149], v225 offset:1024\n\t"
    "v_mul_f32 v44, v243, v44\n\t"
    "v_mfma_f32_16x16x32_bf16 v[144:147], a[232:235], a[40:43], v[144:147]\n\t"
    "ds_read_b64_tr_b16 a[150:151], v225 offset:5120\n\t"
    "v_mul_f32 v45, v243, v45\n\t"
    "v_mfma_f32_16x16x32_bf16 v[144:147], a[236:239], a[44:47], v[144:147]\n\t"
    "ds_read_b64_tr_b16 a[152:153], v226 offset:1536\n\t"
    "v_mul_f32 v46, v243, v46\n\t"
    "v_mfma_f32_16x16x32_bf16 v[148:151], a[240:243], a[32:35], v[192:195]\n\t"
    "ds_read_b64_tr_b16 a[154:155], v226 offset:5632\n\t"
    "v_mul_f32 v47, v243, v47\n\t"
    "v_mfma_f32_16x16x32_bf16 v[148:151], a[244:247], a[36:39], v[148:151]\n\t"
    "ds_read_b64_tr_b16 a[156:157], v225 offset:1536\n\t"
    "v_cvt_pk_bf16_f32 v44, v44, v45\n\t"
    "v_mfma_f32_16x16x32_bf16 v[148:151], a[248:251], a[40:43], v[148:151]\n\t"
    "ds_read_b64_tr_b16 a[158:159], v225 offset:5632\n\t"
    "v_cvt_pk_bf16_f32 v45, v46, v47\n\t"
    "v_mfma_f32_16x16x32_bf16 v[148:151], a[252:255], a[44:47], v[148:151]\n\t"
    "v_rcp_f32 v242, v229\n\t"
    "v_mfma_f32_16x16x32_bf16 v[152:155], a[224:227], a[48:51], v[196:199]\n\t"
    "v_mul_f32 v0, v242, v0\n\t"
    "v_mul_f32 v1, v242, v1\n\t"
    "v_mfma_f32_16x16x32_bf16 v[152:155], a[228:231], a[52:55], v[152:155]\n\t"
    "v_mul_f32 v2, v242, v2\n\t"
    "v_mul_f32 v3, v242, v3\n\t"
    "v_mfma_f32_16x16x32_bf16 v[152:155], a[232:235], a[56:59], v[152:155]\n\t"
    "v_cvt_pk_bf16_f32 v0, v0, v1\n\t"
    "v_cvt_pk_bf16_f32 v1, v2, v3\n\t"
    "ds_write_b64 v234, v[0:1] offset:0\n\t"
    "v_mfma_f32_16x16x32_bf16 v[152:155], a[236:239], a[60:63], v[152:155]\n\t"
    "v_mul_f32 v4, v242, v4\n\t"
    "v_mul_f32 v5, v242, v5\n\t"
    "v_mfma_f32_16x16x32_bf16 v[156:159], a[240:243], a[48:51], v[196:199]\n\t"
    "ds_write_b64 v234, v[32:33] offset:2048\n\t"
    "v_mfma_f32_16x16x32_bf16 v[156:159], a[244:247], a[52:55], v[156:159]\n\t"
    "v_mul_f32 v6, v242, v6\n\t"
    "v_mul_f32 v7, v242, v7\n\t"
    "v_mfma_f32_16x16x32_bf16 v[156:159], a[248:251], a[56:59], v[156:159]\n\t"
    "v_cvt_pk_bf16_f32 v4, v4, v5\n\t"
    "v_cvt_pk_bf16_f32 v5, v6, v7\n\t"
    "ds_write_b64 v237, v[4:5] offset:0\n\t"
    "v_mfma_f32_16x16x32_bf16 v[156:159], a[252:255], a[60:63], v[156:159]\n\t"
    "v_mul_f32 v8, v242, v8\n\t"
    "v_mul_f32 v9, v242, v9\n\t"
    "v_mfma_f32_16x16x32_bf16 v[176:179], a[224:227], a[0:3], v[200:203]\n\t"
    "ds_write_b64 v237, v[36:37] offset:2048\n\t"
    "v_mfma_f32_16x16x32_bf16 v[176:179], a[228:231], a[4:7], v[176:179]\n\t"
    "v_mul_f32 v10, v242, v10\n\t"
    "v_mul_f32 v11, v242, v11\n\t"
    "v_mfma_f32_16x16x32_bf16 v[176:179], a[232:235], a[8:11], v[176:179]\n\t"
    "v_cvt_pk_bf16_f32 v8, v8, v9\n\t"
    "v_cvt_pk_bf16_f32 v9, v10, v11\n\t"
    "ds_write_b64 v246, v[8:9] offset:0\n\t"
    "v_mfma_f32_16x16x32_bf16 v[176:179], a[236:239], a[12:15], v[176:179]\n\t"
    "v_mul_f32 v12, v242, v12\n\t"
    "v_mul_f32 v13, v242, v13\n\t"
    "v_mfma_f32_16x16x32_bf16 v[180:183], a[240:243], a[0:3], v[200:203]\n\t"
    "ds_write_b64 v246, v[40:41] offset:2048\n\t"
    "v_mfma_f32_16x16x32_bf16 v[180:183], a[244:247], a[4:7], v[180:183]\n\t"
    "v_mul_f32 v14, v242, v14\n\t"
    "v_mul_f32 v15, v242, v15\n\t"
    "v_mfma_f32_16x16x32_bf16 v[180:183], a[248:251], a[8:11], v[180:183]\n\t"
    "v_cvt_pk_bf16_f32 v12, v12, v13\n\t"
    "v_cvt_pk_bf16_f32 v13, v14, v15\n\t"
    "ds_write_b64 v247, v[12:13] offset:0\n\t"
    "v_mfma_f32_16x16x32_bf16 v[180:183], a[252:255], a[12:15], v[180:183]\n\t"
    "v_mul_f32 v16, v242, v16\n\t"
    "v_mul_f32 v17, v242, v17\n\t"
    "v_mfma_f32_16x16x32_bf16 v[184:187], a[224:227], a[16:19], v[204:207]\n\t"
    "ds_write_b64 v247, v[44:45] offset:2048\n\t"
    "v_mfma_f32_16x16x32_bf16 v[184:187], a[228:231], a[20:23], v[184:187]\n\t"
    "buffer_load_dwordx4 v249, %1, s40 offen offset:1024 nt lds\n\t"
    "v_mfma_f32_16x16x32_bf16 v[184:187], a[232:235], a[24:27], v[184:187]\n\t"
    "v_mul_f32 v18, v242, v18\n\t"
    "v_mul_f32 v19, v242, v19\n\t"
    "v_mfma_f32_16x16x32_bf16 v[184:187], a[236:239], a[28:31], v[184:187]\n\t"
    "v_cvt_pk_bf16_f32 v16, v16, v17\n\t"
    "v_cvt_pk_bf16_f32 v17, v18, v19\n\t"
    "v_mfma_f32_16x16x32_bf16 v[188:191], a[240:243], a[16:19], v[204:207]\n\t"
    "ds_read_b128 a[160:163], v235 offset:0\n\t"
    "v_mul_f32 v48, v243, v48\n\t"
    "v_mfma_f32_16x16x32_bf16 v[188:191], a[244:247], a[20:23], v[188:191]\n\t"
    "v_mul_f32 v49, v243, v49\n\t"
    "v_mul_f32 v50, v243, v50\n\t"
    "v_mfma_f32_16x16x32_bf16 v[188:191], a[248:251], a[24:27], v[188:191]\n\t"
    "ds_read2_b64 a[164:167], v235 offset0:129 offset1:128\n\t"
    "v_mul_f32 v51, v243, v51\n\t"
    "v_mfma_f32_16x16x32_bf16 v[188:191], a[252:255], a[28:31], v[188:191]\n\t"
    "s_waitcnt lgkmcnt(5)\n\t"
    "ds_read_b128 a[168:171], v235 offset:2048\n\t"
    "ds_read2_b64 a[172:175], v245 offset0:129 offset1:128\n\t"
    "buffer_load_dwordx4 v250, %1, s40 offen offset:2048 nt lds\n\t"
    "ds_write_b64 v234, v[16:17] offset:0\n\t"
    "v_cvt_pk_bf16_f32 v48, v48, v49\n\t"
    "v_exp_f32 v160, v160\n\t"
    "v_exp_f32 v161, v161\n\t"
    "v_cvt_pk_bf16_f32 v49, v50, v51\n\t"
    "v_exp_f32 v162, v162\n\t"
    "v_exp_f32 v163, v163\n\t"
    "ds_write_b64 v234, v[48:49] offset:2048\n\t"
    "v_exp_f32 v164, v164\n\t"
    "v_exp_f32 v165, v165\n\t"
    "v_exp_f32 v166, v166\n\t"
    "v_exp_f32 v167, v167\n\t"
    "v_cvt_pk_bf16_f32 v160, v160, v161\n\t"
    "v_cvt_pk_bf16_f32 v161, v162, v163\n\t"
    "v_cvt_pk_bf16_f32 v162, v164, v165\n\t"
    "v_cvt_pk_bf16_f32 v163, v166, v167\n\t"
    "v_mul_f32 v20, v242, v20\n\t"
    "v_mul_f32 v21, v242, v21\n\t"
    "v_mfma_f32_16x16x32_bf16 v[64:67], a[128:131], v[160:163], v[64:67]\n\t"
    "v_mul_f32 v22, v242, v22\n\t"
    "v_mul_f32 v23, v242, v23\n\t"
    "v_mfma_f32_16x16x32_bf16 v[68:71], a[132:135], v[160:163], v[68:71]\n\t"
    "v_cvt_pk_bf16_f32 v20, v20, v21\n\t"
    "v_cvt_pk_bf16_f32 v21, v22, v23\n\t"
    "v_mfma_f32_16x16x32_bf16 v[72:75], a[136:139], v[160:163], v[72:75]\n\t"
    "v_mul_f32 v52, v243, v52\n\t"
    "v_mul_f32 v53, v243, v53\n\t"
    "v_mfma_f32_16x16x32_bf16 v[76:79], a[140:143], v[160:163], v[76:79]\n\t"
    "v_mul_f32 v54, v243, v54\n\t"
    "v_mul_f32 v55, v243, v55\n\t"
    "v_mfma_f32_16x16x32_bf16 v[80:83], a[144:147], v[160:163], v[80:83]\n\t"
    "v_cvt_pk_bf16_f32 v52, v52, v53\n\t"
    "v_cvt_pk_bf16_f32 v53, v54, v55\n\t"
    "v_mfma_f32_16x16x32_bf16 v[84:87], a[148:151], v[160:163], v[84:87]\n\t"
    "ds_write_b64 v237, v[20:21] offset:0\n\t"
    "v_mfma_f32_16x16x32_bf16 v[88:91], a[152:155], v[160:163], v[88:91]\n\t"
    "v_mul_f32 v24, v242, v24\n\t"
    "v_mul_f32 v25, v242, v25\n\t"
    "v_mfma_f32_16x16x32_bf16 v[92:95], a[156:159], v[160:163], v[92:95]\n\t"
    "buffer_load_dwordx4 v251, %1, s40 offen offset:3072 nt lds\n\t"
    "ds_write_b64 v237, v[52:53] offset:2048\n\t"
    "v_mul_f32 v26, v242, v26\n\t"
    "v_mul_f32 v27, v242, v27\n\t"
    "v_cvt_pk_bf16_f32 v24, v24, v25\n\t"
    "v_cvt_pk_bf16_f32 v25, v26, v27\n\t"
    "ds_write_b64 v246, v[24:25] offset:0\n\t"
    "v_exp_f32 v168, v168\n\t"
    "v_exp_f32 v169, v169\n\t"
    "v_exp_f32 v170, v170\n\t"
    "v_exp_f32 v171, v171\n\t"
    "v_exp_f32 v172, v172\n\t"
    "v_exp_f32 v173, v173\n\t"
    "v_exp_f32 v174, v174\n\t"
    "v_exp_f32 v175, v175\n\t"
    "v_cvt_pk_bf16_f32 v168, v168, v169\n\t"
    "v_cvt_pk_bf16_f32 v169, v170, v171\n\t"
    "v_cvt_pk_bf16_f32 v170, v172, v173\n\t"
    "v_cvt_pk_bf16_f32 v171, v174, v175\n\t"
    "v_mul_f32 v56, v243, v56\n\t"
    "v_mul_f32 v57, v243, v57\n\t"
    "v_mfma_f32_16x16x32_bf16 v[96:99], a[128:131], v[168:171], v[96:99]\n\t"
    "v_mul_f32 v58, v243, v58\n\t"
    "v_mul_f32 v59, v243, v59\n\t"
    "v_mfma_f32_16x16x32_bf16 v[100:103], a[132:135], v[168:171], v[100:103]\n\t"
    "v_cvt_pk_bf16_f32 v56, v56, v57\n\t"
    "v_cvt_pk_bf16_f32 v57, v58, v59\n\t"
    "ds_write_b64 v246, v[56:57] offset:2048\n\t"
    "v_mfma_f32_16x16x32_bf16 v[104:107], a[136:139], v[168:171], v[104:107]\n\t"
    "v_mul_f32 v28, v242, v28\n\t"
    "v_mul_f32 v29, v242, v29\n\t"
    "v_mfma_f32_16x16x32_bf16 v[108:111], a[140:143], v[168:171], v[108:111]\n\t"
    "v_mul_f32 v30, v242, v30\n\t"
    "v_mul_f32 v31, v242, v31\n\t"
    "v_mfma_f32_16x16x32_bf16 v[112:115], a[144:147], v[168:171], v[112:115]\n\t"
    "v_cvt_pk_bf16_f32 v28, v28, v29\n\t"
    "v_cvt_pk_bf16_f32 v29, v30, v31\n\t"
    "ds_write_b64 v247, v[28:29] offset:0\n\t"
    "v_mfma_f32_16x16x32_bf16 v[116:119], a[148:151], v[168:171], v[116:119]\n\t"
    "v_mul_f32 v60, v243, v60\n\t"
    "v_mul_f32 v61, v243, v61\n\t"
    "v_mfma_f32_16x16x32_bf16 v[120:123], a[152:155], v[168:171], v[120:123]\n\t"
    "v_mul_f32 v62, v243, v62\n\t"
    "v_mul_f32 v63, v243, v63\n\t"
    "v_mfma_f32_16x16x32_bf16 v[124:127], a[156:159], v[168:171], v[124:127]\n\t"
    "v_cvt_pk_bf16_f32 v60, v60, v61\n\t"
    "v_cvt_pk_bf16_f32 v61, v62, v63\n\t"
    "ds_write_b64 v247, v[60:61] offset:2048\n\t"
    "v_mfma_f32_16x16x32_bf16 v[216:219], v[252:255], v[160:163], v[216:219]\n\t"
    "v_add_f32 v128, v192, v128\n\t"
    "v_add_f32 v129, v193, v129\n\t"
    "v_mfma_f32_16x16x32_bf16 v[220:223], v[252:255], v[168:171], v[220:223]\n\t"
    "v_add_f32 v130, v194, v130\n\t"
    "v_add_f32 v131, v195, v131\n\t"
    "v_mfma_f32_16x16x32_bf16 v[160:163], a[96:99], a[0:3], v[200:203]\n\t"
    "v_exp_f32 v128, v128\n\t"
    "v_mfma_f32_16x16x32_bf16 v[160:163], a[100:103], a[4:7], v[160:163]\n\t"
    "ds_read_b128 a[176:179], v235 offset:0\n\t"
    "v_add_f32 v132, v192, v132\n\t"
    "v_mfma_f32_16x16x32_bf16 v[160:163], a[104:107], a[8:11], v[160:163]\n\t"
    "v_exp_f32 v129, v129\n\t"
    "v_mfma_f32_16x16x32_bf16 v[160:163], a[108:111], a[12:15], v[160:163]\n\t"
    "ds_read2_b64 a[180:183], v235 offset0:129 offset1:128\n\t"
    "v_add_f32 v133, v193, v133\n\t"
    "v_mfma_f32_16x16x32_bf16 v[164:167], a[112:115], a[0:3], v[200:203]\n\t"
    "v_add_f32 v134, v194, v134\n\t"
    "v_add_f32 v135, v195, v135\n\t"
    "v_mfma_f32_16x16x32_bf16 v[164:167], a[116:119], a[4:7], v[164:167]\n\t"
    "ds_read_b128 a[184:187], v235 offset:2048\n\t"
    "v_cvt_pk_bf16_f32 v128, v128, v129\n\t"
    "v_mfma_f32_16x16x32_bf16 v[164:167], a[120:123], a[8:11], v[164:167]\n\t"
    "v_exp_f32 v130, v130\n\t"
    "v_mfma_f32_16x16x32_bf16 v[164:167], a[124:127], a[12:15], v[164:167]\n\t"
    "ds_read2_b64 a[188:191], v245 offset0:129 offset1:128\n\t"
    "v_add_f32 v137, v197, v137\n\t"
    "v_mfma_f32_16x16x32_bf16 v[168:171], a[96:99], a[16:19], v[204:207]\n\t"
    "v_exp_f32 v131, v131\n\t"
    "v_mfma_f32_16x16x32_bf16 v[168:171], a[100:103], a[20:23], v[168:171]\n\t"
    "v_exp_f32 v132, v132\n\t"
    "v_mfma_f32_16x16x32_bf16 v[168:171], a[104:107], a[24:27], v[168:171]\n\t"
    "v_exp_f32 v133, v133\n\t"
    "v_mfma_f32_16x16x32_bf16 v[168:171], a[108:111], a[28:31], v[168:171]\n\t"
    "v_exp_f32 v134, v134\n\t"
    "v_mfma_f32_16x16x32_bf16 v[172:175], a[112:115], a[16:19], v[204:207]\n\t"
    "v_exp_f32 v135, v135\n\t"
    "v_mfma_f32_16x16x32_bf16 v[172:175], a[116:119], a[20:23], v[172:175]\n\t"
    "v_cvt_pk_bf16_f32 v129, v130, v131\n\t"
    "v_cvt_pk_bf16_f32 v130, v132, v133\n\t"
    "v_mfma_f32_16x16x32_bf16 v[172:175], a[120:123], a[24:27], v[172:175]\n\t"
    "v_cvt_pk_bf16_f32 v131, v134, v135\n\t"
    "v_add_f32 v138, v198, v138\n\t"
    "v_mfma_f32_16x16x32_bf16 v[172:175], a[124:127], a[28:31], v[172:175]\n\t"
    "v_add_f32 v139, v199, v139\n\t"
    "v_add_f32 v140, v196, v140\n\t"
    "v_mfma_f32_16x16x32_bf16 v[0:3], a[128:131], v[128:131], 0\n\t"
    "v_exp_f32 v136, v136\n\t"
    "v_mfma_f32_16x16x32_bf16 v[4:7], a[132:135], v[128:131], 0\n\t"
    "v_exp_f32 v137, v137\n\t"
    "v_mfma_f32_16x16x32_bf16 v[8:11], a[136:139], v[128:131], 0\n\t"
    "v_add_f32 v141, v197, v141\n\t"
    "v_add_f32 v142, v198, v142\n\t"
    "v_mfma_f32_16x16x32_bf16 v[12:15], a[140:143], v[128:131], 0\n\t"
    "v_add_f32 v143, v199, v143\n\t"
    "v_cvt_pk_bf16_f32 v136, v136, v137\n\t"
    "v_mfma_f32_16x16x32_bf16 v[16:19], a[144:147], v[128:131], 0\n\t"
    "v_exp_f32 v138, v138\n\t"
    "v_mfma_f32_16x16x32_bf16 v[20:23], a[148:151], v[128:131], 0\n\t"
    "v_exp_f32 v139, v139\n\t"
    "v_mfma_f32_16x16x32_bf16 v[24:27], a[152:155], v[128:131], 0\n\t"
    "v_exp_f32 v140, v140\n\t"
    "v_mfma_f32_16x16x32_bf16 v[28:31], a[156:159], v[128:131], 0\n\t"
    "v_exp_f32 v141, v141\n\t"
    "v_exp_f32 v142, v142\n\t"
    "v_exp_f32 v143, v143\n\t"
    "v_cvt_pk_bf16_f32 v137, v138, v139\n\t"
    "v_cvt_pk_bf16_f32 v138, v140, v141\n\t"
    "v_cvt_pk_bf16_f32 v139, v142, v143\n\t"
    "v_exp_f32 v144, v144\n\t"
    "s_nop 0\n\t"
    "v_mfma_f32_16x16x32_bf16 v[32:35], a[128:131], v[136:139], 0\n\t"
    "ds_read_b64_tr_b16 a[128:129], v226 offset:8192\n\t"
    "v_add_f32 v232, v229, v233\n\t"
    "v_mfma_f32_16x16x32_bf16 v[36:39], a[132:135], v[136:139], 0\n\t"
    "ds_read_b64_tr_b16 a[130:131], v226 offset:12288\n\t"
    "ds_read_b64_tr_b16 a[132:133], v225 offset:8192\n\t"
    "ds_read_b64_tr_b16 a[134:135], v225 offset:12288\n\t"
    "v_mfma_f32_16x16x32_bf16 v[40:43], a[136:139], v[136:139], 0\n\t"
    "ds_read_b64_tr_b16 a[136:137], v226 offset:8704\n\t"
    "ds_read_b64_tr_b16 a[138:139], v226 offset:12800\n\t"
    "v_mfma_f32_16x16x32_bf16 v[44:47], a[140:143], v[136:139], 0\n\t"
    "ds_read_b64_tr_b16 a[140:141], v225 offset:8704\n\t"
    "ds_read_b64_tr_b16 a[142:143], v225 offset:12800\n\t"
    "v_mfma_f32_16x16x32_bf16 v[48:51], a[144:147], v[136:139], 0\n\t"
    "ds_read_b64_tr_b16 a[144:145], v226 offset:9216\n\t"
    "ds_read_b64_tr_b16 a[146:147], v226 offset:13312\n\t"
    "v_mfma_f32_16x16x32_bf16 v[52:55], a[148:151], v[136:139], 0\n\t"
    "ds_read_b64_tr_b16 a[148:149], v225 offset:9216\n\t"
    "ds_read_b64_tr_b16 a[150:151], v225 offset:13312\n\t"
    "v_mfma_f32_16x16x32_bf16 v[56:59], a[152:155], v[136:139], 0\n\t"
    "ds_read_b64_tr_b16 a[152:153], v226 offset:9728\n\t"
    "ds_read_b64_tr_b16 a[154:155], v226 offset:13824\n\t"
    "v_mfma_f32_16x16x32_bf16 v[60:63], a[156:159], v[136:139], 0\n\t"
    "ds_read_b64_tr_b16 a[156:157], v225 offset:9728\n\t"
    "ds_read_b64_tr_b16 a[158:159], v225 offset:13824\n\t"
    "v_mfma_f32_16x16x32_bf16 v[208:211], v[252:255], v[128:131], 0\n\t"
    "v_exp_f32 v145, v145\n\t"
    "v_mfma_f32_16x16x32_bf16 v[212:215], v[252:255], v[136:139], 0\n\t"
    "v_exp_f32 v146, v146\n\t"
    "v_mfma_f32_16x16x32_bf16 v[128:131], a[96:99], a[32:35], v[192:195]\n\t"
    "v_exp_f32 v147, v147\n\t"
    "v_mfma_f32_16x16x32_bf16 v[128:131], a[100:103], a[36:39], v[128:131]\n\t"
    "v_exp_f32 v148, v148\n\t"
    "v_mfma_f32_16x16x32_bf16 v[128:131], a[104:107], a[40:43], v[128:131]\n\t"
    "v_exp_f32 v149, v149\n\t"
    "v_mfma_f32_16x16x32_bf16 v[128:131], a[108:111], a[44:47], v[128:131]\n\t"
    "v_exp_f32 v150, v150\n\t"
    "v_mfma_f32_16x16x32_bf16 v[132:135], a[112:115], a[32:35], v[192:195]\n\t"
    "v_exp_f32 v151, v151\n\t"
    "v_mfma_f32_16x16x32_bf16 v[132:135], a[116:119], a[36:39], v[132:135]\n\t"
    "v_cvt_pk_bf16_f32 v144, v144, v145\n\t"
    "v_cvt_pk_bf16_f32 v145, v146, v147\n\t"
    "v_mfma_f32_16x16x32_bf16 v[132:135], a[120:123], a[40:43], v[132:135]\n\t"
    "s_waitcnt lgkmcnt(0)\n\t"
    "v_cvt_pk_bf16_f32 v146, v148, v149\n\t"
    "v_cvt_pk_bf16_f32 v147, v150, v151\n\t"
    "s_waitcnt vmcnt(4)\n\t"
    "s_barrier\n\t"
    "s_add_u32 m0, %8, 0\n\t"
    "s_add_u32 s40, %9, 32768\n\t"
    "v_mfma_f32_16x16x32_bf16 v[132:135], a[124:127], a[44:47], v[132:135]\n\t"
    "v_exp_f32 v152, v152\n\t"
    "v_mfma_f32_16x16x32_bf16 v[136:139], a[96:99], a[48:51], v[196:199]\n\t"
    "ds_read_b128 a[96:99], v224 offset:8192\n\t"
    "v_mfma_f32_16x16x32_bf16 v[136:139], a[100:103], a[52:55], v[136:139]\n\t"
    "ds_read_b128 a[100:103], v224 offset:9216\n\t"
    "v_mfma_f32_16x16x32_bf16 v[136:139], a[104:107], a[56:59], v[136:139]\n\t"
    "buffer_load_dwordx4 v227, %3, s40 offen offset:0  lds\n\t"
    "v_mfma_f32_16x16x32_bf16 v[136:139], a[108:111], a[60:63], v[136:139]\n\t"
    "ds_read_b128 a[104:107], v224 offset:10240\n\t"
    "v_mfma_f32_16x16x32_bf16 v[140:143], a[112:115], a[48:51], v[196:199]\n\t"
    "ds_read_b128 a[108:111], v224 offset:11264\n\t"
    "v_mfma_f32_16x16x32_bf16 v[140:143], a[116:119], a[52:55], v[140:143]\n\t"
    "ds_read_b128 a[112:115], v224 offset:12288\n\t"
    "v_mfma_f32_16x16x32_bf16 v[140:143], a[120:123], a[56:59], v[140:143]\n\t"
    "ds_read_b128 a[116:119], v224 offset:13312\n\t"
    "v_mfma_f32_16x16x32_bf16 v[140:143], a[124:127], a[60:63], v[140:143]\n\t"
    "ds_read_b128 a[120:123], v224 offset:14336\n\t"
    "v_mfma_f32_16x16x32_bf16 v[0:3], a[128:131], v[144:147], v[0:3]\n\t"
    "buffer_load_dwordx4 v248, %3, s40 offen offset:1024  lds\n\t"
    "s_add_u32 m0, %8, 8192\n\t"
    "s_add_u32 s40, %9, 40960\n\t"
    "v_mfma_f32_16x16x32_bf16 v[4:7], a[132:135], v[144:147], v[4:7]\n\t"
    "ds_read_b128 a[124:127], v224 offset:15360\n\t"
    "v_mfma_f32_16x16x32_bf16 v[8:11], a[136:139], v[144:147], v[8:11]\n\t"
    "v_exp_f32 v153, v153\n\t"
    "v_mfma_f32_16x16x32_bf16 v[12:15], a[140:143], v[144:147], v[12:15]\n\t"
    "v_exp_f32 v154, v154\n\t"
    "v_mfma_f32_16x16x32_bf16 v[16:19], a[144:147], v[144:147], v[16:19]\n\t"
    "v_exp_f32 v155, v155\n\t"
    "v_mfma_f32_16x16x32_bf16 v[20:23], a[148:151], v[144:147], v[20:23]\n\t"
    "v_exp_f32 v156, v156\n\t"
    "v_mfma_f32_16x16x32_bf16 v[24:27], a[152:155], v[144:147], v[24:27]\n\t"
    "buffer_load_dwordx4 v227, %3, s40 offen offset:0  lds\n\t"
    "v_mfma_f32_16x16x32_bf16 v[28:31], a[156:159], v[144:147], v[28:31]\n\t"
    "buffer_load_dwordx4 v248, %3, s40 offen offset:1024  lds\n\t"
    "v_exp_f32 v157, v157\n\t"
    "v_exp_f32 v158, v158\n\t"
    "v_exp_f32 v159, v159\n\t"
    "v_cvt_pk_bf16_f32 v152, v152, v153\n\t"
    "v_cvt_pk_bf16_f32 v153, v154, v155\n\t"
    "v_cvt_pk_bf16_f32 v154, v156, v157\n\t"
    "v_cvt_pk_bf16_f32 v155, v158, v159\n\t"
    "v_exp_f32 v176, v176\n\t"
    "s_nop 0\n\t"
    "v_mfma_f32_16x16x32_bf16 v[32:35], a[128:131], v[152:155], v[32:35]\n\t"
    "v_exp_f32 v177, v177\n\t"
    "v_mfma_f32_16x16x32_bf16 v[36:39], a[132:135], v[152:155], v[36:39]\n\t"
    "v_exp_f32 v178, v178\n\t"
    "v_mfma_f32_16x16x32_bf16 v[40:43], a[136:139], v[152:155], v[40:43]\n\t"
    "v_exp_f32 v179, v179\n\t"
    "v_mfma_f32_16x16x32_bf16 v[44:47], a[140:143], v[152:155], v[44:47]\n\t"
    "v_exp_f32 v180, v180\n\t"
    "v_mfma_f32_16x16x32_bf16 v[48:51], a[144:147], v[152:155], v[48:51]\n\t"
    "v_exp_f32 v181, v181\n\t"
    "v_mfma_f32_16x16x32_bf16 v[52:55], a[148:151], v[152:155], v[52:55]\n\t"
    "v_exp_f32 v182, v182\n\t"
    "v_mfma_f32_16x16x32_bf16 v[56:59], a[152:155], v[152:155], v[56:59]\n\t"
    "v_exp_f32 v183, v183\n\t"
    "v_mfma_f32_16x16x32_bf16 v[60:63], a[156:159], v[152:155], v[60:63]\n\t"
    "v_cvt_pk_bf16_f32 v176, v176, v177\n\t"
    "v_cvt_pk_bf16_f32 v177, v178, v179\n\t"
    "v_mfma_f32_16x16x32_bf16 v[208:211], v[252:255], v[144:147], v[208:211]\n\t"
    "v_cvt_pk_bf16_f32 v178, v180, v181\n\t"
    "v_cvt_pk_bf16_f32 v179, v182, v183\n\t"
    "v_mfma_f32_16x16x32_bf16 v[212:215], v[252:255], v[152:155], v[212:215]\n\t"
    "v_exp_f32 v184, v184\n\t"
    "v_mfma_f32_16x16x32_bf16 v[64:67], a[128:131], v[176:179], v[64:67]\n\t"
    "v_exp_f32 v185, v185\n\t"
    "v_mfma_f32_16x16x32_bf16 v[68:71], a[132:135], v[176:179], v[68:71]\n\t"
    "v_exp_f32 v186, v186\n\t"
    "v_mfma_f32_16x16x32_bf16 v[72:75], a[136:139], v[176:179], v[72:75]\n\t"
    "v_exp_f32 v187, v187\n\t"
    "v_mfma_f32_16x16x32_bf16 v[76:79], a[140:143], v[176:179], v[76:79]\n\t"
    "v_exp_f32 v188, v188\n\t"
    "v_mfma_f32_16x16x32_bf16 v[80:83], a[144:147], v[176:179], v[80:83]\n\t"
    "v_exp_f32 v189, v189\n\t"
    "v_mfma_f32_16x16x32_bf16 v[84:87], a[148:151], v[176:179], v[84:87]\n\t"
    "v_exp_f32 v190, v190\n\t"
    "v_mfma_f32_16x16x32_bf16 v[88:91], a[152:155], v[176:179], v[88:91]\n\t"
    "ds_read_b128 a[64:67], v230 offset:0\n\t"
    "v_cvt_pk_bf16_f32 v184, v184, v185\n\t"
    "v_mfma_f32_16x16x32_bf16 v[92:95], a[156:159], v[176:179], v[92:95]\n\t"
    "ds_read_b128 a[68:71], v231 offset:0\n\t"
    "v_exp_f32 v191, v191\n\t"
    "v_cvt_pk_bf16_f32 v185, v186, v187\n\t"
    "v_cvt_pk_bf16_f32 v186, v188, v189\n\t"
    "v_cvt_pk_bf16_f32 v187, v190, v191\n\t"
    "v_exp_f32 v128, v128\n\t"
    "s_nop 0\n\t"
    "v_mfma_f32_16x16x32_bf16 v[96:99], a[128:131], v[184:187], v[96:99]\n\t"
    "ds_read_b128 a[80:83], v230 offset:2048\n\t"
    "v_mfma_f32_16x16x32_bf16 v[100:103], a[132:135], v[184:187], v[100:103]\n\t"
    "ds_read_b128 a[84:87], v231 offset:2048\n\t"
    "v_mfma_f32_16x16x32_bf16 v[104:107], a[136:139], v[184:187], v[104:107]\n\t"
    "s_add_u32 s40, %11, 65536\n\t"
    "ds_read_b64_tr_b16 a[128:129], v226 offset:16384\n\t"
    "v_mfma_f32_16x16x32_bf16 v[108:111], a[140:143], v[184:187], v[108:111]\n\t"
    "ds_read_b64_tr_b16 a[130:131], v226 offset:20480\n\t"
    "v_mfma_f32_16x16x32_bf16 v[112:115], a[144:147], v[184:187], v[112:115]\n\t"
    "ds_read_b64_tr_b16 a[132:133], v225 offset:16384\n\t"
    "v_mfma_f32_16x16x32_bf16 v[116:119], a[148:151], v[184:187], v[116:119]\n\t"
    "ds_read_b64_tr_b16 a[134:135], v225 offset:20480\n\t"
    "v_mfma_f32_16x16x32_bf16 v[120:123], a[152:155], v[184:187], v[120:123]\n\t"
    "ds_read_b64_tr_b16 a[136:137], v226 offset:16896\n\t"
    "v_mfma_f32_16x16x32_bf16 v[124:127], a[156:159], v[184:187], v[124:127]\n\t"
    "ds_read_b64_tr_b16 a[138:139], v226 offset:20992\n\t"
    "v_mfma_f32_16x16x32_bf16 v[216:219], v[252:255], v[176:179], v[216:219]\n\t"
    "ds_read_b64_tr_b16 a[140:141], v225 offset:16896\n\t"
    "v_mfma_f32_16x16x32_bf16 v[220:223], v[252:255], v[184:187], v[220:223]\n\t"
    "s_waitcnt lgkmcnt(11)\n\t"
    "ds_read_b64_tr_b16 a[142:143], v225 offset:20992\n\t"
    "v_mfma_f32_16x16x32_bf16 v[144:147], a[96:99], a[32:35], v[192:195]\n\t"
    "ds_read_b64_tr_b16 a[144:145], v226 offset:17408\n\t"
    "v_mfma_f32_16x16x32_bf16 v[144:147], a[100:103], a[36:39], v[144:147]\n\t"
    "ds_read_b64_tr_b16 a[146:147], v226 offset:21504\n\t"
    "v_mfma_f32_16x16x32_bf16 v[144:147], a[104:107], a[40:43], v[144:147]\n\t"
    "ds_read_b64_tr_b16 a[148:149], v225 offset:17408\n\t"
    "v_mfma_f32_16x16x32_bf16 v[144:147], a[108:111], a[44:47], v[144:147]\n\t"
    "ds_read_b64_tr_b16 a[150:151], v225 offset:21504\n\t"
    "v_mfma_f32_16x16x32_bf16 v[148:151], a[112:115], a[32:35], v[192:195]\n\t"
    "ds_read_b64_tr_b16 a[152:153], v226 offset:17920\n\t"
    "v_mfma_f32_16x16x32_bf16 v[148:151], a[116:119], a[36:39], v[148:151]\n\t"
    "ds_read_b64_tr_b16 a[154:155], v226 offset:22016\n\t"
    "v_mfma_f32_16x16x32_bf16 v[148:151], a[120:123], a[40:43], v[148:151]\n\t"
    "ds_read_b64_tr_b16 a[156:157], v225 offset:17920\n\t"
    "v_mfma_f32_16x16x32_bf16 v[148:151], a[124:127], a[44:47], v[148:151]\n\t"
    "ds_read_b64_tr_b16 a[158:159], v225 offset:22016\n\t"
    "v_mfma_f32_16x16x32_bf16 v[152:155], a[96:99], a[48:51], v[196:199]\n\t"
    "v_exp_f32 v129, v129\n\t"
    "v_mfma_f32_16x16x32_bf16 v[152:155], a[100:103], a[52:55], v[152:155]\n\t"
    "v_exp_f32 v130, v130\n\t"
    "v_mfma_f32_16x16x32_bf16 v[152:155], a[104:107], a[56:59], v[152:155]\n\t"
    "v_exp_f32 v131, v131\n\t"
    "v_mfma_f32_16x16x32_bf16 v[152:155], a[108:111], a[60:63], v[152:155]\n\t"
    "v_exp_f32 v132, v132\n\t"
    "v_mfma_f32_16x16x32_bf16 v[156:159], a[112:115], a[48:51], v[196:199]\n\t"
    "v_exp_f32 v133, v133\n\t"
    "v_mfma_f32_16x16x32_bf16 v[156:159], a[116:119], a[52:55], v[156:159]\n\t"
    "v_exp_f32 v134, v134\n\t"
    "v_mfma_f32_16x16x32_bf16 v[156:159], a[120:123], a[56:59], v[156:159]\n\t"
    "v_exp_f32 v135, v135\n\t"
    "v_mfma_f32_16x16x32_bf16 v[156:159], a[124:127], a[60:63], v[156:159]\n\t"
    "v_cvt_pk_bf16_f32 v128, v128, v129\n\t"
    "v_cvt_pk_bf16_f32 v129, v130, v131\n\t"
    "v_mfma_f32_16x16x32_bf16 v[176:179], a[96:99], a[0:3], v[200:203]\n\t"
    "v_cvt_pk_bf16_f32 v130, v132, v133\n\t"
    "v_cvt_pk_bf16_f32 v131, v134, v135\n\t"
    "v_mfma_f32_16x16x32_bf16 v[176:179], a[100:103], a[4:7], v[176:179]\n\t"
    "buffer_store_dwordx4 a[160:163], v236, %4, s40 offen offset:0 sc1\n\t"
    "v_mfma_f32_16x16x32_bf16 v[176:179], a[104:107], a[8:11], v[176:179]\n\t"
    "v_exp_f32 v136, v136\n\t"
    "v_mfma_f32_16x16x32_bf16 v[176:179], a[108:111], a[12:15], v[176:179]\n\t"
    "v_exp_f32 v137, v137\n\t"
    "v_mfma_f32_16x16x32_bf16 v[180:183], a[112:115], a[0:3], v[200:203]\n\t"
    "v_exp_f32 v138, v138\n\t"
    "v_mfma_f32_16x16x32_bf16 v[180:183], a[116:119], a[4:7], v[180:183]\n\t"
    "v_exp_f32 v139, v139\n\t"
    "v_mfma_f32_16x16x32_bf16 v[180:183], a[120:123], a[8:11], v[180:183]\n\t"
    "v_exp_f32 v140, v140\n\t"
    "v_mfma_f32_16x16x32_bf16 v[180:183], a[124:127], a[12:15], v[180:183]\n\t"
    "v_exp_f32 v141, v141\n\t"
    "v_mfma_f32_16x16x32_bf16 v[184:187], a[96:99], a[16:19], v[204:207]\n\t"
    "ds_read_b128 a[96:99], v224 offset:16384\n\t"
    "buffer_store_dwordx4 a[164:167], v236, %4, s40 offen offset:2048 sc1\n\t"
    "s_add_u32 s40, %11, 69632\n\t"
    "v_mfma_f32_16x16x32_bf16 v[184:187], a[100:103], a[20:23], v[184:187]\n\t"
    "ds_read_b128 a[100:103], v224 offset:17408\n\t"
    "v_cvt_pk_bf16_f32 v136, v136, v137\n\t"
    "v_mfma_f32_16x16x32_bf16 v[184:187], a[104:107], a[24:27], v[184:187]\n\t"
    "ds_read_b128 a[104:107], v224 offset:18432\n\t"
    "v_cvt_pk_bf16_f32 v137, v138, v139\n\t"
    "v_mfma_f32_16x16x32_bf16 v[184:187], a[108:111], a[28:31], v[184:187]\n\t"
    "ds_read_b128 a[108:111], v224 offset:19456\n\t"
    "v_cvt_pk_bf16_f32 v138, v140, v141\n\t"
    "v_mfma_f32_16x16x32_bf16 v[188:191], a[112:115], a[16:19], v[204:207]\n\t"
    "ds_read_b128 a[112:115], v224 offset:20480\n\t"
    "v_mfma_f32_16x16x32_bf16 v[188:191], a[116:119], a[20:23], v[188:191]\n\t"
    "ds_read_b128 a[116:119], v224 offset:21504\n\t"
    "v_mfma_f32_16x16x32_bf16 v[188:191], a[120:123], a[24:27], v[188:191]\n\t"
    "ds_read_b128 a[120:123], v224 offset:22528\n\t"
    "v_mfma_f32_16x16x32_bf16 v[188:191], a[124:127], a[28:31], v[188:191]\n\t"
    "s_waitcnt lgkmcnt(7)\n\t"
    "ds_read_b128 a[124:127], v224 offset:23552\n\t"
    "v_mfma_f32_16x16x32_bf16 v[0:3], a[128:131], v[128:131], v[0:3]\n\t"
    "v_exp_f32 v142, v142\n\t"
    "v_mfma_f32_16x16x32_bf16 v[4:7], a[132:135], v[128:131], v[4:7]\n\t"
    "buffer_store_dwordx4 a[168:171], v236, %4, s40 offen offset:0 sc1\n\t"
    "v_mfma_f32_16x16x32_bf16 v[8:11], a[136:139], v[128:131], v[8:11]\n\t"
    "v_exp_f32 v143, v143\n\t"
    "v_mfma_f32_16x16x32_bf16 v[12:15], a[140:143], v[128:131], v[12:15]\n\t"
    "v_cvt_pk_bf16_f32 v139, v142, v143\n\t"
    "v_mfma_f32_16x16x32_bf16 v[16:19], a[144:147], v[128:131], v[16:19]\n\t"
    "v_exp_f32 v160, v160\n\t"
    "v_mfma_f32_16x16x32_bf16 v[20:23], a[148:151], v[128:131], v[20:23]\n\t"
    "v_exp_f32 v161, v161\n\t"
    "v_mfma_f32_16x16x32_bf16 v[24:27], a[152:155], v[128:131], v[24:27]\n\t"
    "v_exp_f32 v162, v162\n\t"
    "v_mfma_f32_16x16x32_bf16 v[28:31], a[156:159], v[128:131], v[28:31]\n\t"
    "v_exp_f32 v163, v163\n\t"
    "v_mfma_f32_16x16x32_bf16 v[32:35], a[128:131], v[136:139], v[32:35]\n\t"
    "v_exp_f32 v164, v164\n\t"
    "v_mfma_f32_16x16x32_bf16 v[36:39], a[132:135], v[136:139], v[36:39]\n\t"
    "buffer_store_dwordx4 a[172:175], v236, %4, s40 offen offset:2048 sc1\n\t"
    "v_mfma_f32_16x16x32_bf16 v[40:43], a[136:139], v[136:139], v[40:43]\n\t"
    "v_exp_f32 v165, v165\n\t"
    "v_mfma_f32_16x16x32_bf16 v[44:47], a[140:143], v[136:139], v[44:47]\n\t"
    "v_exp_f32 v166, v166\n\t"
    "v_mfma_f32_16x16x32_bf16 v[48:51], a[144:147], v[136:139], v[48:51]\n\t"
    "v_exp_f32 v167, v167\n\t"
    "v_mfma_f32_16x16x32_bf16 v[52:55], a[148:151], v[136:139], v[52:55]\n\t"
    "v_cvt_pk_bf16_f32 v160, v160, v161\n\t"
    "v_cvt_pk_bf16_f32 v161, v162, v163\n\t"
    "v_mfma_f32_16x16x32_bf16 v[56:59], a[152:155], v[136:139], v[56:59]\n\t"
    "v_cvt_pk_bf16_f32 v162, v164, v165\n\t"
    "v_cvt_pk_bf16_f32 v163, v166, v167\n\t"
    "v_mfma_f32_16x16x32_bf16 v[60:63], a[156:159], v[136:139], v[60:63]\n\t"
    "v_exp_f32 v168, v168\n\t"
    "v_mfma_f32_16x16x32_bf16 v[208:211], v[252:255], v[128:131], v[208:211]\n\t"
    "v_exp_f32 v169, v169\n\t"
    "v_mfma_f32_16x16x32_bf16 v[212:215], v[252:255], v[136:139], v[212:215]\n\t"
    "v_exp_f32 v170, v170\n\t"
    "v_mfma_f32_16x16x32_bf16 v[64:67], a[128:131], v[160:163], v[64:67]\n\t"
    "v_exp_f32 v171, v171\n\t"
    "v_mfma_f32_16x16x32_bf16 v[68:71], a[132:135], v[160:163], v[68:71]\n\t"
    "v_exp_f32 v172, v172\n\t"
    "v_mfma_f32_16x16x32_bf16 v[72:75], a[136:139], v[160:163], v[72:75]\n\t"
    "v_exp_f32 v173, v173\n\t"
    "v_mfma_f32_16x16x32_bf16 v[76:79], a[140:143], v[160:163], v[76:79]\n\t"
    "v_exp_f32 v174, v174\n\t"
    "v_mfma_f32_16x16x32_bf16 v[80:83], a[144:147], v[160:163], v[80:83]\n\t"
    "v_exp_f32 v175, v175\n\t"
    "v_mfma_f32_16x16x32_bf16 v[84:87], a[148:151], v[160:163], v[84:87]\n\t"
    "v_cvt_pk_bf16_f32 v168, v168, v169\n\t"
    "v_cvt_pk_bf16_f32 v169, v170, v171\n\t"
    "v_mfma_f32_16x16x32_bf16 v[88:91], a[152:155], v[160:163], v[88:91]\n\t"
    "v_cvt_pk_bf16_f32 v170, v172, v173\n\t"
    "v_cvt_pk_bf16_f32 v171, v174, v175\n\t"
    "v_mfma_f32_16x16x32_bf16 v[92:95], a[156:159], v[160:163], v[92:95]\n\t"
    "v_exp_f32 v144, v144\n\t"
    "v_mfma_f32_16x16x32_bf16 v[96:99], a[128:131], v[168:171], v[96:99]\n\t"
    "ds_read_b64_tr_b16 a[128:129], v226 offset:24576\n\t"
    "v_mfma_f32_16x16x32_bf16 v[100:103], a[132:135], v[168:171], v[100:103]\n\t"
    "ds_read_b64_tr_b16 a[130:131], v226 offset:28672\n\t"
    "v_mfma_f32_16x16x32_bf16 v[104:107], a[136:139], v[168:171], v[104:107]\n\t"
    "ds_read_b64_tr_b16 a[132:133], v225 offset:24576\n\t"
    "v_mfma_f32_16x16x32_bf16 v[108:111], a[140:143], v[168:171], v[108:111]\n\t"
    "ds_read_b64_tr_b16 a[134:135], v225 offset:28672\n\t"
    "v_mfma_f32_16x16x32_bf16 v[112:115], a[144:147], v[168:171], v[112:115]\n\t"
    "ds_read_b64_tr_b16 a[136:137], v226 offset:25088\n\t"
    "v_mfma_f32_16x16x32_bf16 v[116:119], a[148:151], v[168:171], v[116:119]\n\t"
    "ds_read_b64_tr_b16 a[138:139], v226 offset:29184\n\t"
    "v_mfma_f32_16x16x32_bf16 v[120:123], a[152:155], v[168:171], v[120:123]\n\t"
    "ds_read_b64_tr_b16 a[140:141], v225 offset:25088\n\t"
    "v_mfma_f32_16x16x32_bf16 v[124:127], a[156:159], v[168:171], v[124:127]\n\t"
    "ds_read_b64_tr_b16 a[142:143], v225 offset:29184\n\t"
    "v_mfma_f32_16x16x32_bf16 v[216:219], v[252:255], v[160:163], v[216:219]\n\t"
    "ds_read_b64_tr_b16 a[144:145], v226 offset:25600\n\t"
    "v_mfma_f32_16x16x32_bf16 v[220:223], v[252:255], v[168:171], v[220:223]\n\t"
    "s_waitcnt lgkmcnt(9)\n\t"
    "ds_read_b64_tr_b16 a[146:147], v226 offset:29696\n\t"
    "v_mfma_f32_16x16x32_bf16 v[128:131], a[96:99], a[32:35], v[192:195]\n\t"
    "ds_read_b64_tr_b16 a[148:149], v225 offset:25600\n\t"
    "v_mfma_f32_16x16x32_bf16 v[128:131], a[100:103], a[36:39], v[128:131]\n\t"
    "ds_read_b64_tr_b16 a[150:151], v225 offset:29696\n\t"
    "v_mfma_f32_16x16x32_bf16 v[128:131], a[104:107], a[40:43], v[128:131]\n\t"
    "ds_read_b64_tr_b16 a[152:153], v226 offset:26112\n\t"
    "v_mfma_f32_16x16x32_bf16 v[128:131], a[108:111], a[44:47], v[128:131]\n\t"
    "ds_read_b64_tr_b16 a[154:155], v226 offset:30208\n\t"
    "v_mfma_f32_16x16x32_bf16 v[132:135], a[112:115], a[32:35], v[192:195]\n\t"
    "ds_read_b64_tr_b16 a[156:157], v225 offset:26112\n\t"
    "v_mfma_f32_16x16x32_bf16 v[132:135], a[116:119], a[36:39], v[132:135]\n\t"
    "ds_read_b64_tr_b16 a[158:159], v225 offset:30208\n\t"
    "v_mfma_f32_16x16x32_bf16 v[132:135], a[120:123], a[40:43], v[132:135]\n\t"
    "v_exp_f32 v145, v145\n\t"
    "v_mfma_f32_16x16x32_bf16 v[132:135], a[124:127], a[44:47], v[132:135]\n\t"
    "v_exp_f32 v146, v146\n\t"
    "v_mfma_f32_16x16x32_bf16 v[136:139], a[96:99], a[48:51], v[196:199]\n\t"
    "v_exp_f32 v147, v147\n\t"
    "v_mfma_f32_16x16x32_bf16 v[136:139], a[100:103], a[52:55], v[136:139]\n\t"
    "v_exp_f32 v148, v148\n\t"
    "v_mfma_f32_16x16x32_bf16 v[136:139], a[104:107], a[56:59], v[136:139]\n\t"
    "v_exp_f32 v149, v149\n\t"
    "v_mfma_f32_16x16x32_bf16 v[136:139], a[108:111], a[60:63], v[136:139]\n\t"
    "v_exp_f32 v150, v150\n\t"
    "v_mfma_f32_16x16x32_bf16 v[140:143], a[112:115], a[48:51], v[196:199]\n\t"
    "v_exp_f32 v151, v151\n\t"
    "v_mfma_f32_16x16x32_bf16 v[140:143], a[116:119], a[52:55], v[140:143]\n\t"
    "v_cvt_pk_bf16_f32 v144, v144, v145\n\t"
    "v_cvt_pk_bf16_f32 v145, v146, v147\n\t"
    "v_mfma_f32_16x16x32_bf16 v[140:143], a[120:123], a[56:59], v[140:143]\n\t"
    "v_cvt_pk_bf16_f32 v146, v148, v149\n\t"
    "v_cvt_pk_bf16_f32 v147, v150, v151\n\t"
    "v_mfma_f32_16x16x32_bf16 v[140:143], a[124:127], a[60:63], v[140:143]\n\t"
    "s_waitcnt lgkmcnt(0)\n\t"
    "s_waitcnt vmcnt(4)\n\t"
    "s_barrier\n\t"
    "v_exp_f32 v152, v152\n\t"
    "v_mfma_f32_16x16x32_bf16 v[160:163], a[96:99], a[0:3], v[200:203]\n\t"
    "s_add_u32 m0, %8, 16384\n\t"
    "s_add_u32 s40, %9, 49152\n\t"
    "v_mfma_f32_16x16x32_bf16 v[160:163], a[100:103], a[4:7], v[160:163]\n\t"
    "v_exp_f32 v153, v153\n\t"
    "v_mfma_f32_16x16x32_bf16 v[160:163], a[104:107], a[8:11], v[160:163]\n\t"
    "v_exp_f32 v154, v154\n\t"
    "v_mfma_f32_16x16x32_bf16 v[160:163], a[108:111], a[12:15], v[160:163]\n\t"
    "v_exp_f32 v155, v155\n\t"
    "v_mfma_f32_16x16x32_bf16 v[164:167], a[112:115], a[0:3], v[200:203]\n\t"
    "v_exp_f32 v156, v156\n\t"
    "v_mfma_f32_16x16x32_bf16 v[164:167], a[116:119], a[4:7], v[164:167]\n\t"
    "v_exp_f32 v157, v157\n\t"
    "v_mfma_f32_16x16x32_bf16 v[164:167], a[120:123], a[8:11], v[164:167]\n\t"
    "v_exp_f32 v158, v158\n\t"
    "v_mfma_f32_16x16x32_bf16 v[164:167], a[124:127], a[12:15], v[164:167]\n\t"
    "v_exp_f32 v159, v159\n\t"
    "v_mfma_f32_16x16x32_bf16 v[168:171], a[96:99], a[16:19], v[204:207]\n\t"
    "ds_read_b128 a[96:99], v224 offset:24576\n\t"
    "v_cvt_pk_bf16_f32 v152, v152, v153\n\t"
    "v_mfma_f32_16x16x32_bf16 v[168:171], a[100:103], a[20:23], v[168:171]\n\t"
    "ds_read_b128 a[100:103], v224 offset:25600\n\t"
    "v_cvt_pk_bf16_f32 v153, v154, v155\n\t"
    "v_mfma_f32_16x16x32_bf16 v[168:171], a[104:107], a[24:27], v[168:171]\n\t"
    "buffer_load_dwordx4 v227, %3, s40 offen offset:0  lds\n\t"
    "v_mfma_f32_16x16x32_bf16 v[168:171], a[108:111], a[28:31], v[168:171]\n\t"
    "ds_read_b128 a[104:107], v224 offset:26624\n\t"
    "v_cvt_pk_bf16_f32 v154, v156, v157\n\t"
    "v_mfma_f32_16x16x32_bf16 v[172:175], a[112:115], a[16:19], v[204:207]\n\t"
    "ds_read_b128 a[108:111], v224 offset:27648\n\t"
    "v_cvt_pk_bf16_f32 v155, v158, v159\n\t"
    "v_mfma_f32_16x16x32_bf16 v[172:175], a[116:119], a[20:23], v[172:175]\n\t"
    "ds_read_b128 a[112:115], v224 offset:28672\n\t"
    "v_mfma_f32_16x16x32_bf16 v[172:175], a[120:123], a[24:27], v[172:175]\n\t"
    "ds_read_b128 a[116:119], v224 offset:29696\n\t"
    "v_mfma_f32_16x16x32_bf16 v[172:175], a[124:127], a[28:31], v[172:175]\n\t"
    "ds_read_b128 a[120:123], v224 offset:30720\n\t"
    "v_mfma_f32_16x16x32_bf16 v[0:3], a[128:131], v[144:147], v[0:3]\n\t"
    "buffer_load_dwordx4 v248, %3, s40 offen offset:1024  lds\n\t"
    "s_add_u32 m0, %8, 24576\n\t"
    "s_add_u32 s40, %9, 57344\n\t"
    "v_mfma_f32_16x16x32_bf16 v[4:7], a[132:135], v[144:147], v[4:7]\n\t"
    "ds_read_b128 a[124:127], v224 offset:31744\n\t"
    "v_mfma_f32_16x16x32_bf16 v[8:11], a[136:139], v[144:147], v[8:11]\n\t"
    "v_exp_f32 v176, v176\n\t"
    "v_mfma_f32_16x16x32_bf16 v[12:15], a[140:143], v[144:147], v[12:15]\n\t"
    "v_exp_f32 v177, v177\n\t"
    "v_mfma_f32_16x16x32_bf16 v[16:19], a[144:147], v[144:147], v[16:19]\n\t"
    "v_exp_f32 v178, v178\n\t"
    "v_mfma_f32_16x16x32_bf16 v[20:23], a[148:151], v[144:147], v[20:23]\n\t"
    "v_exp_f32 v179, v179\n\t"
    "v_mfma_f32_16x16x32_bf16 v[24:27], a[152:155], v[144:147], v[24:27]\n\t"
    "buffer_load_dwordx4 v227, %3, s40 offen offset:0  lds\n\t"
    "v_mfma_f32_16x16x32_bf16 v[28:31], a[156:159], v[144:147], v[28:31]\n\t"
    "v_exp_f32 v180, v180\n\t"
    "v_mfma_f32_16x16x32_bf16 v[32:35], a[128:131], v[152:155], v[32:35]\n\t"
    "v_exp_f32 v181, v181\n\t"
    "v_mfma_f32_16x16x32_bf16 v[36:39], a[132:135], v[152:155], v[36:39]\n\t"
    "v_exp_f32 v182, v182\n\t"
    "v_mfma_f32_16x16x32_bf16 v[40:43], a[136:139], v[152:155], v[40:43]\n\t"
    "v_exp_f32 v183, v183\n\t"
    "v_mfma_f32_16x16x32_bf16 v[44:47], a[140:143], v[152:155], v[44:47]\n\t"
    "v_cvt_pk_bf16_f32 v176, v176, v177\n\t"
    "v_cvt_pk_bf16_f32 v177, v178, v179\n\t"
    "v_mfma_f32_16x16x32_bf16 v[48:51], a[144:147], v[152:155], v[48:51]\n\t"
    "buffer_load_dwordx4 v248, %3, s40 offen offset:1024  lds\n\t"
    "v_mfma_f32_16x16x32_bf16 v[52:55], a[148:151], v[152:155], v[52:55]\n\t"
    "v_cvt_pk_bf16_f32 v178, v180, v181\n\t"
    "v_cvt_pk_bf16_f32 v179, v182, v183\n\t"
    "v_mfma_f32_16x16x32_bf16 v[56:59], a[152:155], v[152:155], v[56:59]\n\t"
    "v_exp_f32 v184, v184\n\t"
    "v_mfma_f32_16x16x32_bf16 v[60:63], a[156:159], v[152:155], v[60:63]\n\t"
    "v_exp_f32 v185, v185\n\t"
    "v_mfma_f32_16x16x32_bf16 v[208:211], v[252:255], v[144:147], v[208:211]\n\t"
    "ds_read_b128 a[72:75], v230 offset:4096\n\t"
    "v_cvt_pk_bf16_f32 v184, v184, v185\n\t"
    "v_mfma_f32_16x16x32_bf16 v[212:215], v[252:255], v[152:155], v[212:215]\n\t"
    "ds_read_b128 a[76:79], v231 offset:4096\n\t"
    "v_mfma_f32_16x16x32_bf16 v[64:67], a[128:131], v[176:179], v[64:67]\n\t"
    "ds_read_b128 a[88:91], v230 offset:6144\n\t"
    "v_mfma_f32_16x16x32_bf16 v[68:71], a[132:135], v[176:179], v[68:71]\n\t"
    "ds_read_b128 a[92:95], v231 offset:6144\n\t"
    "v_mfma_f32_16x16x32_bf16 v[72:75], a[136:139], v[176:179], v[72:75]\n\t"
    "v_exp_f32 v186, v186\n\t"
    "v_mfma_f32_16x16x32_bf16 v[76:79], a[140:143], v[176:179], v[76:79]\n\t"
    "v_exp_f32 v187, v187\n\t"
    "v_mfma_f32_16x16x32_bf16 v[80:83], a[144:147], v[176:179], v[80:83]\n\t"
    "v_exp_f32 v188, v188\n\t"
    "v_mfma_f32_16x16x32_bf16 v[84:87], a[148:151], v[176:179], v[84:87]\n\t"
    "v_exp_f32 v189, v189\n\t"
    "v_mfma_f32_16x16x32_bf16 v[88:91], a[152:155], v[176:179], v[88:91]\n\t"
    "v_exp_f32 v190, v190\n\t"
    "v_mfma_f32_16x16x32_bf16 v[92:95], a[156:159], v[176:179], v[92:95]\n\t"
    "v_exp_f32 v191, v191\n\t"
    "v_cvt_pk_bf16_f32 v185, v186, v187\n\t"
    "v_cvt_pk_bf16_f32 v186, v188, v189\n\t"
    "v_cvt_pk_bf16_f32 v187, v190, v191\n\t"
    "v_exp_f32 v128, v128\n\t"
    "s_nop 0\n\t"
    "v_mfma_f32_16x16x32_bf16 v[96:99], a[128:131], v[184:187], v[96:99]\n\t"
    "ds_read_b64_tr_b16 a[128:129], v226 offset:0\n\t"
    "v_mfma_f32_16x16x32_bf16 v[100:103], a[132:135], v[184:187], v[100:103]\n\t"
    "ds_read_b64_tr_b16 a[130:131], v226 offset:4096\n\t"
    "v_mfma_f32_16x16x32_bf16 v[104:107], a[136:139], v[184:187], v[104:107]\n\t"
    "s_add_u32 s40, %11, 65664\n\t"
    "ds_read_b64_tr_b16 a[132:133], v225 offset:0\n\t"
    "v_mfma_f32_16x16x32_bf16 v[108:111], a[140:143], v[184:187], v[108:111]\n\t"
    "ds_read_b64_tr_b16 a[134:135], v225 offset:4096\n\t"
    "v_mfma_f32_16x16x32_bf16 v[112:115], a[144:147], v[184:187], v[112:115]\n\t"
    "ds_read_b64_tr_b16 a[136:137], v226 offset:512\n\t"
    "v_mfma_f32_16x16x32_bf16 v[116:119], a[148:151], v[184:187], v[116:119]\n\t"
    "ds_read_b64_tr_b16 a[138:139], v226 offset:4608\n\t"
    "v_mfma_f32_16x16x32_bf16 v[120:123], a[152:155], v[184:187], v[120:123]\n\t"
    "ds_read_b64_tr_b16 a[140:141], v225 offset:512\n\t"
    "v_mfma_f32_16x16x32_bf16 v[124:127], a[156:159], v[184:187], v[124:127]\n\t"
    "ds_read_b64_tr_b16 a[142:143], v225 offset:4608\n\t"
    "v_mfma_f32_16x16x32_bf16 v[216:219], v[252:255], v[176:179], v[216:219]\n\t"
    "ds_read_b64_tr_b16 a[144:145], v226 offset:1024\n\t"
    "v_mfma_f32_16x16x32_bf16 v[220:223], v[252:255], v[184:187], v[220:223]\n\t"
    "s_waitcnt lgkmcnt(9)\n\t"
    "ds_read_b64_tr_b16 a[146:147], v226 offset:5120\n\t"
    "v_mfma_f32_16x16x32_bf16 v[144:147], a[96:99], a[32:35], v[192:195]\n\t"
    "ds_read_b64_tr_b16 a[148:149], v225 offset:1024\n\t"
    "v_mfma_f32_16x16x32_bf16 v[144:147], a[100:103], a[36:39], v[144:147]\n\t"
    "ds_read_b64_tr_b16 a[150:151], v225 offset:5120\n\t"
    "v_mfma_f32_16x16x32_bf16 v[144:147], a[104:107], a[40:43], v[144:147]\n\t"
    "ds_read_b64_tr_b16 a[152:153], v226 offset:1536\n\t"
    "v_mfma_f32_16x16x32_bf16 v[144:147], a[108:111], a[44:47], v[144:147]\n\t"
    "ds_read_b64_tr_b16 a[154:155], v226 offset:5632\n\t"
    "v_mfma_f32_16x16x32_bf16 v[148:151], a[112:115], a[32:35], v[192:195]\n\t"
    "ds_read_b64_tr_b16 a[156:157], v225 offset:1536\n\t"
    "v_mfma_f32_16x16x32_bf16 v[148:151], a[116:119], a[36:39], v[148:151]\n\t"
    "ds_read_b64_tr_b16 a[158:159], v225 offset:5632\n\t"
    "v_mfma_f32_16x16x32_bf16 v[148:151], a[120:123], a[40:43], v[148:151]\n\t"
    "v_exp_f32 v129, v129\n\t"
    "v_mfma_f32_16x16x32_bf16 v[148:151], a[124:127], a[44:47], v[148:151]\n\t"
    "v_exp_f32 v130, v130\n\t"
    "v_mfma_f32_16x16x32_bf16 v[152:155], a[96:99], a[48:51], v[196:199]\n\t"
    "v_exp_f32 v131, v131\n\t"
    "v_mfma_f32_16x16x32_bf16 v[152:155], a[100:103], a[52:55], v[152:155]\n\t"
    "v_exp_f32 v132, v132\n\t"
    "v_mfma_f32_16x16x32_bf16 v[152:155], a[104:107], a[56:59], v[152:155]\n\t"
    "v_exp_f32 v133, v133\n\t"
    "v_mfma_f32_16x16x32_bf16 v[152:155], a[108:111], a[60:63], v[152:155]\n\t"
    "v_exp_f32 v134, v134\n\t"
    "v_mfma_f32_16x16x32_bf16 v[156:159], a[112:115], a[48:51], v[196:199]\n\t"
    "v_exp_f32 v135, v135\n\t"
    "v_mfma_f32_16x16x32_bf16 v[156:159], a[116:119], a[52:55], v[156:159]\n\t"
    "v_cvt_pk_bf16_f32 v128, v128, v129\n\t"
    "v_cvt_pk_bf16_f32 v129, v130, v131\n\t"
    "v_mfma_f32_16x16x32_bf16 v[156:159], a[120:123], a[56:59], v[156:159]\n\t"
    "v_cvt_pk_bf16_f32 v130, v132, v133\n\t"
    "v_cvt_pk_bf16_f32 v131, v134, v135\n\t"
    "v_mfma_f32_16x16x32_bf16 v[156:159], a[124:127], a[60:63], v[156:159]\n\t"
    "v_exp_f32 v136, v136\n\t"
    "v_mfma_f32_16x16x32_bf16 v[176:179], a[96:99], a[0:3], v[200:203]\n\t"
    "v_exp_f32 v137, v137\n\t"
    "v_mfma_f32_16x16x32_bf16 v[176:179], a[100:103], a[4:7], v[176:179]\n\t"
    "buffer_store_dwordx4 a[176:179], v236, %4, s40 offen offset:0 sc1\n\t"
    "v_mfma_f32_16x16x32_bf16 v[176:179], a[104:107], a[8:11], v[176:179]\n\t"
    "v_exp_f32 v138, v138\n\t"
    "v_mfma_f32_16x16x32_bf16 v[176:179], a[108:111], a[12:15], v[176:179]\n\t"
    "v_exp_f32 v139, v139\n\t"
    "v_mfma_f32_16x16x32_bf16 v[180:183], a[112:115], a[0:3], v[200:203]\n\t"
    "v_exp_f32 v140, v140\n\t"
    "v_mfma_f32_16x16x32_bf16 v[180:183], a[116:119], a[4:7], v[180:183]\n\t"
    "v_exp_f32 v141, v141\n\t"
    "v_mfma_f32_16x16x32_bf16 v[180:183], a[120:123], a[8:11], v[180:183]\n\t"
    "v_exp_f32 v142, v142\n\t"
    "v_mfma_f32_16x16x32_bf16 v[180:183], a[124:127], a[12:15], v[180:183]\n\t"
    "v_exp_f32 v143, v143\n\t"
    "v_mfma_f32_16x16x32_bf16 v[184:187], a[96:99], a[16:19], v[204:207]\n\t"
    "ds_read_b128 a[96:99], v224 offset:32768\n\t"
    "buffer_store_dwordx4 a[180:183], v236, %4, s40 offen offset:2048 sc1\n\t"
    "s_add_u32 s40, %11, 69760\n\t"
    "v_mfma_f32_16x16x32_bf16 v[184:187], a[100:103], a[20:23], v[184:187]\n\t"
    "ds_read_b128 a[100:103], v224 offset:33792\n\t"
    "v_cvt_pk_bf16_f32 v136, v136, v137\n\t"
    "v_mfma_f32_16x16x32_bf16 v[184:187], a[104:107], a[24:27], v[184:187]\n\t"
    "ds_read_b128 a[104:107], v224 offset:34816\n\t"
    "v_cvt_pk_bf16_f32 v137, v138, v139\n\t"
    "v_mfma_f32_16x16x32_bf16 v[184:187], a[108:111], a[28:31], v[184:187]\n\t"
    "ds_read_b128 a[108:111], v224 offset:35840\n\t"
    "v_cvt_pk_bf16_f32 v138, v140, v141\n\t"
    "v_mfma_f32_16x16x32_bf16 v[188:191], a[112:115], a[16:19], v[204:207]\n\t"
    "ds_read_b128 a[112:115], v224 offset:36864\n\t"
    "v_cvt_pk_bf16_f32 v139, v142, v143\n\t"
    "v_mfma_f32_16x16x32_bf16 v[188:191], a[116:119], a[20:23], v[188:191]\n\t"
    "ds_read_b128 a[116:119], v224 offset:37888\n\t"
    "v_mfma_f32_16x16x32_bf16 v[188:191], a[120:123], a[24:27], v[188:191]\n\t"
    "ds_read_b128 a[120:123], v224 offset:38912\n\t"
    "v_mfma_f32_16x16x32_bf16 v[188:191], a[124:127], a[28:31], v[188:191]\n\t"
    "s_waitcnt lgkmcnt(7)\n\t"
    "ds_read_b128 a[124:127], v224 offset:39936\n\t"
    "v_mfma_f32_16x16x32_bf16 v[0:3], a[128:131], v[128:131], v[0:3]\n\t"
    "v_exp_f32 v160, v160\n\t"
    "v_mfma_f32_16x16x32_bf16 v[4:7], a[132:135], v[128:131], v[4:7]\n\t"
    "buffer_store_dwordx4 a[184:187], v236, %4, s40 offen offset:0 sc1\n\t"
    "v_mfma_f32_16x16x32_bf16 v[8:11], a[136:139], v[128:131], v[8:11]\n\t"
    "v_exp_f32 v161, v161\n\t"
    "v_mfma_f32_16x16x32_bf16 v[12:15], a[140:143], v[128:131], v[12:15]\n\t"
    "v_exp_f32 v162, v162\n\t"
    "v_mfma_f32_16x16x32_bf16 v[16:19], a[144:147], v[128:131], v[16:19]\n\t"
    "v_exp_f32 v163, v163\n\t"
    "v_mfma_f32_16x16x32_bf16 v[20:23], a[148:151], v[128:131], v[20:23]\n\t"
    "v_exp_f32 v164, v164\n\t"
    "v_mfma_f32_16x16x32_bf16 v[24:27], a[152:155], v[128:131], v[24:27]\n\t"
    "v_exp_f32 v165, v165\n\t"
    "v_mfma_f32_16x16x32_bf16 v[28:31], a[156:159], v[128:131], v[28:31]\n\t"
    "v_exp_f32 v166, v166\n\t"
    "v_mfma_f32_16x16x32_bf16 v[32:35], a[128:131], v[136:139], v[32:35]\n\t"
    "v_exp_f32 v167, v167\n\t"
    "v_mfma_f32_16x16x32_bf16 v[36:39], a[132:135], v[136:139], v[36:39]\n\t"
    "buffer_store_dwordx4 a[188:191], v236, %4, s40 offen offset:2048 sc1\n\t"
    "v_mfma_f32_16x16x32_bf16 v[40:43], a[136:139], v[136:139], v[40:43]\n\t"
    "v_cvt_pk_bf16_f32 v160, v160, v161\n\t"
    "v_cvt_pk_bf16_f32 v161, v162, v163\n\t"
    "v_mfma_f32_16x16x32_bf16 v[44:47], a[140:143], v[136:139], v[44:47]\n\t"
    "v_cvt_pk_bf16_f32 v162, v164, v165\n\t"
    "v_cvt_pk_bf16_f32 v163, v166, v167\n\t"
    "v_mfma_f32_16x16x32_bf16 v[48:51], a[144:147], v[136:139], v[48:51]\n\t"
    "v_exp_f32 v168, v168\n\t"
    "v_mfma_f32_16x16x32_bf16 v[52:55], a[148:151], v[136:139], v[52:55]\n\t"
    "v_exp_f32 v169, v169\n\t"
    "v_mfma_f32_16x16x32_bf16 v[56:59], a[152:155], v[136:139], v[56:59]\n\t"
    "v_exp_f32 v170, v170\n\t"
    "v_mfma_f32_16x16x32_bf16 v[60:63], a[156:159], v[136:139], v[60:63]\n\t"
    "v_exp_f32 v171, v171\n\t"
    "v_mfma_f32_16x16x32_bf16 v[208:211], v[252:255], v[128:131], v[208:211]\n\t"
    "v_exp_f32 v172, v172\n\t"
    "v_mfma_f32_16x16x32_bf16 v[212:215], v[252:255], v[136:139], v[212:215]\n\t"
    "v_exp_f32 v173, v173\n\t"
    "v_mfma_f32_16x16x32_bf16 v[64:67], a[128:131], v[160:163], v[64:67]\n\t"
    "v_exp_f32 v174, v174\n\t"
    "v_mfma_f32_16x16x32_bf16 v[68:71], a[132:135], v[160:163], v[68:71]\n\t"
    "v_exp_f32 v175, v175\n\t"
    "v_mfma_f32_16x16x32_bf16 v[72:75], a[136:139], v[160:163], v[72:75]\n\t"
    "v_cvt_pk_bf16_f32 v168, v168, v169\n\t"
    "v_cvt_pk_bf16_f32 v169, v170, v171\n\t"
    "v_mfma_f32_16x16x32_bf16 v[76:79], a[140:143], v[160:163], v[76:79]\n\t"
    "v_cvt_pk_bf16_f32 v170, v172, v173\n\t"
    "v_cvt_pk_bf16_f32 v171, v174, v175\n\t"
    "v_mfma_f32_16x16x32_bf16 v[80:83], a[144:147], v[160:163], v[80:83]\n\t"
    "v_exp_f32 v144, v144\n\t"
    "v_mfma_f32_16x16x32_bf16 v[84:87], a[148:151], v[160:163], v[84:87]\n\t"
    "v_exp_f32 v145, v145\n\t"
    "v_mfma_f32_16x16x32_bf16 v[88:91], a[152:155], v[160:163], v[88:91]\n\t"
    "v_exp_f32 v146, v146\n\t"
    "v_mfma_f32_16x16x32_bf16 v[92:95], a[156:159], v[160:163], v[92:95]\n\t"
    "v_exp_f32 v147, v147\n\t"
    "v_mfma_f32_16x16x32_bf16 v[96:99], a[128:131], v[168:171], v[96:99]\n\t"
    "ds_read_b64_tr_b16 a[128:129], v226 offset:8192\n\t"
    "v_cvt_pk_bf16_f32 v144, v144, v145\n\t"
    "v_mfma_f32_16x16x32_bf16 v[100:103], a[132:135], v[168:171], v[100:103]\n\t"
    "ds_read_b64_tr_b16 a[130:131], v226 offset:12288\n\t"
    "v_cvt_pk_bf16_f32 v145, v146, v147\n\t"
    "v_mfma_f32_16x16x32_bf16 v[104:107], a[136:139], v[168:171], v[104:107]\n\t"
    "ds_read_b64_tr_b16 a[132:133], v225 offset:8192\n\t"
    "v_mfma_f32_16x16x32_bf16 v[108:111], a[140:143], v[168:171], v[108:111]\n\t"
    "ds_read_b64_tr_b16 a[134:135], v225 offset:12288\n\t"
    "v_mfma_f32_16x16x32_bf16 v[112:115], a[144:147], v[168:171], v[112:115]\n\t"
    "ds_read_b64_tr_b16 a[136:137], v226 offset:8704\n\t"
    "v_mfma_f32_16x16x32_bf16 v[116:119], a[148:151], v[168:171], v[116:119]\n\t"
    "ds_read_b64_tr_b16 a[138:139], v226 offset:12800\n\t"
    "v_mfma_f32_16x16x32_bf16 v[120:123], a[152:155], v[168:171], v[120:123]\n\t"
    "ds_read_b64_tr_b16 a[140:141], v225 offset:8704\n\t"
    "v_mfma_f32_16x16x32_bf16 v[124:127], a[156:159], v[168:171], v[124:127]\n\t"
    "ds_read_b64_tr_b16 a[142:143], v225 offset:12800\n\t"
    "v_mfma_f32_16x16x32_bf16 v[216:219], v[252:255], v[160:163], v[216:219]\n\t"
    "ds_read_b64_tr_b16 a[144:145], v226 offset:9216\n\t"
    "v_mfma_f32_16x16x32_bf16 v[220:223], v[252:255], v[168:171], v[220:223]\n\t"
    "s_waitcnt lgkmcnt(9)\n\t"
    "ds_read_b64_tr_b16 a[146:147], v226 offset:13312\n\t"
    "v_mfma_f32_16x16x32_bf16 v[128:131], a[96:99], a[32:35], v[192:195]\n\t"
    "ds_read_b64_tr_b16 a[148:149], v225 offset:9216\n\t"
    "v_mfma_f32_16x16x32_bf16 v[128:131], a[100:103], a[36:39], v[128:131]\n\t"
    "ds_read_b64_tr_b16 a[150:151], v225 offset:13312\n\t"
    "v_mfma_f32_16x16x32_bf16 v[128:131], a[104:107], a[40:43], v[128:131]\n\t"
    "ds_read_b64_tr_b16 a[152:153], v226 offset:9728\n\t"
    "v_mfma_f32_16x16x32_bf16 v[128:131], a[108:111], a[44:47], v[128:131]\n\t"
    "ds_read_b64_tr_b16 a[154:155], v226 offset:13824\n\t"
    "v_mfma_f32_16x16x32_bf16 v[132:135], a[112:115], a[32:35], v[192:195]\n\t"
    "ds_read_b64_tr_b16 a[156:157], v225 offset:9728\n\t"
    "v_mfma_f32_16x16x32_bf16 v[132:135], a[116:119], a[36:39], v[132:135]\n\t"
    "ds_read_b64_tr_b16 a[158:159], v225 offset:13824\n\t"
    "v_mfma_f32_16x16x32_bf16 v[132:135], a[120:123], a[40:43], v[132:135]\n\t"
    "v_exp_f32 v148, v148\n\t"
    "v_mfma_f32_16x16x32_bf16 v[132:135], a[124:127], a[44:47], v[132:135]\n\t"
    "v_exp_f32 v149, v149\n\t"
    "v_mfma_f32_16x16x32_bf16 v[136:139], a[96:99], a[48:51], v[196:199]\n\t"
    "v_exp_f32 v150, v150\n\t"
    "v_mfma_f32_16x16x32_bf16 v[136:139], a[100:103], a[52:55], v[136:139]\n\t"
    "v_exp_f32 v151, v151\n\t"
    "v_mfma_f32_16x16x32_bf16 v[136:139], a[104:107], a[56:59], v[136:139]\n\t"
    "v_cvt_pk_bf16_f32 v146, v148, v149\n\t"
    "v_cvt_pk_bf16_f32 v147, v150, v151\n\t"
    "v_mfma_f32_16x16x32_bf16 v[136:139], a[108:111], a[60:63], v[136:139]\n\t"
    "v_exp_f32 v152, v152\n\t"
    "v_mfma_f32_16x16x32_bf16 v[140:143], a[112:115], a[48:51], v[196:199]\n\t"
    "v_exp_f32 v153, v153\n\t"
    "v_mfma_f32_16x16x32_bf16 v[140:143], a[116:119], a[52:55], v[140:143]\n\t"
    "v_exp_f32 v154, v154\n\t"
    "v_mfma_f32_16x16x32_bf16 v[140:143], a[120:123], a[56:59], v[140:143]\n\t"
    "v_exp_f32 v155, v155\n\t"
    "v_mfma_f32_16x16x32_bf16 v[140:143], a[124:127], a[60:63], v[140:143]\n\t"
    "s_waitcnt lgkmcnt(0)\n\t"
    "s_waitcnt vmcnt(4)\n\t"
    "s_barrier\n\t"
    "v_exp_f32 v156, v156\n\t"
    "v_mfma_f32_16x16x32_bf16 v[160:163], a[96:99], a[64:67], 0\n\t"
    "s_add_u32 m0, %8, 0\n\t"
    "s_add_u32 s40, %9, 65536\n\t"
    "v_cvt_pk_bf16_f32 v152, v152, v153\n\t"
    "v_mfma_f32_16x16x32_bf16 v[160:163], a[100:103], a[68:71], v[160:163]\n\t"
    "v_exp_f32 v157, v157\n\t"
    "v_mfma_f32_16x16x32_bf16 v[160:163], a[104:107], a[72:75], v[160:163]\n\t"
    "v_exp_f32 v158, v158\n\t"
    "v_mfma_f32_16x16x32_bf16 v[160:163], a[108:111], a[76:79], v[160:163]\n\t"
    "v_exp_f32 v159, v159\n\t"
    "v_mfma_f32_16x16x32_bf16 v[164:167], a[112:115], a[64:67], 0\n\t"
    "v_cvt_pk_bf16_f32 v153, v154, v155\n\t"
    "v_cvt_pk_bf16_f32 v154, v156, v157\n\t"
    "v_mfma_f32_16x16x32_bf16 v[164:167], a[116:119], a[68:71], v[164:167]\n\t"
    "v_cvt_pk_bf16_f32 v155, v158, v159\n\t"
    "v_mfma_f32_16x16x32_bf16 v[164:167], a[120:123], a[72:75], v[164:167]\n\t"
    "v_exp_f32 v176, v176\n\t"
    "v_mfma_f32_16x16x32_bf16 v[164:167], a[124:127], a[76:79], v[164:167]\n\t"
    "v_exp_f32 v177, v177\n\t"
    "v_mfma_f32_16x16x32_bf16 v[168:171], a[96:99], a[80:83], 0\n\t"
    "ds_read_b128 a[96:99], v224 offset:40960\n\t"
    "v_cvt_pk_bf16_f32 v176, v176, v177\n\t"
    "v_mfma_f32_16x16x32_bf16 v[168:171], a[100:103], a[84:87], v[168:171]\n\t"
    "ds_read_b128 a[100:103], v224 offset:41984\n\t"
    "v_max3_f32 v229, v160, v161, v162\n\t"
    "v_mfma_f32_16x16x32_bf16 v[168:171], a[104:107], a[88:91], v[168:171]\n\t"
    "buffer_load_dwordx4 v227, %3, s40 offen offset:0  lds\n\t"
    "v_mfma_f32_16x16x32_bf16 v[168:171], a[108:111], a[92:95], v[168:171]\n\t"
    "ds_read_b128 a[104:107], v224 offset:43008\n\t"
    "v_max3_f32 v229, v229, v166, v167\n\t"
    "v_mfma_f32_16x16x32_bf16 v[172:175], a[112:115], a[80:83], 0\n\t"
    "ds_read_b128 a[108:111], v224 offset:44032\n\t"
    "v_mfma_f32_16x16x32_bf16 v[172:175], a[116:119], a[84:87], v[172:175]\n\t"
    "ds_read_b128 a[112:115], v224 offset:45056\n\t"
    "v_mfma_f32_16x16x32_bf16 v[172:175], a[120:123], a[88:91], v[172:175]\n\t"
    "ds_read_b128 a[116:119], v224 offset:46080\n\t"
    "v_max3_f32 v233, v168, v169, v170\n\t"
    "v_mfma_f32_16x16x32_bf16 v[172:175], a[124:127], a[92:95], v[172:175]\n\t"
    "ds_read_b128 a[120:123], v224 offset:47104\n\t"
    "v_mfma_f32_16x16x32_bf16 v[0:3], a[128:131], v[144:147], v[0:3]\n\t"
    "buffer_load_dwordx4 v248, %3, s40 offen offset:1024  lds\n\t"
    "s_add_u32 m0, %8, 8192\n\t"
    "s_add_u32 s40, %9, 73728\n\t"
    "v_mfma_f32_16x16x32_bf16 v[4:7], a[132:135], v[144:147], v[4:7]\n\t"
    "ds_read_b128 a[124:127], v224 offset:48128\n\t"
    "v_mfma_f32_16x16x32_bf16 v[8:11], a[136:139], v[144:147], v[8:11]\n\t"
    "v_exp_f32 v178, v178\n\t"
    "v_mfma_f32_16x16x32_bf16 v[12:15], a[140:143], v[144:147], v[12:15]\n\t"
    "v_exp_f32 v179, v179\n\t"
    "v_mfma_f32_16x16x32_bf16 v[16:19], a[144:147], v[144:147], v[16:19]\n\t"
    "v_exp_f32 v180, v180\n\t"
    "v_mfma_f32_16x16x32_bf16 v[20:23], a[148:151], v[144:147], v[20:23]\n\t"
    "v_exp_f32 v181, v181\n\t"
    "v_mfma_f32_16x16x32_bf16 v[24:27], a[152:155], v[144:147], v[24:27]\n\t"
    "buffer_load_dwordx4 v227, %3, s40 offen offset:0  lds\n\t"
    "v_mfma_f32_16x16x32_bf16 v[28:31], a[156:159], v[144:147], v[28:31]\n\t"
    "v_exp_f32 v182, v182\n\t"
    "v_mfma_f32_16x16x32_bf16 v[32:35], a[128:131], v[152:155], v[32:35]\n\t"
    "v_exp_f32 v183, v183\n\t"
    "v_mfma_f32_16x16x32_bf16 v[36:39], a[132:135], v[152:155], v[36:39]\n\t"
    "v_cvt_pk_bf16_f32 v177, v178, v179\n\t"
    "v_cvt_pk_bf16_f32 v178, v180, v181\n\t"
    "v_mfma_f32_16x16x32_bf16 v[40:43], a[136:139], v[152:155], v[40:43]\n\t"
    "v_exp_f32 v184, v184\n\t"
    "v_mfma_f32_16x16x32_bf16 v[44:47], a[140:143], v[152:155], v[44:47]\n\t"
    "v_exp_f32 v185, v185\n\t"
    "v_mfma_f32_16x16x32_bf16 v[48:51], a[144:147], v[152:155], v[48:51]\n\t"
    "buffer_load_dwordx4 v248, %3, s40 offen offset:1024  lds\n\t"
    "v_mfma_f32_16x16x32_bf16 v[52:55], a[148:151], v[152:155], v[52:55]\n\t"
    "v_cvt_pk_bf16_f32 v179, v182, v183\n\t"
    "v_cvt_pk_bf16_f32 v184, v184, v185\n\t"
    "v_mfma_f32_16x16x32_bf16 v[56:59], a[152:155], v[152:155], v[56:59]\n\t"
    "v_exp_f32 v186, v186\n\t"
    "v_mfma_f32_16x16x32_bf16 v[60:63], a[156:159], v[152:155], v[60:63]\n\t"
    "v_exp_f32 v187, v187\n\t"
    "v_mfma_f32_16x16x32_bf16 v[208:211], v[252:255], v[144:147], v[208:211]\n\t"
    "v_exp_f32 v188, v188\n\t"
    "v_mfma_f32_16x16x32_bf16 v[212:215], v[252:255], v[152:155], v[212:215]\n\t"
    "v_exp_f32 v189, v189\n\t"
    "v_mfma_f32_16x16x32_bf16 v[216:219], v[252:255], v[176:179], v[216:219]\n\t"
    "v_exp_f32 v190, v190\n\t"
    "v_exp_f32 v191, v191\n\t"
    "v_cvt_pk_bf16_f32 v185, v186, v187\n\t"
    "v_cvt_pk_bf16_f32 v186, v188, v189\n\t"
    "v_cvt_pk_bf16_f32 v187, v190, v191\n\t"
    "v_max3_f32 v241, v171, v172, v173\n\t"
    "v_max3_f32 v233, v233, v174, v175\n\t"
    "v_mfma_f32_16x16x32_bf16 v[220:223], v[252:255], v[184:187], v[220:223]\n\t"
    "v_max_f32 v233, v233, v241\n\t"
    "v_mov_b32 v241, v233\n\t"
    "v_mfma_f32_16x16x32_bf16 v[64:67], a[128:131], v[176:179], v[64:67]\n\t"
    "v_exp_f32 v128, v128\n\t"
    "v_mfma_f32_16x16x32_bf16 v[68:71], a[132:135], v[176:179], v[68:71]\n\t"
    "v_permlane16_swap_b32 v233, v241\n\t"
    "v_max_f32 v233, v233, v241\n\t"
    "v_mfma_f32_16x16x32_bf16 v[72:75], a[136:139], v[176:179], v[72:75]\n\t"
    "v_mov_b32 v241, v233\n\t"
    "v_mfma_f32_16x16x32_bf16 v[76:79], a[140:143], v[176:179], v[76:79]\n\t"
    "v_exp_f32 v129, v129\n\t"
    "v_mfma_f32_16x16x32_bf16 v[80:83], a[144:147], v[176:179], v[80:83]\n\t"
    "v_permlane32_swap_b32 v233, v241\n\t"
    "v_min_f32_e64 v204, -v233, -v241\n\t"
    "v_mfma_f32_16x16x32_bf16 v[84:87], a[148:151], v[176:179], v[84:87]\n\t"
    "v_min_f32_e64 v205, -v233, -v241\n\t"
    "v_min_f32_e64 v206, -v233, -v241\n\t"
    "v_mfma_f32_16x16x32_bf16 v[88:91], a[152:155], v[176:179], v[88:91]\n\t"
    "v_min_f32_e64 v207, -v233, -v241\n\t"
    "v_mov_b32 v233, v220\n\t"
    "v_mfma_f32_16x16x32_bf16 v[92:95], a[156:159], v[176:179], v[92:95]\n\t"
    "v_rcp_f32 v243, v233\n\t"
    "v_mfma_f32_16x16x32_bf16 v[96:99], a[128:131], v[184:187], v[96:99]\n\t"
    "ds_read_b64_tr_b16 a[128:129], v226 offset:16384\n\t"
    "v_cvt_pk_bf16_f32 v128, v128, v129\n\t"
    "v_mfma_f32_16x16x32_bf16 v[100:103], a[132:135], v[184:187], v[100:103]\n\t"
    "ds_read_b64_tr_b16 a[130:131], v226 offset:20480\n\t"
    "v_add_f32 v168, v204, v168\n\t"
    "v_mfma_f32_16x16x32_bf16 v[104:107], a[136:139], v[184:187], v[104:107]\n\t"
    "ds_read_b64_tr_b16 a[132:133], v225 offset:16384\n\t"
    "v_add_f32 v169, v205, v169\n\t"
    "v_mfma_f32_16x16x32_bf16 v[108:111], a[140:143], v[184:187], v[108:111]\n\t"
    "ds_read_b64_tr_b16 a[134:135], v225 offset:20480\n\t"
    "v_mul_f32 v96, v243, v96\n\t"
    "v_mfma_f32_16x16x32_bf16 v[112:115], a[144:147], v[184:187], v[112:115]\n\t"
    "ds_read_b64_tr_b16 a[136:137], v226 offset:16896\n\t"
    "v_mul_f32 v97, v243, v97\n\t"
    "v_mfma_f32_16x16x32_bf16 v[116:119], a[148:151], v[184:187], v[116:119]\n\t"
    "ds_read_b64_tr_b16 a[138:139], v226 offset:20992\n\t"
    "v_mul_f32 v98, v243, v98\n\t"
    "v_mfma_f32_16x16x32_bf16 v[120:123], a[152:155], v[184:187], v[120:123]\n\t"
    "ds_read_b64_tr_b16 a[140:141], v225 offset:16896\n\t"
    "v_mul_f32 v99, v243, v99\n\t"
    "v_mfma_f32_16x16x32_bf16 v[124:127], a[156:159], v[184:187], v[124:127]\n\t"
    "s_waitcnt lgkmcnt(7)\n\t"
    "ds_read_b64_tr_b16 a[142:143], v225 offset:20992\n\t"
    "v_cvt_pk_bf16_f32 v96, v96, v97\n\t"
    "v_mfma_f32_16x16x32_bf16 v[144:147], a[96:99], a[32:35], v[192:195]\n\t"
    "ds_read_b64_tr_b16 a[144:145], v226 offset:17408\n\t"
    "v_cvt_pk_bf16_f32 v97, v98, v99\n\t"
    "v_mfma_f32_16x16x32_bf16 v[144:147], a[100:103], a[36:39], v[144:147]\n\t"
    "ds_read_b64_tr_b16 a[146:147], v226 offset:21504\n\t"
    "v_mul_f32 v100, v243, v100\n\t"
    "v_mfma_f32_16x16x32_bf16 v[144:147], a[104:107], a[40:43], v[144:147]\n\t"
    "ds_read_b64_tr_b16 a[148:149], v225 offset:17408\n\t"
    "v_mul_f32 v101, v243, v101\n\t"
    "v_mfma_f32_16x16x32_bf16 v[144:147], a[108:111], a[44:47], v[144:147]\n\t"
    "ds_read_b64_tr_b16 a[150:151], v225 offset:21504\n\t"
    "v_mul_f32 v102, v243, v102\n\t"
    "v_mfma_f32_16x16x32_bf16 v[148:151], a[112:115], a[32:35], v[192:195]\n\t"
    "ds_read_b64_tr_b16 a[152:153], v226 offset:17920\n\t"
    "v_mul_f32 v103, v243, v103\n\t"
    "v_mfma_f32_16x16x32_bf16 v[148:151], a[116:119], a[36:39], v[148:151]\n\t"
    "ds_read_b64_tr_b16 a[154:155], v226 offset:22016\n\t"
    "v_cvt_pk_bf16_f32 v100, v100, v101\n\t"
    "v_mfma_f32_16x16x32_bf16 v[148:151], a[120:123], a[40:43], v[148:151]\n\t"
    "ds_read_b64_tr_b16 a[156:157], v225 offset:17920\n\t"
    "v_cvt_pk_bf16_f32 v101, v102, v103\n\t"
    "v_mfma_f32_16x16x32_bf16 v[148:151], a[124:127], a[44:47], v[148:151]\n\t"
    "ds_read_b64_tr_b16 a[158:159], v225 offset:22016\n\t"
    "v_mul_f32 v104, v243, v104\n\t"
    "v_mfma_f32_16x16x32_bf16 v[152:155], a[96:99], a[48:51], v[196:199]\n\t"
    "v_mul_f32 v105, v243, v105\n\t"
    "v_mul_f32 v106, v243, v106\n\t"
    "v_mfma_f32_16x16x32_bf16 v[152:155], a[100:103], a[52:55], v[152:155]\n\t"
    "v_mul_f32 v107, v243, v107\n\t"
    "v_cvt_pk_bf16_f32 v104, v104, v105\n\t"
    "v_mfma_f32_16x16x32_bf16 v[152:155], a[104:107], a[56:59], v[152:155]\n\t"
    "v_cvt_pk_bf16_f32 v105, v106, v107\n\t"
    "v_mul_f32 v108, v243, v108\n\t"
    "v_mfma_f32_16x16x32_bf16 v[152:155], a[108:111], a[60:63], v[152:155]\n\t"
    "v_mul_f32 v109, v243, v109\n\t"
    "v_mul_f32 v110, v243, v110\n\t"
    "v_mfma_f32_16x16x32_bf16 v[156:159], a[112:115], a[48:51], v[196:199]\n\t"
    "v_mul_f32 v111, v243, v111\n\t"
    "v_cvt_pk_bf16_f32 v108, v108, v109\n\t"
    "v_mfma_f32_16x16x32_bf16 v[156:159], a[116:119], a[52:55], v[156:159]\n\t"
    "v_cvt_pk_bf16_f32 v109, v110, v111\n\t"
    "v_mul_f32 v112, v243, v112\n\t"
    "v_mfma_f32_16x16x32_bf16 v[156:159], a[120:123], a[56:59], v[156:159]\n\t"
    "v_exp_f32 v130, v130\n\t"
    "v_mfma_f32_16x16x32_bf16 v[156:159], a[124:127], a[60:63], v[156:159]\n\t"
    "v_cmp_ngt_f32 vcc, s44, v232\n\t"
    "s_cmp_lg_u64 vcc, 0\n\t"
    "s_cselect_b32 s45, 4, 0\n\t"
    "s_or_b32 s41, s41, s45\n\t"
    "v_max3_f32 v232, v163, v164, v165\n\t"
    "v_max_f32 v229, v229, v232\n\t"
    "v_mov_b32 v232, v229\n\t"
    "v_exp_f32 v131, v131\n\t"
    "v_exp_f32 v132, v132\n\t"
    "v_permlane16_swap_b32 v229, v232\n\t"
    "v_max_f32 v229, v229, v232\n\t"
    "v_mov_b32 v232, v229\n\t"
    "v_exp_f32 v133, v133\n\t"
    "v_exp_f32 v134, v134\n\t"
    "v_permlane32_swap_b32 v229, v232\n\t"
    "v_min_f32_e64 v200, -v229, -v232\n\t"
    "v_min_f32_e64 v201, -v229, -v232\n\t"
    "v_min_f32_e64 v202, -v229, -v232\n\t"
    "v_min_f32_e64 v203, -v229, -v232\n\t"
    "v_mov_b32 v229, v216\n\t"
    "v_cvt_pk_bf16_f32 v129, v130, v131\n\t"
    "v_mfma_f32_16x16x32_bf16 v[176:179], a[96:99], a[64:67], v[200:203]\n\t"
    "v_rcp_f32 v242, v229\n\t"
    "v_mfma_f32_16x16x32_bf16 v[176:179], a[100:103], a[68:71], v[176:179]\n\t"
    "v_mul_f32 v64, v242, v64\n\t"
    "v_mul_f32 v65, v242, v65\n\t"
    "v_mfma_f32_16x16x32_bf16 v[176:179], a[104:107], a[72:75], v[176:179]\n\t"
    "v_mul_f32 v66, v242, v66\n\t"
    "v_mul_f32 v67, v242, v67\n\t"
    "v_mfma_f32_16x16x32_bf16 v[176:179], a[108:111], a[76:79], v[176:179]\n\t"
    "v_cvt_pk_bf16_f32 v64, v64, v65\n\t"
    "v_cvt_pk_bf16_f32 v65, v66, v67\n\t"
    "ds_write_b64 v234, v[64:65] offset:0\n\t"
    "v_mfma_f32_16x16x32_bf16 v[180:183], a[112:115], a[64:67], v[200:203]\n\t"
    "v_mul_f32 v68, v242, v68\n\t"
    "v_mul_f32 v69, v242, v69\n\t"
    "v_mfma_f32_16x16x32_bf16 v[180:183], a[116:119], a[68:71], v[180:183]\n\t"
    "ds_write_b64 v234, v[96:97] offset:2048\n\t"
    "v_mfma_f32_16x16x32_bf16 v[180:183], a[120:123], a[72:75], v[180:183]\n\t"
    "v_mul_f32 v70, v242, v70\n\t"
    "v_mul_f32 v71, v242, v71\n\t"
    "v_mfma_f32_16x16x32_bf16 v[180:183], a[124:127], a[76:79], v[180:183]\n\t"
    "v_cvt_pk_bf16_f32 v68, v68, v69\n\t"
    "v_cvt_pk_bf16_f32 v69, v70, v71\n\t"
    "ds_write_b64 v237, v[68:69] offset:0\n\t"
    "v_mfma_f32_16x16x32_bf16 v[184:187], a[96:99], a[80:83], v[204:207]\n\t"
    "ds_read_b128 a[96:99], v224 offset:49152\n\t"
    "v_mul_f32 v72, v242, v72\n\t"
    "v_mfma_f32_16x16x32_bf16 v[184:187], a[100:103], a[84:87], v[184:187]\n\t"
    "ds_write_b64 v237, v[100:101] offset:2048\n\t"
    "v_mfma_f32_16x16x32_bf16 v[184:187], a[104:107], a[88:91], v[184:187]\n\t"
    "ds_read_b128 a[100:103], v224 offset:50176\n\t"
    "v_mul_f32 v73, v242, v73\n\t"
    "v_mfma_f32_16x16x32_bf16 v[184:187], a[108:111], a[92:95], v[184:187]\n\t"
    "ds_read_b128 a[104:107], v224 offset:51200\n\t"
    "v_mul_f32 v74, v242, v74\n\t"
    "v_mfma_f32_16x16x32_bf16 v[188:191], a[112:115], a[80:83], v[204:207]\n\t"
    "ds_read_b128 a[108:111], v224 offset:52224\n\t"
    "v_mul_f32 v75, v242, v75\n\t"
    "v_mfma_f32_16x16x32_bf16 v[188:191], a[116:119], a[84:87], v[188:191]\n\t"
    "ds_read_b128 a[112:115], v224 offset:53248\n\t"
    "v_cvt_pk_bf16_f32 v72, v72, v73\n\t"
    "v_mfma_f32_16x16x32_bf16 v[188:191], a[120:123], a[88:91], v[188:191]\n\t"
    "ds_read_b128 a[116:119], v224 offset:54272\n\t"
    "v_cvt_pk_bf16_f32 v73, v74, v75\n\t"
    "ds_write_b64 v246, v[72:73] offset:0\n\t"
    "v_mfma_f32_16x16x32_bf16 v[188:191], a[124:127], a[92:95], v[188:191]\n\t"
    "s_waitcnt lgkmcnt(10)\n\t"
    "ds_write_b64 v246, v[104:105] offset:2048\n\t"
    "v_mul_f32 v76, v242, v76\n\t"
    "v_mul_f32 v77, v242, v77\n\t"
    "v_mul_f32 v78, v242, v78\n\t"
    "v_mul_f32 v79, v242, v79\n\t"
    "v_cvt_pk_bf16_f32 v76, v76, v77\n\t"
    "v_cvt_pk_bf16_f32 v77, v78, v79\n\t"
    "ds_write_b64 v247, v[76:77] offset:0\n\t"
    "ds_write_b64 v247, v[108:109] offset:2048\n\t"
    "v_exp_f32 v135, v135\n\t"
    "ds_read_b128 a[160:163], v235 offset:0\n\t"
    "v_cvt_pk_bf16_f32 v130, v132, v133\n\t"
    "v_cvt_pk_bf16_f32 v131, v134, v135\n\t"
    "v_exp_f32 v136, v136\n\t"
    "s_nop 0\n\t"
    "v_mfma_f32_16x16x32_bf16 v[0:3], a[128:131], v[128:131], v[0:3]\n\t"
    "ds_read_b128 a[120:123], v224 offset:55296\n\t"
    "v_mul_f32 v80, v242, v80\n\t"
    "v_mfma_f32_16x16x32_bf16 v[4:7], a[132:135], v[128:131], v[4:7]\n\t"
    "ds_read_b128 a[124:127], v224 offset:56320\n\t"
    "v_mul_f32 v81, v242, v81\n\t"
    "v_mfma_f32_16x16x32_bf16 v[8:11], a[136:139], v[128:131], v[8:11]\n\t"
    "v_exp_f32 v137, v137\n\t"
    "v_mfma_f32_16x16x32_bf16 v[12:15], a[140:143], v[128:131], v[12:15]\n\t"
    "ds_read2_b64 a[164:167], v235 offset0:129 offset1:128\n\t"
    "v_cvt_pk_bf16_f32 v136, v136, v137\n\t"
    "v_mfma_f32_16x16x32_bf16 v[16:19], a[144:147], v[128:131], v[16:19]\n\t"
    "v_exp_f32 v138, v138\n\t"
    "v_mfma_f32_16x16x32_bf16 v[20:23], a[148:151], v[128:131], v[20:23]\n\t"
    "ds_read_b128 a[168:171], v235 offset:2048\n\t"
    "v_mul_f32 v82, v242, v82\n\t"
    "v_mfma_f32_16x16x32_bf16 v[24:27], a[152:155], v[128:131], v[24:27]\n\t"
    "v_exp_f32 v139, v139\n\t"
    "v_mfma_f32_16x16x32_bf16 v[28:31], a[156:159], v[128:131], v[28:31]\n\t"
    "ds_read2_b64 a[172:175], v245 offset0:129 offset1:128\n\t"
    "v_exp_f32 v140, v140\n\t"
    "v_exp_f32 v141, v141\n\t"
    "v_exp_f32 v142, v142\n\t"
    "v_exp_f32 v143, v143\n\t"
    "v_cvt_pk_bf16_f32 v137, v138, v139\n\t"
    "v_cvt_pk_bf16_f32 v138, v140, v141\n\t"
    "v_cvt_pk_bf16_f32 v139, v142, v143\n\t"
    "v_mul_f32 v83, v242, v83\n\t"
    "v_cvt_pk_bf16_f32 v80, v80, v81\n\t"
    "v_mfma_f32_16x16x32_bf16 v[32:35], a[128:131], v[136:139], v[32:35]\n\t"
    "v_cvt_pk_bf16_f32 v81, v82, v83\n\t"
    "v_mul_f32 v113, v243, v113\n\t"
    "v_mfma_f32_16x16x32_bf16 v[36:39], a[132:135], v[136:139], v[36:39]\n\t"
    "v_mul_f32 v114, v243, v114\n\t"
    "v_mul_f32 v115, v243, v115\n\t"
    "v_mfma_f32_16x16x32_bf16 v[40:43], a[136:139], v[136:139], v[40:43]\n\t"
    "v_cvt_pk_bf16_f32 v112, v112, v113\n\t"
    "v_cvt_pk_bf16_f32 v113, v114, v115\n\t"
    "v_mfma_f32_16x16x32_bf16 v[44:47], a[140:143], v[136:139], v[44:47]\n\t"
    "v_mul_f32 v84, v242, v84\n\t"
    "v_mul_f32 v85, v242, v85\n\t"
    "v_mfma_f32_16x16x32_bf16 v[48:51], a[144:147], v[136:139], v[48:51]\n\t"
    "v_mul_f32 v86, v242, v86\n\t"
    "v_mul_f32 v87, v242, v87\n\t"
    "v_mfma_f32_16x16x32_bf16 v[52:55], a[148:151], v[136:139], v[52:55]\n\t"
    "ds_write_b64 v234, v[80:81] offset:0\n\t"
    "v_mfma_f32_16x16x32_bf16 v[56:59], a[152:155], v[136:139], v[56:59]\n\t"
    "v_cvt_pk_bf16_f32 v84, v84, v85\n\t"
    "v_cvt_pk_bf16_f32 v85, v86, v87\n\t"
    "v_mfma_f32_16x16x32_bf16 v[60:63], a[156:159], v[136:139], v[60:63]\n\t"
    "ds_write_b64 v234, v[112:113] offset:2048\n\t"
    "v_mfma_f32_16x16x32_bf16 v[208:211], v[252:255], v[128:131], v[208:211]\n\t"
    "v_mul_f32 v116, v243, v116\n\t"
    "v_mul_f32 v117, v243, v117\n\t"
    "v_mfma_f32_16x16x32_bf16 v[212:215], v[252:255], v[136:139], v[212:215]\n\t"
    "s_waitcnt lgkmcnt(3)\n\t"
    "ds_write_b64 v237, v[84:85] offset:0\n\t"
    "v_mfma_f32_16x16x32_bf16 v[128:131], a[96:99], a[32:35], v[192:195]\n\t"
    "v_mul_f32 v118, v243, v118\n\t"
    "v_mul_f32 v119, v243, v119\n\t"
    "v_mfma_f32_16x16x32_bf16 v[128:131], a[100:103], a[36:39], v[128:131]\n\t"
    "v_cvt_pk_bf16_f32 v116, v116, v117\n\t"
    "v_cvt_pk_bf16_f32 v117, v118, v119\n\t"
    "ds_write_b64 v237, v[116:117] offset:2048\n\t"
    "v_mfma_f32_16x16x32_bf16 v[128:131], a[104:107], a[40:43], v[128:131]\n\t"
    "v_mul_f32 v88, v242, v88\n\t"
    "v_mul_f32 v89, v242, v89\n\t"
    "v_mfma_f32_16x16x32_bf16 v[128:131], a[108:111], a[44:47], v[128:131]\n\t"
    "v_mul_f32 v90, v242, v90\n\t"
    "v_mul_f32 v91, v242, v91\n\t"
    "v_mfma_f32_16x16x32_bf16 v[132:135], a[112:115], a[32:35], v[192:195]\n\t"
    "v_cvt_pk_bf16_f32 v88, v88, v89\n\t"
    "v_cvt_pk_bf16_f32 v89, v90, v91\n\t"
    "ds_write_b64 v246, v[88:89] offset:0\n\t"
    "v_mfma_f32_16x16x32_bf16 v[132:135], a[116:119], a[36:39], v[132:135]\n\t"
    "v_mul_f32 v120, v243, v120\n\t"
    "v_mul_f32 v121, v243, v121\n\t"
    "v_mfma_f32_16x16x32_bf16 v[132:135], a[120:123], a[40:43], v[132:135]\n\t"
    "v_mul_f32 v122, v243, v122\n\t"
    "v_mul_f32 v123, v243, v123\n\t"
    "v_mfma_f32_16x16x32_bf16 v[132:135], a[124:127], a[44:47], v[132:135]\n\t"
    "v_cvt_pk_bf16_f32 v120, v120, v121\n\t"
    "v_cvt_pk_bf16_f32 v121, v122, v123\n\t"
    "ds_write_b64 v246, v[120:121] offset:2048\n\t"
    "v_mfma_f32_16x16x32_bf16 v[136:139], a[96:99], a[48:51], v[196:199]\n\t"
    "v_mul_f32 v92, v242, v92\n\t"
    "v_mul_f32 v93, v242, v93\n\t"
    "v_mfma_f32_16x16x32_bf16 v[136:139], a[100:103], a[52:55], v[136:139]\n\t"
    "v_mul_f32 v94, v242, v94\n\t"
    "v_mul_f32 v95, v242, v95\n\t"
    "v_mfma_f32_16x16x32_bf16 v[136:139], a[104:107], a[56:59], v[136:139]\n\t"
    "v_cvt_pk_bf16_f32 v92, v92, v93\n\t"
    "v_cvt_pk_bf16_f32 v93, v94, v95\n\t"
    "ds_write_b64 v247, v[92:93] offset:0\n\t"
    "v_mfma_f32_16x16x32_bf16 v[136:139], a[108:111], a[60:63], v[136:139]\n\t"
    "v_mul_f32 v124, v243, v124\n\t"
    "v_mul_f32 v125, v243, v125\n\t"
    "v_mfma_f32_16x16x32_bf16 v[140:143], a[112:115], a[48:51], v[196:199]\n\t"
    "v_mul_f32 v126, v243, v126\n\t"
    "v_mul_f32 v127, v243, v127\n\t"
    "v_mfma_f32_16x16x32_bf16 v[140:143], a[116:119], a[52:55], v[140:143]\n\t"
    "v_cvt_pk_bf16_f32 v124, v124, v125\n\t"
    "v_cvt_pk_bf16_f32 v125, v126, v127\n\t"
    "ds_write_b64 v247, v[124:125] offset:2048\n\t"
    "v_mfma_f32_16x16x32_bf16 v[140:143], a[120:123], a[56:59], v[140:143]\n\t"
    "v_add_f32 v160, v200, v160\n\t"
    "v_add_f32 v161, v201, v161\n\t"
    "v_mfma_f32_16x16x32_bf16 v[140:143], a[124:127], a[60:63], v[140:143]\n\t"
    "ds_read_b128 a[176:179], v235 offset:0\n\t"
    "ds_read2_b64 a[180:183], v235 offset0:129 offset1:128\n\t"
    "ds_read_b128 a[184:187], v235 offset:2048\n\t"
    "v_add_f32 v162, v202, v162\n\t"
    "v_add_f32 v163, v203, v163\n\t"
    "v_exp_f32 v160, v160\n\t"
    "v_exp_f32 v161, v161\n\t"
    "v_add_f32 v164, v200, v164\n\t"
    "v_add_f32 v165, v201, v165\n\t"
    "v_add_f32 v166, v202, v166\n\t"
    "v_add_f32 v167, v203, v167\n\t"
    "v_exp_f32 v162, v162\n\t"
    "v_exp_f32 v163, v163\n\t"
    "v_exp_f32 v164, v164\n\t"
    "v_exp_f32 v165, v165\n\t"
    "v_exp_f32 v166, v166\n\t"
    "v_exp_f32 v167, v167\n\t"
    "v_cvt_pk_bf16_f32 v160, v160, v161\n\t"
    "v_cvt_pk_bf16_f32 v161, v162, v163\n\t"
    "v_cvt_pk_bf16_f32 v162, v164, v165\n\t"
    "v_cvt_pk_bf16_f32 v163, v166, v167\n\t"
    "v_add_f32 v170, v206, v170\n\t"
    "v_add_f32 v171, v207, v171\n\t"
    "v_mfma_f32_16x16x32_bf16 v[64:67], a[128:131], v[160:163], 0\n\t"
    "v_exp_f32 v168, v168\n\t"
    "v_mfma_f32_16x16x32_bf16 v[68:71], a[132:135], v[160:163], 0\n\t"
    "ds_read2_b64 a[188:191], v245 offset0:129 offset1:128\n\t"
    "v_add_f32 v172, v204, v172\n\t"
    "v_mfma_f32_16x16x32_bf16 v[72:75], a[136:139], v[160:163], 0\n\t"
    "v_exp_f32 v169, v169\n\t"
    "v_mfma_f32_16x16x32_bf16 v[76:79], a[140:143], v[160:163], 0\n\t"
    "v_add_f32 v173, v205, v173\n\t"
    "v_add_f32 v174, v206, v174\n\t"
    "v_mfma_f32_16x16x32_bf16 v[80:83], a[144:147], v[160:163], 0\n\t"
    "v_add_f32 v175, v207, v175\n\t"
    "v_cvt_pk_bf16_f32 v168, v168, v169\n\t"
    "v_mfma_f32_16x16x32_bf16 v[84:87], a[148:151], v[160:163], 0\n\t"
    "v_exp_f32 v170, v170\n\t"
    "v_mfma_f32_16x16x32_bf16 v[88:91], a[152:155], v[160:163], 0\n\t"
    "v_exp_f32 v171, v171\n\t"
    "v_mfma_f32_16x16x32_bf16 v[92:95], a[156:159], v[160:163], 0\n\t"
    "v_exp_f32 v172, v172\n\t"
    "v_exp_f32 v173, v173\n\t"
    "v_exp_f32 v174, v174\n\t"
    "v_exp_f32 v175, v175\n\t"
    "v_cvt_pk_bf16_f32 v169, v170, v171\n\t"
    "v_cvt_pk_bf16_f32 v170, v172, v173\n\t"
    "v_cvt_pk_bf16_f32 v171, v174, v175\n\t"
    "v_exp_f32 v144, v144\n\t"
    "s_nop 0\n\t"
    "v_mfma_f32_16x16x32_bf16 v[96:99], a[128:131], v[168:171], 0\n\t"
    "ds_read_b64_tr_b16 a[128:129], v226 offset:24576\n\t"
    "v_add_f32 v232, v229, v233\n\t"
    "v_mfma_f32_16x16x32_bf16 v[100:103], a[132:135], v[168:171], 0\n\t"
    "ds_read_b64_tr_b16 a[130:131], v226 offset:28672\n\t"
    "ds_read_b64_tr_b16 a[132:133], v225 offset:24576\n\t"
    "ds_read_b64_tr_b16 a[134:135], v225 offset:28672\n\t"
    "v_mfma_f32_16x16x32_bf16 v[104:107], a[136:139], v[168:171], 0\n\t"
    "ds_read_b64_tr_b16 a[136:137], v226 offset:25088\n\t"
    "ds_read_b64_tr_b16 a[138:139], v226 offset:29184\n\t"
    "v_mfma_f32_16x16x32_bf16 v[108:111], a[140:143], v[168:171], 0\n\t"
    "ds_read_b64_tr_b16 a[140:141], v225 offset:25088\n\t"
    "ds_read_b64_tr_b16 a[142:143], v225 offset:29184\n\t"
    "v_mfma_f32_16x16x32_bf16 v[112:115], a[144:147], v[168:171], 0\n\t"
    "ds_read_b64_tr_b16 a[144:145], v226 offset:25600\n\t"
    "ds_read_b64_tr_b16 a[146:147], v226 offset:29696\n\t"
    "v_mfma_f32_16x16x32_bf16 v[116:119], a[148:151], v[168:171], 0\n\t"
    "ds_read_b64_tr_b16 a[148:149], v225 offset:25600\n\t"
    "ds_read_b64_tr_b16 a[150:151], v225 offset:29696\n\t"
    "v_mfma_f32_16x16x32_bf16 v[120:123], a[152:155], v[168:171], 0\n\t"
    "ds_read_b64_tr_b16 a[152:153], v226 offset:26112\n\t"
    "ds_read_b64_tr_b16 a[154:155], v226 offset:30208\n\t"
    "v_mfma_f32_16x16x32_bf16 v[124:127], a[156:159], v[168:171], 0\n\t"
    "ds_read_b64_tr_b16 a[156:157], v225 offset:26112\n\t"
    "ds_read_b64_tr_b16 a[158:159], v225 offset:30208\n\t"
    "v_mfma_f32_16x16x32_bf16 v[216:219], v[252:255], v[160:163], 0\n\t"
    "v_exp_f32 v145, v145\n\t"
    "v_mfma_f32_16x16x32_bf16 v[220:223], v[252:255], v[168:171], 0\n\t"
    "v_exp_f32 v146, v146\n\t"
    "v_mfma_f32_16x16x32_bf16 v[160:163], a[96:99], a[64:67], v[200:203]\n\t"
    "v_exp_f32 v147, v147\n\t"
    "v_mfma_f32_16x16x32_bf16 v[160:163], a[100:103], a[68:71], v[160:163]\n\t"
    "v_exp_f32 v148, v148\n\t"
    "v_mfma_f32_16x16x32_bf16 v[160:163], a[104:107], a[72:75], v[160:163]\n\t"
    "v_exp_f32 v149, v149\n\t"
    "v_mfma_f32_16x16x32_bf16 v[160:163], a[108:111], a[76:79], v[160:163]\n\t"
    "v_exp_f32 v150, v150\n\t"
    "v_mfma_f32_16x16x32_bf16 v[164:167], a[112:115], a[64:67], v[200:203]\n\t"
    "v_exp_f32 v151, v151\n\t"
    "v_mfma_f32_16x16x32_bf16 v[164:167], a[116:119], a[68:71], v[164:167]\n\t"
    "v_cvt_pk_bf16_f32 v144, v144, v145\n\t"
    "v_cvt_pk_bf16_f32 v145, v146, v147\n\t"
    "v_mfma_f32_16x16x32_bf16 v[164:167], a[120:123], a[72:75], v[164:167]\n\t"
    "s_waitcnt lgkmcnt(0)\n\t"
    "v_cvt_pk_bf16_f32 v146, v148, v149\n\t"
    "v_cvt_pk_bf16_f32 v147, v150, v151\n\t"
    "s_waitcnt vmcnt(0)\n\t"
    "s_barrier\n\t"
    "s_add_u32 m0, %8, 16384\n\t"
    "s_add_u32 s40, %9, 81920\n\t"
    "v_mfma_f32_16x16x32_bf16 v[164:167], a[124:127], a[76:79], v[164:167]\n\t"
    "v_exp_f32 v152, v152\n\t"
    "v_mfma_f32_16x16x32_bf16 v[168:171], a[96:99], a[80:83], v[204:207]\n\t"
    "ds_read_b128 a[96:99], v224 offset:57344\n\t"
    "v_mfma_f32_16x16x32_bf16 v[168:171], a[100:103], a[84:87], v[168:171]\n\t"
    "ds_read_b128 a[100:103], v224 offset:58368\n\t"
    "v_mfma_f32_16x16x32_bf16 v[168:171], a[104:107], a[88:91], v[168:171]\n\t"
    "buffer_load_dwordx4 v227, %3, s40 offen offset:0  lds\n\t"
    "v_mfma_f32_16x16x32_bf16 v[168:171], a[108:111], a[92:95], v[168:171]\n\t"
    "ds_read_b128 a[104:107], v224 offset:59392\n\t"
    "v_mfma_f32_16x16x32_bf16 v[172:175], a[112:115], a[80:83], v[204:207]\n\t"
    "ds_read_b128 a[108:111], v224 offset:60416\n\t"
    "v_mfma_f32_16x16x32_bf16 v[172:175], a[116:119], a[84:87], v[172:175]\n\t"
    "ds_read_b128 a[112:115], v224 offset:61440\n\t"
    "v_mfma_f32_16x16x32_bf16 v[172:175], a[120:123], a[88:91], v[172:175]\n\t"
    "ds_read_b128 a[116:119], v224 offset:62464\n\t"
    "v_mfma_f32_16x16x32_bf16 v[172:175], a[124:127], a[92:95], v[172:175]\n\t"
    "ds_read_b128 a[120:123], v224 offset:63488\n\t"
    "v_mfma_f32_16x16x32_bf16 v[0:3], a[128:131], v[144:147], v[0:3]\n\t"
    "buffer_load_dwordx4 v248, %3, s40 offen offset:1024  lds\n\t"
    "s_add_u32 m0, %8, 24576\n\t"
    "s_add_u32 s40, %9, 90112\n\t"
    "v_mfma_f32_16x16x32_bf16 v[4:7], a[132:135], v[144:147], v[4:7]\n\t"
    "ds_read_b128 a[124:127], v224 offset:64512\n\t"
    "v_mfma_f32_16x16x32_bf16 v[8:11], a[136:139], v[144:147], v[8:11]\n\t"
    "v_exp_f32 v153, v153\n\t"
    "v_mfma_f32_16x16x32_bf16 v[12:15], a[140:143], v[144:147], v[12:15]\n\t"
    "v_exp_f32 v154, v154\n\t"
    "v_mfma_f32_16x16x32_bf16 v[16:19], a[144:147], v[144:147], v[16:19]\n\t"
    "v_exp_f32 v155, v155\n\t"
    "v_mfma_f32_16x16x32_bf16 v[20:23], a[148:151], v[144:147], v[20:23]\n\t"
    "v_exp_f32 v156, v156\n\t"
    "v_mfma_f32_16x16x32_bf16 v[24:27], a[152:155], v[144:147], v[24:27]\n\t"
    "buffer_load_dwordx4 v227, %3, s40 offen offset:0  lds\n\t"
    "v_mfma_f32_16x16x32_bf16 v[28:31], a[156:159], v[144:147], v[28:31]\n\t"
    "buffer_load_dwordx4 v248, %3, s40 offen offset:1024  lds\n\t"
    "v_exp_f32 v157, v157\n\t"
    "v_exp_f32 v158, v158\n\t"
    "v_exp_f32 v159, v159\n\t"
    "v_cvt_pk_bf16_f32 v152, v152, v153\n\t"
    "v_cvt_pk_bf16_f32 v153, v154, v155\n\t"
    "v_cvt_pk_bf16_f32 v154, v156, v157\n\t"
    "v_cvt_pk_bf16_f32 v155, v158, v159\n\t"
    "v_exp_f32 v176, v176\n\t"
    "s_nop 0\n\t"
    "v_mfma_f32_16x16x32_bf16 v[32:35], a[128:131], v[152:155], v[32:35]\n\t"
    "v_exp_f32 v177, v177\n\t"
    "v_mfma_f32_16x16x32_bf16 v[36:39], a[132:135], v[152:155], v[36:39]\n\t"
    "v_exp_f32 v178, v178\n\t"
    "v_mfma_f32_16x16x32_bf16 v[40:43], a[136:139], v[152:155], v[40:43]\n\t"
    "v_exp_f32 v179, v179\n\t"
    "v_mfma_f32_16x16x32_bf16 v[44:47], a[140:143], v[152:155], v[44:47]\n\t"
    "v_exp_f32 v180, v180\n\t"
    "v_mfma_f32_16x16x32_bf16 v[48:51], a[144:147], v[152:155], v[48:51]\n\t"
    "v_exp_f32 v181, v181\n\t"
    "v_mfma_f32_16x16x32_bf16 v[52:55], a[148:151], v[152:155], v[52:55]\n\t"
    "v_exp_f32 v182, v182\n\t"
    "v_mfma_f32_16x16x32_bf16 v[56:59], a[152:155], v[152:155], v[56:59]\n\t"
    "v_exp_f32 v183, v183\n\t"
    "v_mfma_f32_16x16x32_bf16 v[60:63], a[156:159], v[152:155], v[60:63]\n\t"
    "v_cvt_pk_bf16_f32 v176, v176, v177\n\t"
    "v_cvt_pk_bf16_f32 v177, v178, v179\n\t"
    "v_mfma_f32_16x16x32_bf16 v[208:211], v[252:255], v[144:147], v[208:211]\n\t"
    "v_cvt_pk_bf16_f32 v178, v180, v181\n\t"
    "v_cvt_pk_bf16_f32 v179, v182, v183\n\t"
    "v_mfma_f32_16x16x32_bf16 v[212:215], v[252:255], v[152:155], v[212:215]\n\t"
    "v_exp_f32 v184, v184\n\t"
    "v_mfma_f32_16x16x32_bf16 v[64:67], a[128:131], v[176:179], v[64:67]\n\t"
    "v_exp_f32 v185, v185\n\t"
    "v_mfma_f32_16x16x32_bf16 v[68:71], a[132:135], v[176:179], v[68:71]\n\t"
    "v_exp_f32 v186, v186\n\t"
    "v_mfma_f32_16x16x32_bf16 v[72:75], a[136:139], v[176:179], v[72:75]\n\t"
    "v_exp_f32 v187, v187\n\t"
    "v_mfma_f32_16x16x32_bf16 v[76:79], a[140:143], v[176:179], v[76:79]\n\t"
    "v_exp_f32 v188, v188\n\t"
    "v_mfma_f32_16x16x32_bf16 v[80:83], a[144:147], v[176:179], v[80:83]\n\t"
    "v_exp_f32 v189, v189\n\t"
    "v_mfma_f32_16x16x32_bf16 v[84:87], a[148:151], v[176:179], v[84:87]\n\t"
    "v_exp_f32 v190, v190\n\t"
    "v_mfma_f32_16x16x32_bf16 v[88:91], a[152:155], v[176:179], v[88:91]\n\t"
    "v_exp_f32 v191, v191\n\t"
    "v_mfma_f32_16x16x32_bf16 v[92:95], a[156:159], v[176:179], v[92:95]\n\t"
    "v_cvt_pk_bf16_f32 v184, v184, v185\n\t"
    "v_cvt_pk_bf16_f32 v185, v186, v187\n\t"
    "v_cvt_pk_bf16_f32 v186, v188, v189\n\t"
    "v_cvt_pk_bf16_f32 v187, v190, v191\n\t"
    "v_exp_f32 v128, v128\n\t"
    "s_nop 0\n\t"
    "v_mfma_f32_16x16x32_bf16 v[96:99], a[128:131], v[184:187], v[96:99]\n\t"
    "ds_read_b64_tr_b16 a[128:129], v226 offset:0\n\t"
    "v_mfma_f32_16x16x32_bf16 v[100:103], a[132:135], v[184:187], v[100:103]\n\t"
    "ds_read_b64_tr_b16 a[130:131], v226 offset:4096\n\t"
    "v_mfma_f32_16x16x32_bf16 v[104:107], a[136:139], v[184:187], v[104:107]\n\t"
    "s_add_u32 s40, %11, 73728\n\t"
    "ds_read_b64_tr_b16 a[132:133], v225 offset:0\n\t"
    "v_mfma_f32_16x16x32_bf16 v[108:111], a[140:143], v[184:187], v[108:111]\n\t"
    "ds_read_b64_tr_b16 a[134:135], v225 offset:4096\n\t"
    "v_mfma_f32_16x16x32_bf16 v[112:115], a[144:147], v[184:187], v[112:115]\n\t"
    "ds_read_b64_tr_b16 a[136:137], v226 offset:512\n\t"
    "v_mfma_f32_16x16x32_bf16 v[116:119], a[148:151], v[184:187], v[116:119]\n\t"
    "ds_read_b64_tr_b16 a[138:139], v226 offset:4608\n\t"
    "v_mfma_f32_16x16x32_bf16 v[120:123], a[152:155], v[184:187], v[120:123]\n\t"
    "ds_read_b64_tr_b16 a[140:141], v225 offset:512\n\t"
    "v_mfma_f32_16x16x32_bf16 v[124:127], a[156:159], v[184:187], v[124:127]\n\t"
    "ds_read_b64_tr_b16 a[142:143], v225 offset:4608\n\t"
    "v_mfma_f32_16x16x32_bf16 v[216:219], v[252:255], v[176:179], v[216:219]\n\t"
    "ds_read_b64_tr_b16 a[144:145], v226 offset:1024\n\t"
    "v_mfma_f32_16x16x32_bf16 v[220:223], v[252:255], v[184:187], v[220:223]\n\t"
    "s_waitcnt lgkmcnt(9)\n\t"
    "ds_read_b64_tr_b16 a[146:147], v226 offset:5120\n\t"
    "v_mfma_f32_16x16x32_bf16 v[144:147], a[96:99], a[32:35], v[192:195]\n\t"
    "ds_read_b64_tr_b16 a[148:149], v225 offset:1024\n\t"
    "v_mfma_f32_16x16x32_bf16 v[144:147], a[100:103], a[36:39], v[144:147]\n\t"
    "ds_read_b64_tr_b16 a[150:151], v225 offset:5120\n\t"
    "v_mfma_f32_16x16x32_bf16 v[144:147], a[104:107], a[40:43], v[144:147]\n\t"
    "ds_read_b64_tr_b16 a[152:153], v226 offset:1536\n\t"
    "v_mfma_f32_16x16x32_bf16 v[144:147], a[108:111], a[44:47], v[144:147]\n\t"
    "ds_read_b64_tr_b16 a[154:155], v226 offset:5632\n\t"
    "v_mfma_f32_16x16x32_bf16 v[148:151], a[112:115], a[32:35], v[192:195]\n\t"
    "ds_read_b64_tr_b16 a[156:157], v225 offset:1536\n\t"
    "v_mfma_f32_16x16x32_bf16 v[148:151], a[116:119], a[36:39], v[148:151]\n\t"
    "ds_read_b64_tr_b16 a[158:159], v225 offset:5632\n\t"
    "v_mfma_f32_16x16x32_bf16 v[148:151], a[120:123], a[40:43], v[148:151]\n\t"
    "v_exp_f32 v129, v129\n\t"
    "v_mfma_f32_16x16x32_bf16 v[148:151], a[124:127], a[44:47], v[148:151]\n\t"
    "v_exp_f32 v130, v130\n\t"
    "v_mfma_f32_16x16x32_bf16 v[152:155], a[96:99], a[48:51], v[196:199]\n\t"
    "v_exp_f32 v131, v131\n\t"
    "v_mfma_f32_16x16x32_bf16 v[152:155], a[100:103], a[52:55], v[152:155]\n\t"
    "v_exp_f32 v132, v132\n\t"
    "v_mfma_f32_16x16x32_bf16 v[152:155], a[104:107], a[56:59], v[152:155]\n\t"
    "v_exp_f32 v133, v133\n\t"
    "v_mfma_f32_16x16x32_bf16 v[152:155], a[108:111], a[60:63], v[152:155]\n\t"
    "v_exp_f32 v134, v134\n\t"
    "v_mfma_f32_16x16x32_bf16 v[156:159], a[112:115], a[48:51], v[196:199]\n\t"
    "v_exp_f32 v135, v135\n\t"
    "v_mfma_f32_16x16x32_bf16 v[156:159], a[116:119], a[52:55], v[156:159]\n\t"
    "v_cvt_pk_bf16_f32 v128, v128, v129\n\t"
    "v_cvt_pk_bf16_f32 v129, v130, v131\n\t"
    "v_mfma_f32_16x16x32_bf16 v[156:159], a[120:123], a[56:59], v[156:159]\n\t"
    "v_cvt_pk_bf16_f32 v130, v132, v133\n\t"
    "v_cvt_pk_bf16_f32 v131, v134, v135\n\t"
    "v_mfma_f32_16x16x32_bf16 v[156:159], a[124:127], a[60:63], v[156:159]\n\t"
    "v_exp_f32 v136, v136\n\t"
    "v_mfma_f32_16x16x32_bf16 v[176:179], a[96:99], a[64:67], v[200:203]\n\t"
    "v_exp_f32 v137, v137\n\t"
    "v_mfma_f32_16x16x32_bf16 v[176:179], a[100:103], a[68:71], v[176:179]\n\t"
    "buffer_store_dwordx4 a[160:163], v236, %4, s40 offen offset:0 sc1\n\t"
    "v_mfma_f32_16x16x32_bf16 v[176:179], a[104:107], a[72:75], v[176:179]\n\t"
    "v_exp_f32 v138, v138\n\t"
    "v_mfma_f32_16x16x32_bf16 v[176:179], a[108:111], a[76:79], v[176:179]\n\t"
    "v_exp_f32 v139, v139\n\t"
    "v_mfma_f32_16x16x32_bf16 v[180:183], a[112:115], a[64:67], v[200:203]\n\t"
    "v_exp_f32 v140, v140\n\t"
    "v_mfma_f32_16x16x32_bf16 v[180:183], a[116:119], a[68:71], v[180:183]\n\t"
    "v_exp_f32 v141, v141\n\t"
    "v_mfma_f32_16x16x32_bf16 v[180:183], a[120:123], a[72:75], v[180:183]\n\t"
    "v_exp_f32 v142, v142\n\t"
    "v_mfma_f32_16x16x32_bf16 v[180:183], a[124:127], a[76:79], v[180:183]\n\t"
    "v_exp_f32 v143, v143\n\t"
    "v_mfma_f32_16x16x32_bf16 v[184:187], a[96:99], a[80:83], v[204:207]\n\t"
    "ds_read_b128 a[96:99], v244 offset:0\n\t"
    "buffer_store_dwordx4 a[164:167], v236, %4, s40 offen offset:2048 sc1\n\t"
    "s_add_u32 s40, %11, 77824\n\t"
    "v_mfma_f32_16x16x32_bf16 v[184:187], a[100:103], a[84:87], v[184:187]\n\t"
    "ds_read_b128 a[100:103], v244 offset:1024\n\t"
    "v_cvt_pk_bf16_f32 v136, v136, v137\n\t"
    "v_mfma_f32_16x16x32_bf16 v[184:187], a[104:107], a[88:91], v[184:187]\n\t"
    "ds_read_b128 a[104:107], v244 offset:2048\n\t"
    "v_cvt_pk_bf16_f32 v137, v138, v139\n\t"
    "v_mfma_f32_16x16x32_bf16 v[184:187], a[108:111], a[92:95], v[184:187]\n\t"
    "ds_read_b128 a[108:111], v244 offset:3072\n\t"
    "v_cvt_pk_bf16_f32 v138, v140, v141\n\t"
    "v_mfma_f32_16x16x32_bf16 v[188:191], a[112:115], a[80:83], v[204:207]\n\t"
    "ds_read_b128 a[112:115], v244 offset:4096\n\t"
    "v_cvt_pk_bf16_f32 v139, v142, v143\n\t"
    "v_mfma_f32_16x16x32_bf16 v[188:191], a[116:119], a[84:87], v[188:191]\n\t"
    "ds_read_b128 a[116:119], v244 offset:5120\n\t"
    "v_mfma_f32_16x16x32_bf16 v[188:191], a[120:123], a[88:91], v[188:191]\n\t"
    "ds_read_b128 a[120:123], v244 offset:6144\n\t"
    "v_mfma_f32_16x16x32_bf16 v[188:191], a[124:127], a[92:95], v[188:191]\n\t"
    "s_waitcnt lgkmcnt(7)\n\t"
    "ds_read_b128 a[124:127], v244 offset:7168\n\t"
    "v_mfma_f32_16x16x32_bf16 v[0:3], a[128:131], v[128:131], v[0:3]\n\t"
    "v_exp_f32 v160, v160\n\t"
    "v_mfma_f32_16x16x32_bf16 v[4:7], a[132:135], v[128:131], v[4:7]\n\t"
    "buffer_store_dwordx4 a[168:171], v236, %4, s40 offen offset:0 sc1\n\t"
    "v_mfma_f32_16x16x32_bf16 v[8:11], a[136:139], v[128:131], v[8:11]\n\t"
    "v_exp_f32 v161, v161\n\t"
    "v_mfma_f32_16x16x32_bf16 v[12:15], a[140:143], v[128:131], v[12:15]\n\t"
    "v_exp_f32 v162, v162\n\t"
    "v_mfma_f32_16x16x32_bf16 v[16:19], a[144:147], v[128:131], v[16:19]\n\t"
    "v_exp_f32 v163, v163\n\t"
    "v_mfma_f32_16x16x32_bf16 v[20:23], a[148:151], v[128:131], v[20:23]\n\t"
    "v_exp_f32 v164, v164\n\t"
    "v_mfma_f32_16x16x32_bf16 v[24:27], a[152:155], v[128:131], v[24:27]\n\t"
    "v_exp_f32 v165, v165\n\t"
    "v_mfma_f32_16x16x32_bf16 v[28:31], a[156:159], v[128:131], v[28:31]\n\t"
    "v_exp_f32 v166, v166\n\t"
    "v_mfma_f32_16x16x32_bf16 v[32:35], a[128:131], v[136:139], v[32:35]\n\t"
    "v_exp_f32 v167, v167\n\t"
    "v_mfma_f32_16x16x32_bf16 v[36:39], a[132:135], v[136:139], v[36:39]\n\t"
    "buffer_store_dwordx4 a[172:175], v236, %4, s40 offen offset:2048 sc1\n\t"
    "v_mfma_f32_16x16x32_bf16 v[40:43], a[136:139], v[136:139], v[40:43]\n\t"
    "v_cvt_pk_bf16_f32 v160, v160, v161\n\t"
    "v_cvt_pk_bf16_f32 v161, v162, v163\n\t"
    "v_mfma_f32_16x16x32_bf16 v[44:47], a[140:143], v[136:139], v[44:47]\n\t"
    "v_cvt_pk_bf16_f32 v162, v164, v165\n\t"
    "v_cvt_pk_bf16_f32 v163, v166, v167\n\t"
    "v_mfma_f32_16x16x32_bf16 v[48:51], a[144:147], v[136:139], v[48:51]\n\t"
    "v_exp_f32 v168, v168\n\t"
    "v_mfma_f32_16x16x32_bf16 v[52:55], a[148:151], v[136:139], v[52:55]\n\t"
    "v_exp_f32 v169, v169\n\t"
    "v_mfma_f32_16x16x32_bf16 v[56:59], a[152:155], v[136:139], v[56:59]\n\t"
    "v_exp_f32 v170, v170\n\t"
    "v_mfma_f32_16x16x32_bf16 v[60:63], a[156:159], v[136:139], v[60:63]\n\t"
    "v_exp_f32 v171, v171\n\t"
    "v_mfma_f32_16x16x32_bf16 v[208:211], v[252:255], v[128:131], v[208:211]\n\t"
    "v_exp_f32 v172, v172\n\t"
    "v_mfma_f32_16x16x32_bf16 v[212:215], v[252:255], v[136:139], v[212:215]\n\t"
    "v_exp_f32 v173, v173\n\t"
    "v_mfma_f32_16x16x32_bf16 v[64:67], a[128:131], v[160:163], v[64:67]\n\t"
    "v_exp_f32 v174, v174\n\t"
    "v_mfma_f32_16x16x32_bf16 v[68:71], a[132:135], v[160:163], v[68:71]\n\t"
    "v_exp_f32 v175, v175\n\t"
    "v_mfma_f32_16x16x32_bf16 v[72:75], a[136:139], v[160:163], v[72:75]\n\t"
    "v_cvt_pk_bf16_f32 v168, v168, v169\n\t"
    "v_cvt_pk_bf16_f32 v169, v170, v171\n\t"
    "v_mfma_f32_16x16x32_bf16 v[76:79], a[140:143], v[160:163], v[76:79]\n\t"
    "v_cvt_pk_bf16_f32 v170, v172, v173\n\t"
    "v_cvt_pk_bf16_f32 v171, v174, v175\n\t"
    "v_mfma_f32_16x16x32_bf16 v[80:83], a[144:147], v[160:163], v[80:83]\n\t"
    "v_exp_f32 v144, v144\n\t"
    "v_mfma_f32_16x16x32_bf16 v[84:87], a[148:151], v[160:163], v[84:87]\n\t"
    "v_exp_f32 v145, v145\n\t"
    "v_mfma_f32_16x16x32_bf16 v[88:91], a[152:155], v[160:163], v[88:91]\n\t"
    "v_exp_f32 v146, v146\n\t"
    "v_mfma_f32_16x16x32_bf16 v[92:95], a[156:159], v[160:163], v[92:95]\n\t"
    "v_exp_f32 v147, v147\n\t"
    "v_mfma_f32_16x16x32_bf16 v[96:99], a[128:131], v[168:171], v[96:99]\n\t"
    "ds_read_b64_tr_b16 a[128:129], v226 offset:8192\n\t"
    "v_cvt_pk_bf16_f32 v144, v144, v145\n\t"
    "v_mfma_f32_16x16x32_bf16 v[100:103], a[132:135], v[168:171], v[100:103]\n\t"
    "ds_read_b64_tr_b16 a[130:131], v226 offset:12288\n\t"
    "v_cvt_pk_bf16_f32 v145, v146, v147\n\t"
    "v_mfma_f32_16x16x32_bf16 v[104:107], a[136:139], v[168:171], v[104:107]\n\t"
    "ds_read_b64_tr_b16 a[132:133], v225 offset:8192\n\t"
    "v_mfma_f32_16x16x32_bf16 v[108:111], a[140:143], v[168:171], v[108:111]\n\t"
    "ds_read_b64_tr_b16 a[134:135], v225 offset:12288\n\t"
    "v_mfma_f32_16x16x32_bf16 v[112:115], a[144:147], v[168:171], v[112:115]\n\t"
    "ds_read_b64_tr_b16 a[136:137], v226 offset:8704\n\t"
    "v_mfma_f32_16x16x32_bf16 v[116:119], a[148:151], v[168:171], v[116:119]\n\t"
    "ds_read_b64_tr_b16 a[138:139], v226 offset:12800\n\t"
    "v_mfma_f32_16x16x32_bf16 v[120:123], a[152:155], v[168:171], v[120:123]\n\t"
    "ds_read_b64_tr_b16 a[140:141], v225 offset:8704\n\t"
    "v_mfma_f32_16x16x32_bf16 v[124:127], a[156:159], v[168:171], v[124:127]\n\t"
    "ds_read_b64_tr_b16 a[142:143], v225 offset:12800\n\t"
    "v_mfma_f32_16x16x32_bf16 v[216:219], v[252:255], v[160:163], v[216:219]\n\t"
    "ds_read_b64_tr_b16 a[144:145], v226 offset:9216\n\t"
    "v_mfma_f32_16x16x32_bf16 v[220:223], v[252:255], v[168:171], v[220:223]\n\t"
    "s_waitcnt lgkmcnt(9)\n\t"
    "ds_read_b64_tr_b16 a[146:147], v226 offset:13312\n\t"
    "v_mfma_f32_16x16x32_bf16 v[128:131], a[96:99], a[32:35], v[192:195]\n\t"
    "ds_read_b64_tr_b16 a[148:149], v225 offset:9216\n\t"
    "v_mfma_f32_16x16x32_bf16 v[128:131], a[100:103], a[36:39], v[128:131]\n\t"
    "ds_read_b64_tr_b16 a[150:151], v225 offset:13312\n\t"
    "v_mfma_f32_16x16x32_bf16 v[128:131], a[104:107], a[40:43], v[128:131]\n\t"
    "ds_read_b64_tr_b16 a[152:153], v226 offset:9728\n\t"
    "v_mfma_f32_16x16x32_bf16 v[128:131], a[108:111], a[44:47], v[128:131]\n\t"
    "ds_read_b64_tr_b16 a[154:155], v226 offset:13824\n\t"
    "v_mfma_f32_16x16x32_bf16 v[132:135], a[112:115], a[32:35], v[192:195]\n\t"
    "ds_read_b64_tr_b16 a[156:157], v225 offset:9728\n\t"
    "v_mfma_f32_16x16x32_bf16 v[132:135], a[116:119], a[36:39], v[132:135]\n\t"
    "ds_read_b64_tr_b16 a[158:159], v225 offset:13824\n\t"
    "v_mfma_f32_16x16x32_bf16 v[132:135], a[120:123], a[40:43], v[132:135]\n\t"
    "v_exp_f32 v148, v148\n\t"
    "v_mfma_f32_16x16x32_bf16 v[132:135], a[124:127], a[44:47], v[132:135]\n\t"
    "v_exp_f32 v149, v149\n\t"
    "v_mfma_f32_16x16x32_bf16 v[136:139], a[96:99], a[48:51], v[196:199]\n\t"
    "v_exp_f32 v150, v150\n\t"
    "v_mfma_f32_16x16x32_bf16 v[136:139], a[100:103], a[52:55], v[136:139]\n\t"
    "v_exp_f32 v151, v151\n\t"
    "v_mfma_f32_16x16x32_bf16 v[136:139], a[104:107], a[56:59], v[136:139]\n\t"
    "v_cvt_pk_bf16_f32 v146, v148, v149\n\t"
    "v_cvt_pk_bf16_f32 v147, v150, v151\n\t"
    "v_mfma_f32_16x16x32_bf16 v[136:139], a[108:111], a[60:63], v[136:139]\n\t"
    "v_exp_f32 v152, v152\n\t"
    "v_mfma_f32_16x16x32_bf16 v[140:143], a[112:115], a[48:51], v[196:199]\n\t"
    "v_exp_f32 v153, v153\n\t"
    "v_mfma_f32_16x16x32_bf16 v[140:143], a[116:119], a[52:55], v[140:143]\n\t"
    "v_exp_f32 v154, v154\n\t"
    "v_mfma_f32_16x16x32_bf16 v[140:143], a[120:123], a[56:59], v[140:143]\n\t"
    "v_exp_f32 v155, v155\n\t"
    "v_mfma_f32_16x16x32_bf16 v[140:143], a[124:127], a[60:63], v[140:143]\n\t"
    "s_waitcnt lgkmcnt(0)\n\t"
    "s_waitcnt vmcnt(4)\n\t"
    "s_barrier\n\t"
    "v_exp_f32 v156, v156\n\t"
    "v_mfma_f32_16x16x32_bf16 v[160:163], a[96:99], a[64:67], v[200:203]\n\t"
    "s_add_u32 m0, %8, 0\n\t"
    "s_add_u32 s40, %9, 0\n\t"
    "v_cvt_pk_bf16_f32 v152, v152, v153\n\t"
    "v_mfma_f32_16x16x32_bf16 v[160:163], a[100:103], a[68:71], v[160:163]\n\t"
    "v_exp_f32 v157, v157\n\t"
    "v_mfma_f32_16x16x32_bf16 v[160:163], a[104:107], a[72:75], v[160:163]\n\t"
    "v_exp_f32 v158, v158\n\t"
    "v_mfma_f32_16x16x32_bf16 v[160:163], a[108:111], a[76:79], v[160:163]\n\t"
    "v_exp_f32 v159, v159\n\t"
    "v_mfma_f32_16x16x32_bf16 v[164:167], a[112:115], a[64:67], v[200:203]\n\t"
    "v_cvt_pk_bf16_f32 v153, v154, v155\n\t"
    "v_cvt_pk_bf16_f32 v154, v156, v157\n\t"
    "v_mfma_f32_16x16x32_bf16 v[164:167], a[116:119], a[68:71], v[164:167]\n\t"
    "v_cvt_pk_bf16_f32 v155, v158, v159\n\t"
    "v_mfma_f32_16x16x32_bf16 v[164:167], a[120:123], a[72:75], v[164:167]\n\t"
    "v_exp_f32 v176, v176\n\t"
    "v_mfma_f32_16x16x32_bf16 v[164:167], a[124:127], a[76:79], v[164:167]\n\t"
    "v_exp_f32 v177, v177\n\t"
    "v_mfma_f32_16x16x32_bf16 v[168:171], a[96:99], a[80:83], v[204:207]\n\t"
    "ds_read_b128 a[96:99], v244 offset:8192\n\t"
    "v_cvt_pk_bf16_f32 v176, v176, v177\n\t"
    "v_mfma_f32_16x16x32_bf16 v[168:171], a[100:103], a[84:87], v[168:171]\n\t"
    "ds_read_b128 a[100:103], v244 offset:9216\n\t"
    "v_mfma_f32_16x16x32_bf16 v[168:171], a[104:107], a[88:91], v[168:171]\n\t"
    "buffer_load_dwordx4 v227, %3, s40 offen offset:0  lds\n\t"
    "v_mfma_f32_16x16x32_bf16 v[168:171], a[108:111], a[92:95], v[168:171]\n\t"
    "ds_read_b128 a[104:107], v244 offset:10240\n\t"
    "v_mfma_f32_16x16x32_bf16 v[172:175], a[112:115], a[80:83], v[204:207]\n\t"
    "ds_read_b128 a[108:111], v244 offset:11264\n\t"
    "v_mfma_f32_16x16x32_bf16 v[172:175], a[116:119], a[84:87], v[172:175]\n\t"
    "ds_read_b128 a[112:115], v244 offset:12288\n\t"
    "v_mfma_f32_16x16x32_bf16 v[172:175], a[120:123], a[88:91], v[172:175]\n\t"
    "ds_read_b128 a[116:119], v244 offset:13312\n\t"
    "v_mfma_f32_16x16x32_bf16 v[172:175], a[124:127], a[92:95], v[172:175]\n\t"
    "ds_read_b128 a[120:123], v244 offset:14336\n\t"
    "v_mfma_f32_16x16x32_bf16 v[0:3], a[128:131], v[144:147], v[0:3]\n\t"
    "buffer_load_dwordx4 v248, %3, s40 offen offset:1024  lds\n\t"
    "s_add_u32 m0, %8, 8192\n\t"
    "s_add_u32 s40, %9, 8192\n\t"
    "v_mfma_f32_16x16x32_bf16 v[4:7], a[132:135], v[144:147], v[4:7]\n\t"
    "ds_read_b128 a[124:127], v244 offset:15360\n\t"
    "v_mfma_f32_16x16x32_bf16 v[8:11], a[136:139], v[144:147], v[8:11]\n\t"
    "v_exp_f32 v178, v178\n\t"
    "v_mfma_f32_16x16x32_bf16 v[12:15], a[140:143], v[144:147], v[12:15]\n\t"
    "v_exp_f32 v179, v179\n\t"
    "v_mfma_f32_16x16x32_bf16 v[16:19], a[144:147], v[144:147], v[16:19]\n\t"
    "v_exp_f32 v180, v180\n\t"
    "v_mfma_f32_16x16x32_bf16 v[20:23], a[148:151], v[144:147], v[20:23]\n\t"
    "v_exp_f32 v181, v181\n\t"
    "v_mfma_f32_16x16x32_bf16 v[24:27], a[152:155], v[144:147], v[24:27]\n\t"
    "buffer_load_dwordx4 v227, %3, s40 offen offset:0  lds\n\t"
    "v_mfma_f32_16x16x32_bf16 v[28:31], a[156:159], v[144:147], v[28:31]\n\t"
    "v_exp_f32 v182, v182\n\t"
    "v_mfma_f32_16x16x32_bf16 v[32:35], a[128:131], v[152:155], v[32:35]\n\t"
    "v_exp_f32 v183, v183\n\t"
    "v_mfma_f32_16x16x32_bf16 v[36:39], a[132:135], v[152:155], v[36:39]\n\t"
    "v_cvt_pk_bf16_f32 v177, v178, v179\n\t"
    "v_cvt_pk_bf16_f32 v178, v180, v181\n\t"
    "v_mfma_f32_16x16x32_bf16 v[40:43], a[136:139], v[152:155], v[40:43]\n\t"
    "v_cvt_pk_bf16_f32 v179, v182, v183\n\t"
    "v_mfma_f32_16x16x32_bf16 v[44:47], a[140:143], v[152:155], v[44:47]\n\t"
    "v_exp_f32 v184, v184\n\t"
    "v_mfma_f32_16x16x32_bf16 v[48:51], a[144:147], v[152:155], v[48:51]\n\t"
    "buffer_load_dwordx4 v248, %3, s40 offen offset:1024  lds\n\t"
    "v_mfma_f32_16x16x32_bf16 v[52:55], a[148:151], v[152:155], v[52:55]\n\t"
    "v_exp_f32 v185, v185\n\t"
    "v_mfma_f32_16x16x32_bf16 v[56:59], a[152:155], v[152:155], v[56:59]\n\t"
    "v_exp_f32 v186, v186\n\t"
    "v_mfma_f32_16x16x32_bf16 v[60:63], a[156:159], v[152:155], v[60:63]\n\t"
    "v_exp_f32 v187, v187\n\t"
    "v_mfma_f32_16x16x32_bf16 v[208:211], v[252:255], v[144:147], v[208:211]\n\t"
    "v_exp_f32 v188, v188\n\t"
    "v_mfma_f32_16x16x32_bf16 v[212:215], v[252:255], v[152:155], v[212:215]\n\t"
    "v_exp_f32 v189, v189\n\t"
    "v_mfma_f32_16x16x32_bf16 v[64:67], a[128:131], v[176:179], v[64:67]\n\t"
    "v_exp_f32 v190, v190\n\t"
    "v_mfma_f32_16x16x32_bf16 v[68:71], a[132:135], v[176:179], v[68:71]\n\t"
    "v_exp_f32 v191, v191\n\t"
    "v_mfma_f32_16x16x32_bf16 v[72:75], a[136:139], v[176:179], v[72:75]\n\t"
    "v_cvt_pk_bf16_f32 v184, v184, v185\n\t"
    "v_cvt_pk_bf16_f32 v185, v186, v187\n\t"
    "v_mfma_f32_16x16x32_bf16 v[76:79], a[140:143], v[176:179], v[76:79]\n\t"
    "v_cvt_pk_bf16_f32 v186, v188, v189\n\t"
    "v_cvt_pk_bf16_f32 v187, v190, v191\n\t"
    "v_mfma_f32_16x16x32_bf16 v[80:83], a[144:147], v[176:179], v[80:83]\n\t"
    "v_exp_f32 v128, v128\n\t"
    "v_mfma_f32_16x16x32_bf16 v[84:87], a[148:151], v[176:179], v[84:87]\n\t"
    "v_exp_f32 v129, v129\n\t"
    "v_mfma_f32_16x16x32_bf16 v[88:91], a[152:155], v[176:179], v[88:91]\n\t"
    "v_exp_f32 v130, v130\n\t"
    "v_mfma_f32_16x16x32_bf16 v[92:95], a[156:159], v[176:179], v[92:95]\n\t"
    "v_exp_f32 v131, v131\n\t"
    "v_mfma_f32_16x16x32_bf16 v[96:99], a[128:131], v[184:187], v[96:99]\n\t"
    "ds_read_b64_tr_b16 a[128:129], v226 offset:16384\n\t"
    "v_cvt_pk_bf16_f32 v128, v128, v129\n\t"
    "v_mfma_f32_16x16x32_bf16 v[100:103], a[132:135], v[184:187], v[100:103]\n\t"
    "ds_read_b64_tr_b16 a[130:131], v226 offset:20480\n\t"
    "v_cvt_pk_bf16_f32 v129, v130, v131\n\t"
    "v_mfma_f32_16x16x32_bf16 v[104:107], a[136:139], v[184:187], v[104:107]\n\t"
    "s_add_u32 s40, %11, 73856\n\t"
    "ds_read_b64_tr_b16 a[132:133], v225 offset:16384\n\t"
    "v_mfma_f32_16x16x32_bf16 v[108:111], a[140:143], v[184:187], v[108:111]\n\t"
    "ds_read_b64_tr_b16 a[134:135], v225 offset:20480\n\t"
    "v_mfma_f32_16x16x32_bf16 v[112:115], a[144:147], v[184:187], v[112:115]\n\t"
    "ds_read_b64_tr_b16 a[136:137], v226 offset:16896\n\t"
    "v_mfma_f32_16x16x32_bf16 v[116:119], a[148:151], v[184:187], v[116:119]\n\t"
    "ds_read_b64_tr_b16 a[138:139], v226 offset:20992\n\t"
    "v_mfma_f32_16x16x32_bf16 v[120:123], a[152:155], v[184:187], v[120:123]\n\t"
    "ds_read_b64_tr_b16 a[140:141], v225 offset:16896\n\t"
    "v_mfma_f32_16x16x32_bf16 v[124:127], a[156:159], v[184:187], v[124:127]\n\t"
    "ds_read_b64_tr_b16 a[142:143], v225 offset:20992\n\t"
    "v_mfma_f32_16x16x32_bf16 v[216:219], v[252:255], v[176:179], v[216:219]\n\t"
    "ds_read_b64_tr_b16 a[144:145], v226 offset:17408\n\t"
    "v_mfma_f32_16x16x32_bf16 v[220:223], v[252:255], v[184:187], v[220:223]\n\t"
    "s_waitcnt lgkmcnt(9)\n\t"
    "ds_read_b64_tr_b16 a[146:147], v226 offset:21504\n\t"
    "v_mfma_f32_16x16x32_bf16 v[144:147], a[96:99], a[32:35], v[192:195]\n\t"
    "ds_read_b64_tr_b16 a[148:149], v225 offset:17408\n\t"
    "v_mfma_f32_16x16x32_bf16 v[144:147], a[100:103], a[36:39], v[144:147]\n\t"
    "ds_read_b64_tr_b16 a[150:151], v225 offset:21504\n\t"
    "v_mfma_f32_16x16x32_bf16 v[144:147], a[104:107], a[40:43], v[144:147]\n\t"
    "ds_read_b64_tr_b16 a[152:153], v226 offset:17920\n\t"
    "v_mfma_f32_16x16x32_bf16 v[144:147], a[108:111], a[44:47], v[144:147]\n\t"
    "ds_read_b64_tr_b16 a[154:155], v226 offset:22016\n\t"
    "v_mfma_f32_16x16x32_bf16 v[148:151], a[112:115], a[32:35], v[192:195]\n\t"
    "ds_read_b64_tr_b16 a[156:157], v225 offset:17920\n\t"
    "v_mfma_f32_16x16x32_bf16 v[148:151], a[116:119], a[36:39], v[148:151]\n\t"
    "ds_read_b64_tr_b16 a[158:159], v225 offset:22016\n\t"
    "v_mfma_f32_16x16x32_bf16 v[148:151], a[120:123], a[40:43], v[148:151]\n\t"
    "v_exp_f32 v132, v132\n\t"
    "v_mfma_f32_16x16x32_bf16 v[148:151], a[124:127], a[44:47], v[148:151]\n\t"
    "v_exp_f32 v133, v133\n\t"
    "v_mfma_f32_16x16x32_bf16 v[152:155], a[96:99], a[48:51], v[196:199]\n\t"
    "v_exp_f32 v134, v134\n\t"
    "v_mfma_f32_16x16x32_bf16 v[152:155], a[100:103], a[52:55], v[152:155]\n\t"
    "v_exp_f32 v135, v135\n\t"
    "v_mfma_f32_16x16x32_bf16 v[152:155], a[104:107], a[56:59], v[152:155]\n\t"
    "v_cvt_pk_bf16_f32 v130, v132, v133\n\t"
    "v_cvt_pk_bf16_f32 v131, v134, v135\n\t"
    "v_mfma_f32_16x16x32_bf16 v[152:155], a[108:111], a[60:63], v[152:155]\n\t"
    "v_exp_f32 v136, v136\n\t"
    "v_mfma_f32_16x16x32_bf16 v[156:159], a[112:115], a[48:51], v[196:199]\n\t"
    "v_exp_f32 v137, v137\n\t"
    "v_mfma_f32_16x16x32_bf16 v[156:159], a[116:119], a[52:55], v[156:159]\n\t"
    "v_exp_f32 v138, v138\n\t"
    "v_mfma_f32_16x16x32_bf16 v[156:159], a[120:123], a[56:59], v[156:159]\n\t"
    "v_exp_f32 v139, v139\n\t"
    "v_mfma_f32_16x16x32_bf16 v[156:159], a[124:127], a[60:63], v[156:159]\n\t"
    "v_exp_f32 v140, v140\n\t"
    "v_mfma_f32_16x16x32_bf16 v[176:179], a[96:99], a[64:67], v[200:203]\n\t"
    "v_exp_f32 v141, v141\n\t"
    "v_mfma_f32_16x16x32_bf16 v[176:179], a[100:103], a[68:71], v[176:179]\n\t"
    "buffer_store_dwordx4 a[176:179], v236, %4, s40 offen offset:0 sc1\n\t"
    "v_mfma_f32_16x16x32_bf16 v[176:179], a[104:107], a[72:75], v[176:179]\n\t"
    "v_exp_f32 v142, v142\n\t"
    "v_mfma_f32_16x16x32_bf16 v[176:179], a[108:111], a[76:79], v[176:179]\n\t"
    "v_exp_f32 v143, v143\n\t"
    "v_mfma_f32_16x16x32_bf16 v[180:183], a[112:115], a[64:67], v[200:203]\n\t"
    "v_cvt_pk_bf16_f32 v136, v136, v137\n\t"
    "v_cvt_pk_bf16_f32 v137, v138, v139\n\t"
    "v_mfma_f32_16x16x32_bf16 v[180:183], a[116:119], a[68:71], v[180:183]\n\t"
    "v_cvt_pk_bf16_f32 v138, v140, v141\n\t"
    "v_cvt_pk_bf16_f32 v139, v142, v143\n\t"
    "v_mfma_f32_16x16x32_bf16 v[180:183], a[120:123], a[72:75], v[180:183]\n\t"
    "v_exp_f32 v160, v160\n\t"
    "v_mfma_f32_16x16x32_bf16 v[180:183], a[124:127], a[76:79], v[180:183]\n\t"
    "v_exp_f32 v161, v161\n\t"
    "v_mfma_f32_16x16x32_bf16 v[184:187], a[96:99], a[80:83], v[204:207]\n\t"
    "v_exp_f32 v162, v162\n\t"
    "v_mfma_f32_16x16x32_bf16 v[184:187], a[100:103], a[84:87], v[184:187]\n\t"
    "buffer_store_dwordx4 a[180:183], v236, %4, s40 offen offset:2048 sc1\n\t"
    "s_add_u32 s40, %11, 77952\n\t"
    "v_mfma_f32_16x16x32_bf16 v[184:187], a[104:107], a[88:91], v[184:187]\n\t"
    "v_exp_f32 v163, v163\n\t"
    "v_mfma_f32_16x16x32_bf16 v[184:187], a[108:111], a[92:95], v[184:187]\n\t"
    "v_exp_f32 v164, v164\n\t"
    "v_mfma_f32_16x16x32_bf16 v[188:191], a[112:115], a[80:83], v[204:207]\n\t"
    "v_exp_f32 v165, v165\n\t"
    "v_mfma_f32_16x16x32_bf16 v[188:191], a[116:119], a[84:87], v[188:191]\n\t"
    "v_exp_f32 v166, v166\n\t"
    "v_mfma_f32_16x16x32_bf16 v[188:191], a[120:123], a[88:91], v[188:191]\n\t"
    "v_exp_f32 v167, v167\n\t"
    "v_mfma_f32_16x16x32_bf16 v[188:191], a[124:127], a[92:95], v[188:191]\n\t"
    "s_waitcnt lgkmcnt(0)\n\t"
    "v_cvt_pk_bf16_f32 v160, v160, v161\n\t"
    "v_cvt_pk_bf16_f32 v161, v162, v163\n\t"
    "v_mfma_f32_16x16x32_bf16 v[0:3], a[128:131], v[128:131], v[0:3]\n\t"
    "v_cvt_pk_bf16_f32 v162, v164, v165\n\t"
    "v_cvt_pk_bf16_f32 v163, v166, v167\n\t"
    "v_mfma_f32_16x16x32_bf16 v[4:7], a[132:135], v[128:131], v[4:7]\n\t"
    "buffer_store_dwordx4 a[184:187], v236, %4, s40 offen offset:0 sc1\n\t"
    "v_mfma_f32_16x16x32_bf16 v[8:11], a[136:139], v[128:131], v[8:11]\n\t"
    "v_exp_f32 v168, v168\n\t"
    "v_mfma_f32_16x16x32_bf16 v[12:15], a[140:143], v[128:131], v[12:15]\n\t"
    "v_exp_f32 v169, v169\n\t"
    "v_mfma_f32_16x16x32_bf16 v[16:19], a[144:147], v[128:131], v[16:19]\n\t"
    "v_exp_f32 v170, v170\n\t"
    "v_mfma_f32_16x16x32_bf16 v[20:23], a[148:151], v[128:131], v[20:23]\n\t"
    "v_exp_f32 v171, v171\n\t"
    "v_mfma_f32_16x16x32_bf16 v[24:27], a[152:155], v[128:131], v[24:27]\n\t"
    "v_exp_f32 v172, v172\n\t"
    "v_mfma_f32_16x16x32_bf16 v[28:31], a[156:159], v[128:131], v[28:31]\n\t"
    "v_exp_f32 v173, v173\n\t"
    "v_mfma_f32_16x16x32_bf16 v[32:35], a[128:131], v[136:139], v[32:35]\n\t"
    "v_exp_f32 v174, v174\n\t"
    "v_mfma_f32_16x16x32_bf16 v[36:39], a[132:135], v[136:139], v[36:39]\n\t"
    "buffer_store_dwordx4 a[188:191], v236, %4, s40 offen offset:2048 sc1\n\t"
    "v_mfma_f32_16x16x32_bf16 v[40:43], a[136:139], v[136:139], v[40:43]\n\t"
    "v_exp_f32 v175, v175\n\t"
    "v_mfma_f32_16x16x32_bf16 v[44:47], a[140:143], v[136:139], v[44:47]\n\t"
    "v_cvt_pk_bf16_f32 v168, v168, v169\n\t"
    "v_cvt_pk_bf16_f32 v169, v170, v171\n\t"
    "v_mfma_f32_16x16x32_bf16 v[48:51], a[144:147], v[136:139], v[48:51]\n\t"
    "v_cvt_pk_bf16_f32 v170, v172, v173\n\t"
    "v_cvt_pk_bf16_f32 v171, v174, v175\n\t"
    "v_mfma_f32_16x16x32_bf16 v[52:55], a[148:151], v[136:139], v[52:55]\n\t"
    "v_exp_f32 v144, v144\n\t"
    "v_mfma_f32_16x16x32_bf16 v[56:59], a[152:155], v[136:139], v[56:59]\n\t"
    "v_exp_f32 v145, v145\n\t"
    "v_mfma_f32_16x16x32_bf16 v[60:63], a[156:159], v[136:139], v[60:63]\n\t"
    "v_exp_f32 v146, v146\n\t"
    "v_mfma_f32_16x16x32_bf16 v[208:211], v[252:255], v[128:131], v[208:211]\n\t"
    "v_exp_f32 v147, v147\n\t"
    "v_mfma_f32_16x16x32_bf16 v[212:215], v[252:255], v[136:139], v[212:215]\n\t"
    "v_exp_f32 v148, v148\n\t"
    "v_mfma_f32_16x16x32_bf16 v[64:67], a[128:131], v[160:163], v[64:67]\n\t"
    "v_exp_f32 v149, v149\n\t"
    "v_mfma_f32_16x16x32_bf16 v[68:71], a[132:135], v[160:163], v[68:71]\n\t"
    "v_exp_f32 v150, v150\n\t"
    "v_mfma_f32_16x16x32_bf16 v[72:75], a[136:139], v[160:163], v[72:75]\n\t"
    "v_exp_f32 v151, v151\n\t"
    "v_mfma_f32_16x16x32_bf16 v[76:79], a[140:143], v[160:163], v[76:79]\n\t"
    "v_cvt_pk_bf16_f32 v144, v144, v145\n\t"
    "v_cvt_pk_bf16_f32 v145, v146, v147\n\t"
    "v_mfma_f32_16x16x32_bf16 v[80:83], a[144:147], v[160:163], v[80:83]\n\t"
    "v_exp_f32 v152, v152\n\t"
    "v_mfma_f32_16x16x32_bf16 v[84:87], a[148:151], v[160:163], v[84:87]\n\t"
    "v_exp_f32 v153, v153\n\t"
    "v_mfma_f32_16x16x32_bf16 v[88:91], a[152:155], v[160:163], v[88:91]\n\t"
    "v_cvt_pk_bf16_f32 v146, v148, v149\n\t"
    "v_cvt_pk_bf16_f32 v147, v150, v151\n\t"
    "v_mfma_f32_16x16x32_bf16 v[92:95], a[156:159], v[160:163], v[92:95]\n\t"
    "v_exp_f32 v154, v154\n\t"
    "v_mfma_f32_16x16x32_bf16 v[96:99], a[128:131], v[168:171], v[96:99]\n\t"
    "ds_read_b64_tr_b16 a[128:129], v226 offset:24576\n\t"
    "v_cvt_pk_bf16_f32 v152, v152, v153\n\t"
    "v_mfma_f32_16x16x32_bf16 v[100:103], a[132:135], v[168:171], v[100:103]\n\t"
    "ds_read_b64_tr_b16 a[130:131], v226 offset:28672\n\t"
    "ds_read_b64_tr_b16 a[132:133], v225 offset:24576\n\t"
    "ds_read_b64_tr_b16 a[134:135], v225 offset:28672\n\t"
    "v_mfma_f32_16x16x32_bf16 v[104:107], a[136:139], v[168:171], v[104:107]\n\t"
    "ds_read_b64_tr_b16 a[136:137], v226 offset:25088\n\t"
    "ds_read_b64_tr_b16 a[138:139], v226 offset:29184\n\t"
    "v_mfma_f32_16x16x32_bf16 v[108:111], a[140:143], v[168:171], v[108:111]\n\t"
    "ds_read_b64_tr_b16 a[140:141], v225 offset:25088\n\t"
    "ds_read_b64_tr_b16 a[142:143], v225 offset:29184\n\t"
    "v_mfma_f32_16x16x32_bf16 v[112:115], a[144:147], v[168:171], v[112:115]\n\t"
    "ds_read_b64_tr_b16 a[144:145], v226 offset:25600\n\t"
    "ds_read_b64_tr_b16 a[146:147], v226 offset:29696\n\t"
    "v_mfma_f32_16x16x32_bf16 v[116:119], a[148:151], v[168:171], v[116:119]\n\t"
    "ds_read_b64_tr_b16 a[148:149], v225 offset:25600\n\t"
    "ds_read_b64_tr_b16 a[150:151], v225 offset:29696\n\t"
    "v_mfma_f32_16x16x32_bf16 v[120:123], a[152:155], v[168:171], v[120:123]\n\t"
    "ds_read_b64_tr_b16 a[152:153], v226 offset:26112\n\t"
    "ds_read_b64_tr_b16 a[154:155], v226 offset:30208\n\t"
    "v_mfma_f32_16x16x32_bf16 v[124:127], a[156:159], v[168:171], v[124:127]\n\t"
    "ds_read_b64_tr_b16 a[156:157], v225 offset:26112\n\t"
    "ds_read_b64_tr_b16 a[158:159], v225 offset:30208\n\t"
    "v_mfma_f32_16x16x32_bf16 v[216:219], v[252:255], v[160:163], v[216:219]\n\t"
    "v_exp_f32 v155, v155\n\t"
    "v_mfma_f32_16x16x32_bf16 v[220:223], v[252:255], v[168:171], v[220:223]\n\t"
    "v_exp_f32 v156, v156\n\t"
    "v_mfma_f32_16x16x32_bf16 v[160:163], a[192:195], a[64:67], v[200:203]\n\t"
    "v_exp_f32 v157, v157\n\t"
    "v_mfma_f32_16x16x32_bf16 v[160:163], a[196:199], a[68:71], v[160:163]\n\t"
    "v_exp_f32 v158, v158\n\t"
    "v_mfma_f32_16x16x32_bf16 v[160:163], a[200:203], a[72:75], v[160:163]\n\t"
    "ds_read_b128 a[96:99], v224 offset:0\n\t"
    "v_cvt_pk_bf16_f32 v153, v154, v155\n\t"
    "v_mfma_f32_16x16x32_bf16 v[160:163], a[204:207], a[76:79], v[160:163]\n\t"
    "ds_read_b128 a[100:103], v224 offset:1024\n\t"
    "v_cvt_pk_bf16_f32 v154, v156, v157\n\t"
    "v_mfma_f32_16x16x32_bf16 v[164:167], a[208:211], a[64:67], v[200:203]\n\t"
    "ds_read_b128 a[104:107], v224 offset:2048\n\t"
    "v_mfma_f32_16x16x32_bf16 v[164:167], a[212:215], a[68:71], v[164:167]\n\t"
    "ds_read_b128 a[108:111], v224 offset:3072\n\t"
    "v_mfma_f32_16x16x32_bf16 v[164:167], a[216:219], a[72:75], v[164:167]\n\t"
    "ds_read_b128 a[112:115], v224 offset:4096\n\t"
    "v_mfma_f32_16x16x32_bf16 v[164:167], a[220:223], a[76:79], v[164:167]\n\t"
    "s_waitcnt lgkmcnt(5)\n\t"
    "s_waitcnt vmcnt(4)\n\t"
    "s_barrier\n\t"
    "s_add_u32 m0, %8, 16384\n\t"
    "s_add_u32 s40, %9, 16384\n\t"
    "v_mfma_f32_16x16x32_bf16 v[168:171], a[192:195], a[80:83], v[204:207]\n\t"
    "ds_read_b128 a[116:119], v224 offset:5120\n\t"
    "v_mfma_f32_16x16x32_bf16 v[168:171], a[196:199], a[84:87], v[168:171]\n\t"
    "ds_read_b128 a[120:123], v224 offset:6144\n\t"
    "v_mfma_f32_16x16x32_bf16 v[168:171], a[200:203], a[88:91], v[168:171]\n\t"
    "buffer_load_dwordx4 v227, %3, s40 offen offset:0  lds\n\t"
    "v_mfma_f32_16x16x32_bf16 v[168:171], a[204:207], a[92:95], v[168:171]\n\t"
    "ds_read_b128 a[124:127], v224 offset:7168\n\t"
    "v_mfma_f32_16x16x32_bf16 v[172:175], a[208:211], a[80:83], v[204:207]\n\t"
    "v_exp_f32 v159, v159\n\t"
    "v_mfma_f32_16x16x32_bf16 v[172:175], a[212:215], a[84:87], v[172:175]\n\t"
    "v_cvt_pk_bf16_f32 v155, v158, v159\n\t"
    "v_mfma_f32_16x16x32_bf16 v[172:175], a[216:219], a[88:91], v[172:175]\n\t"
    "v_exp_f32 v176, v176\n\t"
    "v_mfma_f32_16x16x32_bf16 v[172:175], a[220:223], a[92:95], v[172:175]\n\t"
    "v_exp_f32 v177, v177\n\t"
    "v_mfma_f32_16x16x32_bf16 v[208:211], v[252:255], v[144:147], v[208:211]\n\t"
    "buffer_load_dwordx4 v248, %3, s40 offen offset:1024  lds\n\t"
    "s_add_u32 m0, %8, 24576\n\t"
    "s_add_u32 s40, %9, 24576\n\t"
    "v_mfma_f32_16x16x32_bf16 v[212:215], v[252:255], v[152:155], v[212:215]\n\t"
    "v_exp_f32 v178, v178\n\t"
    "v_mfma_f32_16x16x32_bf16 v[0:3], a[128:131], v[144:147], v[0:3]\n\t"
    "v_exp_f32 v179, v179\n\t"
    "v_mfma_f32_16x16x32_bf16 v[4:7], a[132:135], v[144:147], v[4:7]\n\t"
    "buffer_load_dwordx4 v227, %3, s40 offen offset:0  lds\n\t"
    "buffer_load_dwordx4 v248, %3, s40 offen offset:1024  lds\n\t"
    "v_mfma_f32_16x16x32_bf16 v[8:11], a[136:139], v[144:147], v[8:11]\n\t"
    "v_exp_f32 v180, v180\n\t"
    "v_mfma_f32_16x16x32_bf16 v[12:15], a[140:143], v[144:147], v[12:15]\n\t"
    "v_exp_f32 v181, v181\n\t"
    "v_mfma_f32_16x16x32_bf16 v[16:19], a[144:147], v[144:147], v[16:19]\n\t"
    "v_exp_f32 v182, v182\n\t"
    "v_mfma_f32_16x16x32_bf16 v[20:23], a[148:151], v[144:147], v[20:23]\n\t"
    "v_exp_f32 v183, v183\n\t"
    "v_mfma_f32_16x16x32_bf16 v[24:27], a[152:155], v[144:147], v[24:27]\n\t"
    "v_cvt_pk_bf16_f32 v176, v176, v177\n\t"
    "v_cvt_pk_bf16_f32 v177, v178, v179\n\t"
    "v_mfma_f32_16x16x32_bf16 v[28:31], a[156:159], v[144:147], v[28:31]\n\t"
    "v_cvt_pk_bf16_f32 v178, v180, v181\n\t"
    "v_cvt_pk_bf16_f32 v179, v182, v183\n\t"
    "v_mfma_f32_16x16x32_bf16 v[32:35], a[128:131], v[152:155], v[32:35]\n\t"
    "v_mov_b32 v229, v208\n\t"
    "v_mov_b32 v233, v212\n\t"
    "v_mfma_f32_16x16x32_bf16 v[36:39], a[132:135], v[152:155], v[36:39]\n\t"
    "v_rcp_f32 v242, v229\n\t"
    "v_mfma_f32_16x16x32_bf16 v[40:43], a[136:139], v[152:155], v[40:43]\n\t"
    "v_mul_f32 v0, v242, v0\n\t"
    "v_mul_f32 v1, v242, v1\n\t"
    "v_mfma_f32_16x16x32_bf16 v[44:47], a[140:143], v[152:155], v[44:47]\n\t"
    "v_mul_f32 v2, v242, v2\n\t"
    "v_mul_f32 v3, v242, v3\n\t"
    "v_mfma_f32_16x16x32_bf16 v[48:51], a[144:147], v[152:155], v[48:51]\n\t"
    "v_cvt_pk_bf16_f32 v0, v0, v1\n\t"
    "v_cvt_pk_bf16_f32 v1, v2, v3\n\t"
    "v_mfma_f32_16x16x32_bf16 v[52:55], a[148:151], v[152:155], v[52:55]\n\t"
    "v_exp_f32 v184, v184\n\t"
    "v_mfma_f32_16x16x32_bf16 v[56:59], a[152:155], v[152:155], v[56:59]\n\t"
    "v_exp_f32 v185, v185\n\t"
    "v_mfma_f32_16x16x32_bf16 v[60:63], a[156:159], v[152:155], v[60:63]\n\t"
    "v_exp_f32 v186, v186\n\t"
    "v_mfma_f32_16x16x32_bf16 v[64:67], a[128:131], v[176:179], v[64:67]\n\t"
    "v_exp_f32 v187, v187\n\t"
    "v_mfma_f32_16x16x32_bf16 v[68:71], a[132:135], v[176:179], v[68:71]\n\t"
    "v_exp_f32 v188, v188\n\t"
    "v_mfma_f32_16x16x32_bf16 v[72:75], a[136:139], v[176:179], v[72:75]\n\t"
    "v_exp_f32 v189, v189\n\t"
    "v_mfma_f32_16x16x32_bf16 v[76:79], a[140:143], v[176:179], v[76:79]\n\t"
    "v_exp_f32 v190, v190\n\t"
    "ds_write_b64 v234, v[0:1] offset:0\n\t"
    "v_mfma_f32_16x16x32_bf16 v[80:83], a[144:147], v[176:179], v[80:83]\n\t"
    "v_exp_f32 v191, v191\n\t"
    "v_mfma_f32_16x16x32_bf16 v[84:87], a[148:151], v[176:179], v[84:87]\n\t"
    "v_cvt_pk_bf16_f32 v184, v184, v185\n\t"
    "v_cvt_pk_bf16_f32 v185, v186, v187\n\t"
    "v_mfma_f32_16x16x32_bf16 v[88:91], a[152:155], v[176:179], v[88:91]\n\t"
    "v_cvt_pk_bf16_f32 v186, v188, v189\n\t"
    "v_cvt_pk_bf16_f32 v187, v190, v191\n\t"
    "v_mfma_f32_16x16x32_bf16 v[92:95], a[156:159], v[176:179], v[92:95]\n\t"
    "v_rcp_f32 v243, v233\n\t"
    "v_mfma_f32_16x16x32_bf16 v[96:99], a[128:131], v[184:187], v[96:99]\n\t"
    "ds_read_b64_tr_b16 a[128:129], v226 offset:0\n\t"
    "v_mul_f32 v32, v243, v32\n\t"
    "v_mfma_f32_16x16x32_bf16 v[100:103], a[132:135], v[184:187], v[100:103]\n\t"
    "ds_read_b64_tr_b16 a[130:131], v226 offset:4096\n\t"
    "v_mul_f32 v33, v243, v33\n\t"
    "v_mfma_f32_16x16x32_bf16 v[104:107], a[136:139], v[184:187], v[104:107]\n\t"
    "ds_read_b64_tr_b16 a[132:133], v225 offset:0\n\t"
    "v_mul_f32 v34, v243, v34\n\t"
    "v_mfma_f32_16x16x32_bf16 v[108:111], a[140:143], v[184:187], v[108:111]\n\t"
    "ds_read_b64_tr_b16 a[134:135], v225 offset:4096\n\t"
    "v_mul_f32 v35, v243, v35\n\t"
    "v_mfma_f32_16x16x32_bf16 v[112:115], a[144:147], v[184:187], v[112:115]\n\t"
    "ds_read_b64_tr_b16 a[136:137], v226 offset:512\n\t"
    "v_cvt_pk_bf16_f32 v32, v32, v33\n\t"
    "v_mfma_f32_16x16x32_bf16 v[116:119], a[148:151], v[184:187], v[116:119]\n\t"
    "ds_read_b64_tr_b16 a[138:139], v226 offset:4608\n\t"
    "v_cvt_pk_bf16_f32 v33, v34, v35\n\t"
    "ds_write_b64 v234, v[32:33] offset:2048\n\t"
    "v_mfma_f32_16x16x32_bf16 v[120:123], a[152:155], v[184:187], v[120:123]\n\t"
    "ds_read_b64_tr_b16 a[140:141], v225 offset:512\n\t"
    "v_mul_f32 v4, v242, v4\n\t"
    "v_mfma_f32_16x16x32_bf16 v[124:127], a[156:159], v[184:187], v[124:127]\n\t"
    "ds_read_b64_tr_b16 a[142:143], v225 offset:4608\n\t"
    "v_mul_f32 v5, v242, v5\n\t"
    "v_mfma_f32_16x16x32_bf16 v[216:219], v[252:255], v[176:179], v[216:219]\n\t"
    "ds_read_b64_tr_b16 a[144:145], v226 offset:1024\n\t"
    "v_mul_f32 v6, v242, v6\n\t"
    "v_mfma_f32_16x16x32_bf16 v[220:223], v[252:255], v[184:187], v[220:223]\n\t"
    "ds_read_b64_tr_b16 a[146:147], v226 offset:5120\n\t"
    "v_mul_f32 v7, v242, v7\n\t"
    "v_mfma_f32_16x16x32_bf16 v[176:179], a[224:227], a[64:67], v[200:203]\n\t"
    "ds_read_b64_tr_b16 a[148:149], v225 offset:1024\n\t"
    "v_cvt_pk_bf16_f32 v4, v4, v5\n\t"
    "v_mfma_f32_16x16x32_bf16 v[176:179], a[228:231], a[68:71], v[176:179]\n\t"
    "ds_read_b64_tr_b16 a[150:151], v225 offset:5120\n\t"
    "v_cvt_pk_bf16_f32 v5, v6, v7\n\t"
    "ds_write_b64 v237, v[4:5] offset:0\n\t"
    "v_mfma_f32_16x16x32_bf16 v[176:179], a[232:235], a[72:75], v[176:179]\n\t"
    "ds_read_b64_tr_b16 a[152:153], v226 offset:1536\n\t"
    "v_mul_f32 v36, v243, v36\n\t"
    "v_mfma_f32_16x16x32_bf16 v[176:179], a[236:239], a[76:79], v[176:179]\n\t"
    "ds_read_b64_tr_b16 a[154:155], v226 offset:5632\n\t"
    "v_mul_f32 v37, v243, v37\n\t"
    "v_mfma_f32_16x16x32_bf16 v[180:183], a[240:243], a[64:67], v[200:203]\n\t"
    "ds_read_b64_tr_b16 a[156:157], v225 offset:1536\n\t"
    "v_mul_f32 v38, v243, v38\n\t"
    "v_mfma_f32_16x16x32_bf16 v[180:183], a[244:247], a[68:71], v[180:183]\n\t"
    "ds_read_b64_tr_b16 a[158:159], v225 offset:5632\n\t"
    "v_mul_f32 v39, v243, v39\n\t"
    "v_mfma_f32_16x16x32_bf16 v[180:183], a[248:251], a[72:75], v[180:183]\n\t"
    "v_cvt_pk_bf16_f32 v36, v36, v37\n\t"
    "v_cvt_pk_bf16_f32 v37, v38, v39\n\t"
    "ds_write_b64 v237, v[36:37] offset:2048\n\t"
    "v_mfma_f32_16x16x32_bf16 v[180:183], a[252:255], a[76:79], v[180:183]\n\t"
    "v_mul_f32 v8, v242, v8\n\t"
    "v_mul_f32 v9, v242, v9\n\t"
    "v_mfma_f32_16x16x32_bf16 v[184:187], a[224:227], a[80:83], v[204:207]\n\t"
    "v_mul_f32 v10, v242, v10\n\t"
    "v_mul_f32 v11, v242, v11\n\t"
    "v_mfma_f32_16x16x32_bf16 v[184:187], a[228:231], a[84:87], v[184:187]\n\t"
    "v_cvt_pk_bf16_f32 v8, v8, v9\n\t"
    "v_cvt_pk_bf16_f32 v9, v10, v11\n\t"
    "ds_write_b64 v246, v[8:9] offset:0\n\t"
    "v_mfma_f32_16x16x32_bf16 v[184:187], a[232:235], a[88:91], v[184:187]\n\t"
    "v_mul_f32 v40, v243, v40\n\t"
    "v_mul_f32 v41, v243, v41\n\t"
    "v_mfma_f32_16x16x32_bf16 v[184:187], a[236:239], a[92:95], v[184:187]\n\t"
    "v_mul_f32 v42, v243, v42\n\t"
    "v_mul_f32 v43, v243, v43\n\t"
    "v_mfma_f32_16x16x32_bf16 v[188:191], a[240:243], a[80:83], v[204:207]\n\t"
    "v_cvt_pk_bf16_f32 v40, v40, v41\n\t"
    "v_cvt_pk_bf16_f32 v41, v42, v43\n\t"
    "ds_write_b64 v246, v[40:41] offset:2048\n\t"
    "v_mfma_f32_16x16x32_bf16 v[188:191], a[244:247], a[84:87], v[188:191]\n\t"
    "v_mul_f32 v12, v242, v12\n\t"
    "v_mul_f32 v13, v242, v13\n\t"
    "v_mfma_f32_16x16x32_bf16 v[188:191], a[248:251], a[88:91], v[188:191]\n\t"
    "v_mul_f32 v14, v242, v14\n\t"
    "v_mul_f32 v15, v242, v15\n\t"
    "v_mfma_f32_16x16x32_bf16 v[188:191], a[252:255], a[92:95], v[188:191]\n\t"
    "s_waitcnt lgkmcnt(4)\n\t"
    "v_cvt_pk_bf16_f32 v12, v12, v13\n\t"
    "v_cvt_pk_bf16_f32 v13, v14, v15\n\t"
    "ds_write_b64 v247, v[12:13] offset:0\n\t"
    "v_mul_f32 v44, v243, v44\n\t"
    "v_mul_f32 v45, v243, v45\n\t"
    "v_mul_f32 v46, v243, v46\n\t"
    "v_mul_f32 v47, v243, v47\n\t"
    "v_cvt_pk_bf16_f32 v44, v44, v45\n\t"
    "v_cvt_pk_bf16_f32 v45, v46, v47\n\t"
    "ds_write_b64 v247, v[44:45] offset:2048\n\t"
    "ds_read_b128 a[160:163], v235 offset:0\n\t"
    "v_exp_f32 v160, v160\n\t"
    "v_exp_f32 v161, v161\n\t"
    "v_exp_f32 v162, v162\n\t"
    "v_exp_f32 v163, v163\n\t"
    "v_exp_f32 v164, v164\n\t"
    "v_exp_f32 v165, v165\n\t"
    "v_exp_f32 v166, v166\n\t"
    "v_exp_f32 v167, v167\n\t"
    "v_cvt_pk_bf16_f32 v160, v160, v161\n\t"
    "v_cvt_pk_bf16_f32 v161, v162, v163\n\t"
    "v_cvt_pk_bf16_f32 v162, v164, v165\n\t"
    "v_cvt_pk_bf16_f32 v163, v166, v167\n\t"
    "v_exp_f32 v168, v168\n\t"
    "s_nop 0\n\t"
    "v_mfma_f32_16x16x32_bf16 v[64:67], a[128:131], v[160:163], v[64:67]\n\t"
    "v_exp_f32 v169, v169\n\t"
    "v_mfma_f32_16x16x32_bf16 v[68:71], a[132:135], v[160:163], v[68:71]\n\t"
    "v_exp_f32 v170, v170\n\t"
    "v_mfma_f32_16x16x32_bf16 v[72:75], a[136:139], v[160:163], v[72:75]\n\t"
    "v_exp_f32 v171, v171\n\t"
    "v_mfma_f32_16x16x32_bf16 v[76:79], a[140:143], v[160:163], v[76:79]\n\t"
    "ds_read2_b64 a[164:167], v235 offset0:129 offset1:128\n\t"
    "v_cvt_pk_bf16_f32 v168, v168, v169\n\t"
    "v_mfma_f32_16x16x32_bf16 v[80:83], a[144:147], v[160:163], v[80:83]\n\t"
    "v_exp_f32 v172, v172\n\t"
    "v_mfma_f32_16x16x32_bf16 v[84:87], a[148:151], v[160:163], v[84:87]\n\t"
    "ds_read_b128 a[168:171], v235 offset:2048\n\t"
    "v_cvt_pk_bf16_f32 v169, v170, v171\n\t"
    "v_mfma_f32_16x16x32_bf16 v[88:91], a[152:155], v[160:163], v[88:91]\n\t"
    "s_waitcnt lgkmcnt(4)\n\t"
    "v_exp_f32 v173, v173\n\t"
    "v_mfma_f32_16x16x32_bf16 v[92:95], a[156:159], v[160:163], v[92:95]\n\t"
    "ds_read2_b64 a[172:175], v245 offset0:129 offset1:128\n\t"
    "v_exp_f32 v174, v174\n\t"
    "v_exp_f32 v175, v175\n\t"
    "v_cvt_pk_bf16_f32 v170, v172, v173\n\t"
    "v_cvt_pk_bf16_f32 v171, v174, v175\n\t"
    "v_mul_f32 v16, v242, v16\n\t"
    "v_mul_f32 v17, v242, v17\n\t"
    "v_mfma_f32_16x16x32_bf16 v[96:99], a[128:131], v[168:171], v[96:99]\n\t"
    "ds_read_b64_tr_b16 a[128:129], v226 offset:8192\n\t"
    "v_mul_f32 v18, v242, v18\n\t"
    "v_mfma_f32_16x16x32_bf16 v[100:103], a[132:135], v[168:171], v[100:103]\n\t"
    "ds_read_b64_tr_b16 a[130:131], v226 offset:12288\n\t"
    "ds_read_b64_tr_b16 a[132:133], v225 offset:8192\n\t"
    "ds_read_b64_tr_b16 a[134:135], v225 offset:12288\n\t"
    "v_mfma_f32_16x16x32_bf16 v[104:107], a[136:139], v[168:171], v[104:107]\n\t"
    "ds_read_b64_tr_b16 a[136:137], v226 offset:8704\n\t"
    "ds_read_b64_tr_b16 a[138:139], v226 offset:12800\n\t"
    "v_mfma_f32_16x16x32_bf16 v[108:111], a[140:143], v[168:171], v[108:111]\n\t"
    "ds_read_b64_tr_b16 a[140:141], v225 offset:8704\n\t"
    "ds_read_b64_tr_b16 a[142:143], v225 offset:12800\n\t"
    "v_mfma_f32_16x16x32_bf16 v[112:115], a[144:147], v[168:171], v[112:115]\n\t"
    "ds_read_b64_tr_b16 a[144:145], v226 offset:9216\n\t"
    "ds_read_b64_tr_b16 a[146:147], v226 offset:13312\n\t"
    "v_mfma_f32_16x16x32_bf16 v[116:119], a[148:151], v[168:171], v[116:119]\n\t"
    "ds_read_b64_tr_b16 a[148:149], v225 offset:9216\n\t"
    "ds_read_b64_tr_b16 a[150:151], v225 offset:13312\n\t"
    "v_mfma_f32_16x16x32_bf16 v[120:123], a[152:155], v[168:171], v[120:123]\n\t"
    "ds_read_b64_tr_b16 a[152:153], v226 offset:9728\n\t"
    "ds_read_b64_tr_b16 a[154:155], v226 offset:13824\n\t"
    "v_mfma_f32_16x16x32_bf16 v[124:127], a[156:159], v[168:171], v[124:127]\n\t"
    "ds_read_b64_tr_b16 a[156:157], v225 offset:9728\n\t"
    "ds_read_b64_tr_b16 a[158:159], v225 offset:13824\n\t"
    "v_mfma_f32_16x16x32_bf16 v[216:219], v[252:255], v[160:163], v[216:219]\n\t"
    "v_mul_f32 v19, v242, v19\n\t"
    "v_cvt_pk_bf16_f32 v16, v16, v17\n\t"
    "v_mfma_f32_16x16x32_bf16 v[220:223], v[252:255], v[168:171], v[220:223]\n\t"
    "v_cvt_pk_bf16_f32 v17, v18, v19\n\t"
    "ds_write_b64 v234, v[16:17] offset:0\n\t"
    "v_mfma_f32_16x16x32_bf16 v[160:163], a[96:99], a[64:67], v[200:203]\n\t"
    "v_mul_f32 v48, v243, v48\n\t"
    "v_mul_f32 v49, v243, v49\n\t"
    "v_mfma_f32_16x16x32_bf16 v[160:163], a[100:103], a[68:71], v[160:163]\n\t"
    "v_mul_f32 v50, v243, v50\n\t"
    "v_mul_f32 v51, v243, v51\n\t"
    "v_mfma_f32_16x16x32_bf16 v[160:163], a[104:107], a[72:75], v[160:163]\n\t"
    "v_cvt_pk_bf16_f32 v48, v48, v49\n\t"
    "v_cvt_pk_bf16_f32 v49, v50, v51\n\t"
    "ds_write_b64 v234, v[48:49] offset:2048\n\t"
    "v_mfma_f32_16x16x32_bf16 v[160:163], a[108:111], a[76:79], v[160:163]\n\t"
    "v_mul_f32 v20, v242, v20\n\t"
    "v_mul_f32 v21, v242, v21\n\t"
    "v_mfma_f32_16x16x32_bf16 v[164:167], a[112:115], a[64:67], v[200:203]\n\t"
    "s_waitcnt lgkmcnt(2)\n\t"
    "v_mul_f32 v22, v242, v22\n\t"
    "v_mul_f32 v23, v242, v23\n\t"
    "s_waitcnt vmcnt(0)\n\t"
    "s_barrier\n\t"
    "s_add_u32 m0, %8, 0\n\t"
    "s_add_u32 s40, %9, 32768\n\t"
    "v_mfma_f32_16x16x32_bf16 v[164:167], a[116:119], a[68:71], v[164:167]\n\t"
    "v_cvt_pk_bf16_f32 v20, v20, v21\n\t"
    "v_cvt_pk_bf16_f32 v21, v22, v23\n\t"
    "ds_write_b64 v237, v[20:21] offset:0\n\t"
    "v_mfma_f32_16x16x32_bf16 v[164:167], a[120:123], a[72:75], v[164:167]\n\t"
    "v_mul_f32 v52, v243, v52\n\t"
    "v_mul_f32 v53, v243, v53\n\t"
    "v_mfma_f32_16x16x32_bf16 v[164:167], a[124:127], a[76:79], v[164:167]\n\t"
    "v_mul_f32 v54, v243, v54\n\t"
    "v_mul_f32 v55, v243, v55\n\t"
    "v_mfma_f32_16x16x32_bf16 v[168:171], a[96:99], a[80:83], v[204:207]\n\t"
    "ds_read_b128 a[96:99], v224 offset:8192\n\t"
    "v_cvt_pk_bf16_f32 v52, v52, v53\n\t"
    "v_mfma_f32_16x16x32_bf16 v[168:171], a[100:103], a[84:87], v[168:171]\n\t"
    "ds_read_b128 a[100:103], v224 offset:9216\n\t"
    "v_cvt_pk_bf16_f32 v53, v54, v55\n\t"
    "ds_write_b64 v237, v[52:53] offset:2048\n\t"
    "v_mfma_f32_16x16x32_bf16 v[168:171], a[104:107], a[88:91], v[168:171]\n\t"
    "buffer_load_dwordx4 v227, %3, s40 offen offset:0  lds\n\t"
    "v_mfma_f32_16x16x32_bf16 v[168:171], a[108:111], a[92:95], v[168:171]\n\t"
    "ds_read_b128 a[104:107], v224 offset:10240\n\t"
    "v_mul_f32 v24, v242, v24\n\t"
    "v_mfma_f32_16x16x32_bf16 v[172:175], a[112:115], a[80:83], v[204:207]\n\t"
    "ds_read_b128 a[108:111], v224 offset:11264\n\t"
    "v_mul_f32 v25, v242, v25\n\t"
    "v_mfma_f32_16x16x32_bf16 v[172:175], a[116:119], a[84:87], v[172:175]\n\t"
    "ds_read_b128 a[112:115], v224 offset:12288\n\t"
    "v_mul_f32 v26, v242, v26\n\t"
    "v_mfma_f32_16x16x32_bf16 v[172:175], a[120:123], a[88:91], v[172:175]\n\t"
    "ds_read_b128 a[116:119], v224 offset:13312\n\t"
    "v_mul_f32 v27, v242, v27\n\t"
    "v_mfma_f32_16x16x32_bf16 v[172:175], a[124:127], a[92:95], v[172:175]\n\t"
    "buffer_load_dwordx4 v248, %3, s40 offen offset:1024  lds\n\t"
    "s_add_u32 m0, %8, 8192\n\t"
    "s_add_u32 s40, %9, 40960\n\t"
    "buffer_load_dwordx4 v227, %3, s40 offen offset:0  lds\n\t"
    "ds_read_b128 a[120:123], v224 offset:14336\n\t"
    "v_cvt_pk_bf16_f32 v24, v24, v25\n\t"
    "v_cvt_pk_bf16_f32 v25, v26, v27\n\t"
    "ds_write_b64 v246, v[24:25] offset:0\n\t"
    "v_mul_f32 v56, v243, v56\n\t"
    "v_mul_f32 v57, v243, v57\n\t"
    "v_mul_f32 v58, v243, v58\n\t"
    "v_mul_f32 v59, v243, v59\n\t"
    "v_cvt_pk_bf16_f32 v56, v56, v57\n\t"
    "v_cvt_pk_bf16_f32 v57, v58, v59\n\t"
    "ds_write_b64 v246, v[56:57] offset:2048\n\t"
    "v_mul_f32 v28, v242, v28\n\t"
    "v_mul_f32 v29, v242, v29\n\t"
    "v_mul_f32 v30, v242, v30\n\t"
    "v_mul_f32 v31, v242, v31\n\t"
    "v_cvt_pk_bf16_f32 v28, v28, v29\n\t"
    "v_cvt_pk_bf16_f32 v29, v30, v31\n\t"
    "ds_write_b64 v247, v[28:29] offset:0\n\t"
    "v_mul_f32 v60, v243, v60\n\t"
    "v_mul_f32 v61, v243, v61\n\t"
    "v_mul_f32 v62, v243, v62\n\t"
    "v_mul_f32 v63, v243, v63\n\t"
    "v_cvt_pk_bf16_f32 v60, v60, v61\n\t"
    "v_cvt_pk_bf16_f32 v61, v62, v63\n\t"
    "ds_write_b64 v247, v[60:61] offset:2048\n\t"
    "v_exp_f32 v176, v176\n\t"
    "v_exp_f32 v177, v177\n\t"
    "v_exp_f32 v178, v178\n\t"
    "v_exp_f32 v179, v179\n\t"
    "v_exp_f32 v180, v180\n\t"
    "v_exp_f32 v181, v181\n\t"
    "v_exp_f32 v182, v182\n\t"
    "v_exp_f32 v183, v183\n\t"
    "v_cvt_pk_bf16_f32 v176, v176, v177\n\t"
    "v_cvt_pk_bf16_f32 v177, v178, v179\n\t"
    "v_cvt_pk_bf16_f32 v178, v180, v181\n\t"
    "v_cvt_pk_bf16_f32 v179, v182, v183\n\t"
    "v_exp_f32 v184, v184\n\t"
    "s_nop 0\n\t"
    "v_mfma_f32_16x16x32_bf16 v[64:67], a[128:131], v[176:179], v[64:67]\n\t"
    "ds_read_b128 a[124:127], v224 offset:15360\n\t"
    "v_mfma_f32_16x16x32_bf16 v[68:71], a[132:135], v[176:179], v[68:71]\n\t"
    "v_exp_f32 v185, v185\n\t"
    "v_mfma_f32_16x16x32_bf16 v[72:75], a[136:139], v[176:179], v[72:75]\n\t"
    "v_exp_f32 v186, v186\n\t"
    "v_mfma_f32_16x16x32_bf16 v[76:79], a[140:143], v[176:179], v[76:79]\n\t"
    "ds_read_b128 a[176:179], v235 offset:0\n\t"
    "v_cvt_pk_bf16_f32 v184, v184, v185\n\t"
    "v_mfma_f32_16x16x32_bf16 v[80:83], a[144:147], v[176:179], v[80:83]\n\t"
    "v_exp_f32 v187, v187\n\t"
    "v_mfma_f32_16x16x32_bf16 v[84:87], a[148:151], v[176:179], v[84:87]\n\t"
    "ds_read2_b64 a[180:183], v235 offset0:129 offset1:128\n\t"
    "v_cvt_pk_bf16_f32 v185, v186, v187\n\t"
    "v_mfma_f32_16x16x32_bf16 v[88:91], a[152:155], v[176:179], v[88:91]\n\t"
    "v_exp_f32 v188, v188\n\t"
    "v_mfma_f32_16x16x32_bf16 v[92:95], a[156:159], v[176:179], v[92:95]\n\t"
    "buffer_load_dwordx4 v248, %3, s40 offen offset:1024  lds\n\t"
    "ds_read_b128 a[184:187], v235 offset:2048\n\t"
    "ds_read2_b64 a[188:191], v245 offset0:129 offset1:128\n\t"
    "v_exp_f32 v189, v189\n\t"
    "v_exp_f32 v190, v190\n\t"
    "v_exp_f32 v191, v191\n\t"
    "v_cvt_pk_bf16_f32 v186, v188, v189\n\t"
    "v_cvt_pk_bf16_f32 v187, v190, v191\n\t"
    "v_exp_f32 v160, v160\n\t"
    "s_nop 0\n\t"
    "v_mfma_f32_16x16x32_bf16 v[96:99], a[128:131], v[184:187], v[96:99]\n\t"
    "ds_read_b64_tr_b16 a[128:129], v226 offset:16384\n\t"
    "v_mfma_f32_16x16x32_bf16 v[100:103], a[132:135], v[184:187], v[100:103]\n\t"
    "ds_read_b64_tr_b16 a[130:131], v226 offset:20480\n\t"
    "v_mfma_f32_16x16x32_bf16 v[104:107], a[136:139], v[184:187], v[104:107]\n\t"
    "ds_read_b64_tr_b16 a[132:133], v225 offset:16384\n\t"
    "v_mfma_f32_16x16x32_bf16 v[108:111], a[140:143], v[184:187], v[108:111]\n\t"
    "ds_read_b64_tr_b16 a[134:135], v225 offset:20480\n\t"
    "v_mfma_f32_16x16x32_bf16 v[112:115], a[144:147], v[184:187], v[112:115]\n\t"
    "ds_read_b64_tr_b16 a[136:137], v226 offset:16896\n\t"
    "v_mfma_f32_16x16x32_bf16 v[116:119], a[148:151], v[184:187], v[116:119]\n\t"
    "ds_read_b64_tr_b16 a[138:139], v226 offset:20992\n\t"
    "v_mfma_f32_16x16x32_bf16 v[120:123], a[152:155], v[184:187], v[120:123]\n\t"
    "ds_read_b64_tr_b16 a[140:141], v225 offset:16896\n\t"
    "v_mfma_f32_16x16x32_bf16 v[124:127], a[156:159], v[184:187], v[124:127]\n\t"
    "ds_read_b64_tr_b16 a[142:143], v225 offset:20992\n\t"
    "v_mfma_f32_16x16x32_bf16 v[216:219], v[252:255], v[176:179], v[216:219]\n\t"
    "ds_read_b64_tr_b16 a[144:145], v226 offset:17408\n\t"
    "v_mfma_f32_16x16x32_bf16 v[220:223], v[252:255], v[184:187], v[220:223]\n\t"
    "s_waitcnt lgkmcnt(11)\n\t"
    "ds_read_b64_tr_b16 a[146:147], v226 offset:21504\n\t"
    "v_mfma_f32_16x16x32_bf16 v[176:179], a[96:99], a[64:67], v[200:203]\n\t"
    "ds_read_b64_tr_b16 a[148:149], v225 offset:17408\n\t"
    "v_mfma_f32_16x16x32_bf16 v[176:179], a[100:103], a[68:71], v[176:179]\n\t"
    "ds_read_b64_tr_b16 a[150:151], v225 offset:21504\n\t"
    "v_mfma_f32_16x16x32_bf16 v[176:179], a[104:107], a[72:75], v[176:179]\n\t"
    "ds_read_b64_tr_b16 a[152:153], v226 offset:17920\n\t"
    "v_mfma_f32_16x16x32_bf16 v[176:179], a[108:111], a[76:79], v[176:179]\n\t"
    "ds_read_b64_tr_b16 a[154:155], v226 offset:22016\n\t"
    "v_mfma_f32_16x16x32_bf16 v[180:183], a[112:115], a[64:67], v[200:203]\n\t"
    "ds_read_b64_tr_b16 a[156:157], v225 offset:17920\n\t"
    "v_mfma_f32_16x16x32_bf16 v[180:183], a[116:119], a[68:71], v[180:183]\n\t"
    "ds_read_b64_tr_b16 a[158:159], v225 offset:22016\n\t"
    "v_mfma_f32_16x16x32_bf16 v[180:183], a[120:123], a[72:75], v[180:183]\n\t"
    "v_exp_f32 v161, v161\n\t"
    "v_mfma_f32_16x16x32_bf16 v[180:183], a[124:127], a[76:79], v[180:183]\n\t"
    "v_exp_f32 v162, v162\n\t"
    "v_mfma_f32_16x16x32_bf16 v[184:187], a[96:99], a[80:83], v[204:207]\n\t"
    "ds_read_b128 a[96:99], v224 offset:16384\n\t"
    "v_cvt_pk_bf16_f32 v160, v160, v161\n\t"
    "v_mfma_f32_16x16x32_bf16 v[184:187], a[100:103], a[84:87], v[184:187]\n\t"
    "ds_read_b128 a[100:103], v224 offset:17408\n\t"
    "v_mfma_f32_16x16x32_bf16 v[184:187], a[104:107], a[88:91], v[184:187]\n\t"
    "ds_read_b128 a[104:107], v224 offset:18432\n\t"
    "s_add_u32 s40, %11, 131072\n\t"
    "v_mfma_f32_16x16x32_bf16 v[184:187], a[108:111], a[92:95], v[184:187]\n\t"
    "ds_read_b128 a[108:111], v224 offset:19456\n\t"
    "v_mfma_f32_16x16x32_bf16 v[188:191], a[112:115], a[80:83], v[204:207]\n\t"
    "ds_read_b128 a[112:115], v224 offset:20480\n\t"
    "v_mfma_f32_16x16x32_bf16 v[188:191], a[116:119], a[84:87], v[188:191]\n\t"
    "ds_read_b128 a[116:119], v224 offset:21504\n\t"
    "v_mfma_f32_16x16x32_bf16 v[188:191], a[120:123], a[88:91], v[188:191]\n\t"
    "ds_read_b128 a[120:123], v224 offset:22528\n\t"
    "v_mfma_f32_16x16x32_bf16 v[188:191], a[124:127], a[92:95], v[188:191]\n\t"
    "s_waitcnt lgkmcnt(12)\n\t"
    "buffer_store_dwordx4 a[160:163], v236, %4, s40 offen offset:0 sc1\n\t"
    "ds_read_b128 a[124:127], v224 offset:23552\n\t"
    "v_exp_f32 v163, v163\n\t"
    "v_exp_f32 v164, v164\n\t"
    "v_exp_f32 v165, v165\n\t"
    "v_exp_f32 v166, v166\n\t"
    "v_exp_f32 v167, v167\n\t"
    "v_cvt_pk_bf16_f32 v161, v162, v163\n\t"
    "v_cvt_pk_bf16_f32 v162, v164, v165\n\t"
    "v_cvt_pk_bf16_f32 v163, v166, v167\n\t"
    "v_exp_f32 v168, v168\n\t"
    "s_nop 0\n\t"
    "v_mfma_f32_16x16x32_bf16 v[64:67], a[128:131], v[160:163], v[64:67]\n\t"
    "v_exp_f32 v169, v169\n\t"
    "v_mfma_f32_16x16x32_bf16 v[68:71], a[132:135], v[160:163], v[68:71]\n\t"
    "v_exp_f32 v170, v170\n\t"
    "v_mfma_f32_16x16x32_bf16 v[72:75], a[136:139], v[160:163], v[72:75]\n\t"
    "v_exp_f32 v171, v171\n\t"
    "v_mfma_f32_16x16x32_bf16 v[76:79], a[140:143], v[160:163], v[76:79]\n\t"
    "v_exp_f32 v172, v172\n\t"
    "v_mfma_f32_16x16x32_bf16 v[80:83], a[144:147], v[160:163], v[80:83]\n\t"
    "s_waitcnt lgkmcnt(5)\n\t"
    "v_exp_f32 v173, v173\n\t"
    "v_mfma_f32_16x16x32_bf16 v[84:87], a[148:151], v[160:163], v[84:87]\n\t"
    "v_exp_f32 v174, v174\n\t"
    "v_mfma_f32_16x16x32_bf16 v[88:91], a[152:155], v[160:163], v[88:91]\n\t"
    "v_exp_f32 v175, v175\n\t"
    "v_mfma_f32_16x16x32_bf16 v[92:95], a[156:159], v[160:163], v[92:95]\n\t"
    "buffer_store_dwordx4 a[164:167], v236, %4, s40 offen offset:2048 sc1\n\t"
    "v_cvt_pk_bf16_f32 v168, v168, v169\n\t"
    "v_cvt_pk_bf16_f32 v169, v170, v171\n\t"
    "v_cvt_pk_bf16_f32 v170, v172, v173\n\t"
    "v_cvt_pk_bf16_f32 v171, v174, v175\n\t"
    "v_exp_f32 v176, v176\n\t"
    "s_nop 0\n\t"
    "v_mfma_f32_16x16x32_bf16 v[96:99], a[128:131], v[168:171], v[96:99]\n\t"
    "ds_read_b64_tr_b16 a[128:129], v226 offset:24576\n\t"
    "v_mfma_f32_16x16x32_bf16 v[100:103], a[132:135], v[168:171], v[100:103]\n\t"
    "ds_read_b64_tr_b16 a[130:131], v226 offset:28672\n\t"
    "v_mfma_f32_16x16x32_bf16 v[104:107], a[136:139], v[168:171], v[104:107]\n\t"
    "ds_read_b64_tr_b16 a[132:133], v225 offset:24576\n\t"
    "s_add_u32 s40, %11, 135168\n\t"
    "v_mfma_f32_16x16x32_bf16 v[108:111], a[140:143], v[168:171], v[108:111]\n\t"
    "ds_read_b64_tr_b16 a[134:135], v225 offset:28672\n\t"
    "v_mfma_f32_16x16x32_bf16 v[112:115], a[144:147], v[168:171], v[112:115]\n\t"
    "ds_read_b64_tr_b16 a[136:137], v226 offset:25088\n\t"
    "v_mfma_f32_16x16x32_bf16 v[116:119], a[148:151], v[168:171], v[116:119]\n\t"
    "ds_read_b64_tr_b16 a[138:139], v226 offset:29184\n\t"
    "v_mfma_f32_16x16x32_bf16 v[120:123], a[152:155], v[168:171], v[120:123]\n\t"
    "ds_read_b64_tr_b16 a[140:141], v225 offset:25088\n\t"
    "v_mfma_f32_16x16x32_bf16 v[124:127], a[156:159], v[168:171], v[124:127]\n\t"
    "ds_read_b64_tr_b16 a[142:143], v225 offset:29184\n\t"
    "v_mfma_f32_16x16x32_bf16 v[216:219], v[252:255], v[160:163], v[216:219]\n\t"
    "ds_read_b64_tr_b16 a[144:145], v226 offset:25600\n\t"
    "v_mfma_f32_16x16x32_bf16 v[220:223], v[252:255], v[168:171], v[220:223]\n\t"
    "ds_read_b64_tr_b16 a[146:147], v226 offset:29696\n\t"
    "v_mfma_f32_16x16x32_bf16 v[160:163], a[96:99], a[64:67], v[200:203]\n\t"
    "ds_read_b64_tr_b16 a[148:149], v225 offset:25600\n\t"
    "v_mfma_f32_16x16x32_bf16 v[160:163], a[100:103], a[68:71], v[160:163]\n\t"
    "ds_read_b64_tr_b16 a[150:151], v225 offset:29696\n\t"
    "v_mfma_f32_16x16x32_bf16 v[160:163], a[104:107], a[72:75], v[160:163]\n\t"
    "s_waitcnt lgkmcnt(12)\n\t"
    "ds_read_b64_tr_b16 a[152:153], v226 offset:26112\n\t"
    "v_mfma_f32_16x16x32_bf16 v[160:163], a[108:111], a[76:79], v[160:163]\n\t"
    "ds_read_b64_tr_b16 a[154:155], v226 offset:30208\n\t"
    "v_mfma_f32_16x16x32_bf16 v[164:167], a[112:115], a[64:67], v[200:203]\n\t"
    "ds_read_b64_tr_b16 a[156:157], v225 offset:26112\n\t"
    "v_mfma_f32_16x16x32_bf16 v[164:167], a[116:119], a[68:71], v[164:167]\n\t"
    "ds_read_b64_tr_b16 a[158:159], v225 offset:30208\n\t"
    "v_mfma_f32_16x16x32_bf16 v[164:167], a[120:123], a[72:75], v[164:167]\n\t"
    "v_exp_f32 v177, v177\n\t"
    "v_mfma_f32_16x16x32_bf16 v[164:167], a[124:127], a[76:79], v[164:167]\n\t"
    "v_exp_f32 v178, v178\n\t"
    "v_mfma_f32_16x16x32_bf16 v[168:171], a[96:99], a[80:83], v[204:207]\n\t"
    "ds_read_b128 a[96:99], v224 offset:24576\n\t"
    "v_cvt_pk_bf16_f32 v176, v176, v177\n\t"
    "v_mfma_f32_16x16x32_bf16 v[168:171], a[100:103], a[84:87], v[168:171]\n\t"
    "ds_read_b128 a[100:103], v224 offset:25600\n\t"
    "v_mfma_f32_16x16x32_bf16 v[168:171], a[104:107], a[88:91], v[168:171]\n\t"
    "ds_read_b128 a[104:107], v224 offset:26624\n\t"
    "v_mfma_f32_16x16x32_bf16 v[168:171], a[108:111], a[92:95], v[168:171]\n\t"
    "ds_read_b128 a[108:111], v224 offset:27648\n\t"
    "v_mfma_f32_16x16x32_bf16 v[172:175], a[112:115], a[80:83], v[204:207]\n\t"
    "ds_read_b128 a[112:115], v224 offset:28672\n\t"
    "v_mfma_f32_16x16x32_bf16 v[172:175], a[116:119], a[84:87], v[172:175]\n\t"
    "ds_read_b128 a[116:119], v224 offset:29696\n\t"
    "v_mfma_f32_16x16x32_bf16 v[172:175], a[120:123], a[88:91], v[172:175]\n\t"
    "ds_read_b128 a[120:123], v224 offset:30720\n\t"
    "v_mfma_f32_16x16x32_bf16 v[172:175], a[124:127], a[92:95], v[172:175]\n\t"
    "s_waitcnt lgkmcnt(12)\n\t"
    "buffer_store_dwordx4 a[168:171], v236, %4, s40 offen offset:0 sc1\n\t"
    "buffer_store_dwordx4 a[172:175], v236, %4, s40 offen offset:2048 sc1\n\t"
    "ds_read_b128 a[124:127], v224 offset:31744\n\t"
    "v_exp_f32 v179, v179\n\t"
    "v_exp_f32 v180, v180\n\t"
    "v_exp_f32 v181, v181\n\t"
    "v_exp_f32 v182, v182\n\t"
    "v_exp_f32 v183, v183\n\t"
    "v_cvt_pk_bf16_f32 v177, v178, v179\n\t"
    "v_cvt_pk_bf16_f32 v178, v180, v181\n\t"
    "v_cvt_pk_bf16_f32 v179, v182, v183\n\t"
    "v_exp_f32 v184, v184\n\t"
    "s_nop 0\n\t"
    "v_mfma_f32_16x16x32_bf16 v[64:67], a[128:131], v[176:179], v[64:67]\n\t"
    "s_waitcnt lgkmcnt(7)\n\t"
    "s_waitcnt vmcnt(4)\n\t"
    "s_barrier\n\t"
    "v_exp_f32 v185, v185\n\t"
    "v_mfma_f32_16x16x32_bf16 v[68:71], a[132:135], v[176:179], v[68:71]\n\t"
    "v_exp_f32 v186, v186\n\t"
    "v_mfma_f32_16x16x32_bf16 v[72:75], a[136:139], v[176:179], v[72:75]\n\t"
    "v_exp_f32 v187, v187\n\t"
    "v_mfma_f32_16x16x32_bf16 v[76:79], a[140:143], v[176:179], v[76:79]\n\t"
    "v_exp_f32 v188, v188\n\t"
    "v_mfma_f32_16x16x32_bf16 v[80:83], a[144:147], v[176:179], v[80:83]\n\t"
    "v_exp_f32 v189, v189\n\t"
    "v_mfma_f32_16x16x32_bf16 v[84:87], a[148:151], v[176:179], v[84:87]\n\t"
    "v_exp_f32 v190, v190\n\t"
    "v_mfma_f32_16x16x32_bf16 v[88:91], a[152:155], v[176:179], v[88:91]\n\t"
    "v_exp_f32 v191, v191\n\t"
    "v_mfma_f32_16x16x32_bf16 v[92:95], a[156:159], v[176:179], v[92:95]\n\t"
    "v_cvt_pk_bf16_f32 v184, v184, v185\n\t"
    "v_cvt_pk_bf16_f32 v185, v186, v187\n\t"
    "v_cvt_pk_bf16_f32 v186, v188, v189\n\t"
    "v_cvt_pk_bf16_f32 v187, v190, v191\n\t"
    "v_exp_f32 v160, v160\n\t"
    "s_nop 0\n\t"
    "v_mfma_f32_16x16x32_bf16 v[96:99], a[128:131], v[184:187], v[96:99]\n\t"
    "ds_read_b64_tr_b16 a[128:129], v226 offset:0\n\t"
    "v_mfma_f32_16x16x32_bf16 v[100:103], a[132:135], v[184:187], v[100:103]\n\t"
    "ds_read_b64_tr_b16 a[130:131], v226 offset:4096\n\t"
    "v_mfma_f32_16x16x32_bf16 v[104:107], a[136:139], v[184:187], v[104:107]\n\t"
    "ds_read_b64_tr_b16 a[132:133], v225 offset:0\n\t"
    "v_mfma_f32_16x16x32_bf16 v[108:111], a[140:143], v[184:187], v[108:111]\n\t"
    "ds_read_b64_tr_b16 a[134:135], v225 offset:4096\n\t"
    "v_mfma_f32_16x16x32_bf16 v[112:115], a[144:147], v[184:187], v[112:115]\n\t"
    "ds_read_b64_tr_b16 a[136:137], v226 offset:512\n\t"
    "v_mfma_f32_16x16x32_bf16 v[116:119], a[148:151], v[184:187], v[116:119]\n\t"
    "ds_read_b64_tr_b16 a[138:139], v226 offset:4608\n\t"
    "v_mfma_f32_16x16x32_bf16 v[120:123], a[152:155], v[184:187], v[120:123]\n\t"
    "ds_read_b64_tr_b16 a[140:141], v225 offset:512\n\t"
    "v_mfma_f32_16x16x32_bf16 v[124:127], a[156:159], v[184:187], v[124:127]\n\t"
    "ds_read_b64_tr_b16 a[142:143], v225 offset:4608\n\t"
    "v_mfma_f32_16x16x32_bf16 v[216:219], v[252:255], v[176:179], v[216:219]\n\t"
    "ds_read_b64_tr_b16 a[144:145], v226 offset:1024\n\t"
    "v_mfma_f32_16x16x32_bf16 v[220:223], v[252:255], v[184:187], v[220:223]\n\t"
    "ds_read_b64_tr_b16 a[146:147], v226 offset:5120\n\t"
    "v_mfma_f32_16x16x32_bf16 v[176:179], a[96:99], a[64:67], v[200:203]\n\t"
    "s_waitcnt lgkmcnt(10)\n\t"
    "ds_read_b64_tr_b16 a[148:149], v225 offset:1024\n\t"
    "v_mfma_f32_16x16x32_bf16 v[176:179], a[100:103], a[68:71], v[176:179]\n\t"
    "ds_read_b64_tr_b16 a[150:151], v225 offset:5120\n\t"
    "v_mfma_f32_16x16x32_bf16 v[176:179], a[104:107], a[72:75], v[176:179]\n\t"
    "ds_read_b64_tr_b16 a[152:153], v226 offset:1536\n\t"
    "v_mfma_f32_16x16x32_bf16 v[176:179], a[108:111], a[76:79], v[176:179]\n\t"
    "ds_read_b64_tr_b16 a[154:155], v226 offset:5632\n\t"
    "v_mfma_f32_16x16x32_bf16 v[180:183], a[112:115], a[64:67], v[200:203]\n\t"
    "ds_read_b64_tr_b16 a[156:157], v225 offset:1536\n\t"
    "v_mfma_f32_16x16x32_bf16 v[180:183], a[116:119], a[68:71], v[180:183]\n\t"
    "ds_read_b64_tr_b16 a[158:159], v225 offset:5632\n\t"
    "v_mfma_f32_16x16x32_bf16 v[180:183], a[120:123], a[72:75], v[180:183]\n\t"
    "v_exp_f32 v161, v161\n\t"
    "v_mfma_f32_16x16x32_bf16 v[180:183], a[124:127], a[76:79], v[180:183]\n\t"
    "v_exp_f32 v162, v162\n\t"
    "v_mfma_f32_16x16x32_bf16 v[184:187], a[96:99], a[80:83], v[204:207]\n\t"
    "v_exp_f32 v163, v163\n\t"
    "v_mfma_f32_16x16x32_bf16 v[184:187], a[100:103], a[84:87], v[184:187]\n\t"
    "v_exp_f32 v164, v164\n\t"
    "v_mfma_f32_16x16x32_bf16 v[184:187], a[104:107], a[88:91], v[184:187]\n\t"
    "s_add_u32 s40, %11, 131200\n\t"
    "v_exp_f32 v165, v165\n\t"
    "v_mfma_f32_16x16x32_bf16 v[184:187], a[108:111], a[92:95], v[184:187]\n\t"
    "v_exp_f32 v166, v166\n\t"
    "v_mfma_f32_16x16x32_bf16 v[188:191], a[112:115], a[80:83], v[204:207]\n\t"
    "v_exp_f32 v167, v167\n\t"
    "v_mfma_f32_16x16x32_bf16 v[188:191], a[116:119], a[84:87], v[188:191]\n\t"
    "v_cvt_pk_bf16_f32 v160, v160, v161\n\t"
    "v_cvt_pk_bf16_f32 v161, v162, v163\n\t"
    "v_mfma_f32_16x16x32_bf16 v[188:191], a[120:123], a[88:91], v[188:191]\n\t"
    "v_cvt_pk_bf16_f32 v162, v164, v165\n\t"
    "v_cvt_pk_bf16_f32 v163, v166, v167\n\t"
    "v_mfma_f32_16x16x32_bf16 v[188:191], a[124:127], a[92:95], v[188:191]\n\t"
    "s_waitcnt lgkmcnt(5)\n\t"
    "v_exp_f32 v168, v168\n\t"
    "v_mfma_f32_16x16x32_bf16 v[64:67], a[128:131], v[160:163], v[64:67]\n\t"
    "v_exp_f32 v169, v169\n\t"
    "v_mfma_f32_16x16x32_bf16 v[68:71], a[132:135], v[160:163], v[68:71]\n\t"
    "v_exp_f32 v170, v170\n\t"
    "v_mfma_f32_16x16x32_bf16 v[72:75], a[136:139], v[160:163], v[72:75]\n\t"
    "v_exp_f32 v171, v171\n\t"
    "v_mfma_f32_16x16x32_bf16 v[76:79], a[140:143], v[160:163], v[76:79]\n\t"
    "v_exp_f32 v172, v172\n\t"
    "v_mfma_f32_16x16x32_bf16 v[80:83], a[144:147], v[160:163], v[80:83]\n\t"
    "s_waitcnt lgkmcnt(0)\n\t"
    "v_exp_f32 v173, v173\n\t"
    "v_mfma_f32_16x16x32_bf16 v[84:87], a[148:151], v[160:163], v[84:87]\n\t"
    "v_exp_f32 v174, v174\n\t"
    "v_mfma_f32_16x16x32_bf16 v[88:91], a[152:155], v[160:163], v[88:91]\n\t"
    "v_exp_f32 v175, v175\n\t"
    "v_mfma_f32_16x16x32_bf16 v[92:95], a[156:159], v[160:163], v[92:95]\n\t"
    "buffer_store_dwordx4 a[176:179], v236, %4, s40 offen offset:0 sc1\n\t"
    "buffer_store_dwordx4 a[180:183], v236, %4, s40 offen offset:2048 sc1\n\t"
    "v_cvt_pk_bf16_f32 v168, v168, v169\n\t"
    "v_cvt_pk_bf16_f32 v169, v170, v171\n\t"
    "v_cvt_pk_bf16_f32 v170, v172, v173\n\t"
    "v_cvt_pk_bf16_f32 v171, v174, v175\n\t"
    "v_exp_f32 v176, v176\n\t"
    "s_nop 0\n\t"
    "v_mfma_f32_16x16x32_bf16 v[96:99], a[128:131], v[168:171], v[96:99]\n\t"
    "ds_read_b64_tr_b16 a[128:129], v226 offset:8192\n\t"
    "ds_read_b64_tr_b16 a[130:131], v226 offset:12288\n\t"
    "v_mfma_f32_16x16x32_bf16 v[100:103], a[132:135], v[168:171], v[100:103]\n\t"
    "ds_read_b64_tr_b16 a[132:133], v225 offset:8192\n\t"
    "ds_read_b64_tr_b16 a[134:135], v225 offset:12288\n\t"
    "v_mfma_f32_16x16x32_bf16 v[104:107], a[136:139], v[168:171], v[104:107]\n\t"
    "ds_read_b64_tr_b16 a[136:137], v226 offset:8704\n\t"
    "s_add_u32 s40, %11, 135296\n\t"
    "ds_read_b64_tr_b16 a[138:139], v226 offset:12800\n\t"
    "v_mfma_f32_16x16x32_bf16 v[108:111], a[140:143], v[168:171], v[108:111]\n\t"
    "ds_read_b64_tr_b16 a[140:141], v225 offset:8704\n\t"
    "ds_read_b64_tr_b16 a[142:143], v225 offset:12800\n\t"
    "v_mfma_f32_16x16x32_bf16 v[112:115], a[144:147], v[168:171], v[112:115]\n\t"
    "ds_read_b64_tr_b16 a[144:145], v226 offset:9216\n\t"
    "ds_read_b64_tr_b16 a[146:147], v226 offset:13312\n\t"
    "v_mfma_f32_16x16x32_bf16 v[116:119], a[148:151], v[168:171], v[116:119]\n\t"
    "ds_read_b64_tr_b16 a[148:149], v225 offset:9216\n\t"
    "ds_read_b64_tr_b16 a[150:151], v225 offset:13312\n\t"
    "v_mfma_f32_16x16x32_bf16 v[120:123], a[152:155], v[168:171], v[120:123]\n\t"
    "ds_read_b64_tr_b16 a[152:153], v226 offset:9728\n\t"
    "ds_read_b64_tr_b16 a[154:155], v226 offset:13824\n\t"
    "v_mfma_f32_16x16x32_bf16 v[124:127], a[156:159], v[168:171], v[124:127]\n\t"
    "ds_read_b64_tr_b16 a[156:157], v225 offset:9728\n\t"
    "ds_read_b64_tr_b16 a[158:159], v225 offset:13824\n\t"
    "v_mfma_f32_16x16x32_bf16 v[216:219], v[252:255], v[160:163], v[216:219]\n\t"
    "v_exp_f32 v177, v177\n\t"
    "v_mfma_f32_16x16x32_bf16 v[220:223], v[252:255], v[168:171], v[220:223]\n\t"
    "buffer_store_dwordx4 a[184:187], v236, %4, s40 offen offset:0 sc1\n\t"
    "v_exp_f32 v178, v178\n\t"
    "v_exp_f32 v179, v179\n\t"
    "v_exp_f32 v180, v180\n\t"
    "v_exp_f32 v181, v181\n\t"
    "v_exp_f32 v182, v182\n\t"
    "v_exp_f32 v183, v183\n\t"
    "v_cvt_pk_bf16_f32 v176, v176, v177\n\t"
    "v_cvt_pk_bf16_f32 v177, v178, v179\n\t"
    "v_exp_f32 v184, v184\n\t"
    "v_exp_f32 v185, v185\n\t"
    "v_cvt_pk_bf16_f32 v178, v180, v181\n\t"
    "v_cvt_pk_bf16_f32 v179, v182, v183\n\t"
    "v_exp_f32 v186, v186\n\t"
    "s_nop 0\n\t"
    "v_mfma_f32_16x16x32_bf16 v[216:219], v[252:255], v[176:179], v[216:219]\n\t"
    "buffer_store_dwordx4 a[188:191], v236, %4, s40 offen offset:2048 sc1\n\t"
    "v_exp_f32 v187, v187\n\t"
    "v_exp_f32 v188, v188\n\t"
    "v_exp_f32 v189, v189\n\t"
    "v_exp_f32 v190, v190\n\t"
    "v_exp_f32 v191, v191\n\t"
    "v_cvt_pk_bf16_f32 v184, v184, v185\n\t"
    "v_cvt_pk_bf16_f32 v185, v186, v187\n\t"
    "v_cvt_pk_bf16_f32 v186, v188, v189\n\t"
    "v_cvt_pk_bf16_f32 v187, v190, v191\n\t"
    "s_nop 1\n\t"
    "v_mfma_f32_16x16x32_bf16 v[220:223], v[252:255], v[184:187], v[220:223]\n\t"
    "s_waitcnt lgkmcnt(0)\n\t"
    "v_mfma_f32_16x16x32_bf16 v[64:67], a[128:131], v[176:179], v[64:67]\n\t"
    "v_mfma_f32_16x16x32_bf16 v[68:71], a[132:135], v[176:179], v[68:71]\n\t"
    "v_mfma_f32_16x16x32_bf16 v[72:75], a[136:139], v[176:179], v[72:75]\n\t"
    "v_mfma_f32_16x16x32_bf16 v[76:79], a[140:143], v[176:179], v[76:79]\n\t"
    "v_mfma_f32_16x16x32_bf16 v[80:83], a[144:147], v[176:179], v[80:83]\n\t"
    "v_mfma_f32_16x16x32_bf16 v[84:87], a[148:151], v[176:179], v[84:87]\n\t"
    "v_mfma_f32_16x16x32_bf16 v[88:91], a[152:155], v[176:179], v[88:91]\n\t"
    "v_mfma_f32_16x16x32_bf16 v[92:95], a[156:159], v[176:179], v[92:95]\n\t"
    "v_mfma_f32_16x16x32_bf16 v[96:99], a[128:131], v[184:187], v[96:99]\n\t"
    "v_mfma_f32_16x16x32_bf16 v[100:103], a[132:135], v[184:187], v[100:103]\n\t"
    "v_mfma_f32_16x16x32_bf16 v[104:107], a[136:139], v[184:187], v[104:107]\n\t"
    "v_mfma_f32_16x16x32_bf16 v[108:111], a[140:143], v[184:187], v[108:111]\n\t"
    "v_mfma_f32_16x16x32_bf16 v[112:115], a[144:147], v[184:187], v[112:115]\n\t"
    "v_mfma_f32_16x16x32_bf16 v[116:119], a[148:151], v[184:187], v[116:119]\n\t"
    "v_mfma_f32_16x16x32_bf16 v[120:123], a[152:155], v[184:187], v[120:123]\n\t"
    "v_mfma_f32_16x16x32_bf16 v[124:127], a[156:159], v[184:187], v[124:127]\n\t"
    "v_cmp_ngt_f32 vcc, s44, v232\n\t"
    "s_cmp_lg_u64 vcc, 0\n\t"
    "s_cselect_b32 s45, 8, 0\n\t"
    "s_or_b32 s41, s41, s45\n\t"
    "v_add_f32 v232, v229, v233\n\t"
    "v_mov_b32 v229, v216\n\t"
    "v_rcp_f32 v242, v229\n\t"
    "v_mov_b32 v233, v220\n\t"
    "v_rcp_f32 v243, v233\n\t"
    "v_mul_f32 v64, v242, v64\n\t"
    "v_mul_f32 v65, v242, v65\n\t"
    "v_mul_f32 v66, v242, v66\n\t"
    "v_mul_f32 v67, v242, v67\n\t"
    "v_cvt_pk_bf16_f32 v64, v64, v65\n\t"
    "v_mul_f32 v96, v243, v96\n\t"
    "v_mul_f32 v97, v243, v97\n\t"
    "v_cvt_pk_bf16_f32 v65, v66, v67\n\t"
    "v_mul_f32 v98, v243, v98\n\t"
    "v_mul_f32 v99, v243, v99\n\t"
    "v_cvt_pk_bf16_f32 v96, v96, v97\n\t"
    "v_mul_f32 v68, v242, v68\n\t"
    "v_mul_f32 v69, v242, v69\n\t"
    "v_cvt_pk_bf16_f32 v97, v98, v99\n\t"
    "v_mul_f32 v70, v242, v70\n\t"
    "v_mul_f32 v71, v242, v71\n\t"
    "v_cvt_pk_bf16_f32 v68, v68, v69\n\t"
    "v_mul_f32 v100, v243, v100\n\t"
    "v_mul_f32 v101, v243, v101\n\t"
    "v_cvt_pk_bf16_f32 v69, v70, v71\n\t"
    "v_mul_f32 v102, v243, v102\n\t"
    "v_mul_f32 v103, v243, v103\n\t"
    "v_cvt_pk_bf16_f32 v100, v100, v101\n\t"
    "v_mul_f32 v72, v242, v72\n\t"
    "v_mul_f32 v73, v242, v73\n\t"
    "v_cvt_pk_bf16_f32 v101, v102, v103\n\t"
    "v_mul_f32 v74, v242, v74\n\t"
    "v_mul_f32 v75, v242, v75\n\t"
    "v_cvt_pk_bf16_f32 v72, v72, v73\n\t"
    "v_mul_f32 v104, v243, v104\n\t"
    "v_mul_f32 v105, v243, v105\n\t"
    "v_cvt_pk_bf16_f32 v73, v74, v75\n\t"
    "v_mul_f32 v106, v243, v106\n\t"
    "v_mul_f32 v107, v243, v107\n\t"
    "v_cvt_pk_bf16_f32 v104, v104, v105\n\t"
    "v_mul_f32 v76, v242, v76\n\t"
    "v_mul_f32 v77, v242, v77\n\t"
    "v_cvt_pk_bf16_f32 v105, v106, v107\n\t"
    "v_mul_f32 v78, v242, v78\n\t"
    "v_mul_f32 v79, v242, v79\n\t"
    "v_cvt_pk_bf16_f32 v76, v76, v77\n\t"
    "v_mul_f32 v108, v243, v108\n\t"
    "v_mul_f32 v109, v243, v109\n\t"
    "v_cvt_pk_bf16_f32 v77, v78, v79\n\t"
    "v_mul_f32 v110, v243, v110\n\t"
    "v_mul_f32 v111, v243, v111\n\t"
    "v_cvt_pk_bf16_f32 v108, v108, v109\n\t"
    "v_cvt_pk_bf16_f32 v109, v110, v111\n\t"
    "v_mul_f32 v80, v242, v80\n\t"
    "v_mul_f32 v81, v242, v81\n\t"
    "v_mul_f32 v82, v242, v82\n\t"
    "v_mul_f32 v83, v242, v83\n\t"
    "v_cvt_pk_bf16_f32 v80, v80, v81\n\t"
    "v_mul_f32 v112, v243, v112\n\t"
    "v_mul_f32 v113, v243, v113\n\t"
    "v_cvt_pk_bf16_f32 v81, v82, v83\n\t"
    "v_mul_f32 v114, v243, v114\n\t"
    "v_mul_f32 v115, v243, v115\n\t"
    "v_cvt_pk_bf16_f32 v112, v112, v113\n\t"
    "v_mul_f32 v84, v242, v84\n\t"
    "v_mul_f32 v85, v242, v85\n\t"
    "v_cvt_pk_bf16_f32 v113, v114, v115\n\t"
    "v_mul_f32 v86, v242, v86\n\t"
    "v_mul_f32 v87, v242, v87\n\t"
    "v_cvt_pk_bf16_f32 v84, v84, v85\n\t"
    "v_mul_f32 v116, v243, v116\n\t"
    "v_mul_f32 v117, v243, v117\n\t"
    "v_cvt_pk_bf16_f32 v85, v86, v87\n\t"
    "v_mul_f32 v118, v243, v118\n\t"
    "v_mul_f32 v119, v243, v119\n\t"
    "v_cvt_pk_bf16_f32 v116, v116, v117\n\t"
    "v_mul_f32 v88, v242, v88\n\t"
    "v_mul_f32 v89, v242, v89\n\t"
    "v_cvt_pk_bf16_f32 v117, v118, v119\n\t"
    "v_mul_f32 v90, v242, v90\n\t"
    "v_mul_f32 v91, v242, v91\n\t"
    "v_cvt_pk_bf16_f32 v88, v88, v89\n\t"
    "v_mul_f32 v120, v243, v120\n\t"
    "v_mul_f32 v121, v243, v121\n\t"
    "v_cvt_pk_bf16_f32 v89, v90, v91\n\t"
    "v_mul_f32 v122, v243, v122\n\t"
    "v_mul_f32 v123, v243, v123\n\t"
    "v_cvt_pk_bf16_f32 v120, v120, v121\n\t"
    "v_mul_f32 v92, v242, v92\n\t"
    "v_mul_f32 v93, v242, v93\n\t"
    "v_cvt_pk_bf16_f32 v121, v122, v123\n\t"
    "v_mul_f32 v94, v242, v94\n\t"
    "v_mul_f32 v95, v242, v95\n\t"
    "v_cvt_pk_bf16_f32 v92, v92, v93\n\t"
    "v_mul_f32 v124, v243, v124\n\t"
    "v_mul_f32 v125, v243, v125\n\t"
    "v_cvt_pk_bf16_f32 v93, v94, v95\n\t"
    "v_mul_f32 v126, v243, v126\n\t"
    "v_mul_f32 v127, v243, v127\n\t"
    "v_cvt_pk_bf16_f32 v124, v124, v125\n\t"
    "v_cvt_pk_bf16_f32 v125, v126, v127\n\t"
    "v_cmp_ngt_f32 vcc, s44, v232\n\t"
    "s_cmp_lg_u64 vcc, 0\n\t"
    "s_cselect_b32 s45, 16, 0\n\t"
    "s_or_b32 s41, s41, s45\n\t"
    "v_add_f32 v232, v229, v233\n\t"
    "v_cmp_ngt_f32 vcc, s44, v232\n\t"
    "s_cmp_lg_u64 vcc, 0\n\t"
    "s_cselect_b32 s45, 32, 0\n\t"
    "s_or_b32 s41, s41, s45\n\t"
    "ds_write_b64 v234, v[64:65] offset:0\n\t"
    "ds_write_b64 v234, v[96:97] offset:2048\n\t"
    "ds_write_b64 v237, v[68:69] offset:0\n\t"
    "ds_write_b64 v237, v[100:101] offset:2048\n\t"
    "ds_write_b64 v246, v[72:73] offset:0\n\t"
    "ds_write_b64 v246, v[104:105] offset:2048\n\t"
    "ds_write_b64 v247, v[76:77] offset:0\n\t"
    "ds_write_b64 v247, v[108:109] offset:2048\n\t"
    "ds_read_b128 a[160:163], v235 offset:0\n\t"
    "ds_read2_b64 a[164:167], v235 offset0:129 offset1:128\n\t"
    "ds_read_b128 a[168:171], v235 offset:2048\n\t"
    "ds_read2_b64 a[172:175], v245 offset0:129 offset1:128\n\t"
    "ds_write_b64 v234, v[80:81] offset:0\n\t"
    "ds_write_b64 v234, v[112:113] offset:2048\n\t"
    "ds_write_b64 v237, v[84:85] offset:0\n\t"
    "ds_write_b64 v237, v[116:117] offset:2048\n\t"
    "ds_write_b64 v246, v[88:89] offset:0\n\t"
    "ds_write_b64 v246, v[120:121] offset:2048\n\t"
    "ds_write_b64 v247, v[92:93] offset:0\n\t"
    "ds_write_b64 v247, v[124:125] offset:2048\n\t"
    "ds_read_b128 a[176:179], v235 offset:0\n\t"
    "ds_read2_b64 a[180:183], v235 offset0:129 offset1:128\n\t"
    "ds_read_b128 a[184:187], v235 offset:2048\n\t"
    "ds_read2_b64 a[188:191], v245 offset0:129 offset1:128\n\t"
    "s_waitcnt lgkmcnt(15)\n\t"
    "s_add_u32 s40, %11, 139264\n\t"
    "buffer_store_dwordx4 a[160:163], v236, %4, s40 offen offset:0 sc1\n\t"
    "s_waitcnt lgkmcnt(14)\n\t"
    "buffer_store_dwordx4 a[164:167], v236, %4, s40 offen offset:2048 sc1\n\t"
    "s_waitcnt lgkmcnt(13)\n\t"
    "s_add_u32 s40, %11, 143360\n\t"
    "buffer_store_dwordx4 a[168:171], v236, %4, s40 offen offset:0 sc1\n\t"
    "s_waitcnt lgkmcnt(11)\n\t"
    "buffer_store_dwordx4 a[172:175], v236, %4, s40 offen offset:2048 sc1\n\t"
    "s_waitcnt lgkmcnt(3)\n\t"
    "s_add_u32 s40, %11, 139392\n\t"
    "buffer_store_dwordx4 a[176:179], v236, %4, s40 offen offset:0 sc1\n\t"
    "s_waitcnt lgkmcnt(2)\n\t"
    "buffer_store_dwordx4 a[180:183], v236, %4, s40 offen offset:2048 sc1\n\t"
    "s_waitcnt lgkmcnt(1)\n\t"
    "s_add_u32 s40, %11, 143488\n\t"
    "buffer_store_dwordx4 a[184:187], v236, %4, s40 offen offset:0 sc1\n\t"
    "s_waitcnt lgkmcnt(0)\n\t"
    "buffer_store_dwordx4 a[188:191], v236, %4, s40 offen offset:2048 sc1\n\t"
    "L_end_%=:\n\t"
    "s_waitcnt vmcnt(0) lgkmcnt(0)\n\t"
    "s_mov_b32 %0, s41\n\t"
    : "=&s"(flags)
    : "s"(srdQ), "s"(srdK), "s"(srdV), "s"(srdO), "s"(sLds), "s"(sKl), "s"(sKg), "s"(sVl), "s"(sVg), "s"(sSt), "s"(sQg), "s"(sK0a), "s"(sK0b), "s"(sPf), "s"(sPf0)
    : "memory", "vcc", "scc", "v0","v1","v2","v3","v4","v5","v6","v7","v8","v9","v10","v11","v12","v13","v14","v15","v16","v17","v18","v19","v20","v21","v22","v23","v24","v25","v26","v27","v28","v29","v30","v31","v32","v33","v34","v35","v36","v37","v38","v39","v40","v41","v42","v43","v44","v45","v46","v47","v48","v49","v50","v51","v52","v53","v54","v55","v56","v57","v58","v59","v60","v61","v62","v63","v64","v65","v66","v67","v68","v69","v70","v71","v72","v73","v74","v75","v76","v77","v78","v79","v80","v81","v82","v83","v84","v85","v86","v87","v88","v89","v90","v91","v92","v93","v94","v95","v96","v97","v98","v99","v100","v101","v102","v103","v104","v105","v106","v107","v108","v109","v110","v111","v112","v113","v114","v115","v116","v117","v118","v119","v120","v121","v122","v123","v124","v125","v126","v127","v128","v129","v130","v131","v132","v133","v134","v135","v136","v137","v138","v139","v140","v141","v142","v143","v144","v145","v146","v147","v148","v149","v150","v151","v152","v153","v154","v155","v156","v157","v158","v159","v160","v161","v162","v163","v164","v165","v166","v167","v168","v169","v170","v171","v172","v173","v174","v175","v176","v177","v178","v179","v180","v181","v182","v183","v184","v185","v186","v187","v188","v189","v190","v191","v192","v193","v194","v195","v196","v197","v198","v199","v200","v201","v202","v203","v204","v205","v206","v207","v208","v209","v210","v211","v212","v213","v214","v215","v216","v217","v218","v219","v220","v221","v222","v223","v224","v225","v226","v227","v228","v229","v230","v231","v232","v233","v234","v235","v236","v237","v238","v239","v240","v241","v242","v243","v244","v245","v246","v247","v248","v249","v250","v251","v252","v253","v254","v255","a0","a1","a2","a3","a4","a5","a6","a7","a8","a9","a10","a11","a12","a13","a14","a15","a16","a17","a18","a19","a20","a21","a22","a23","a24","a25","a26","a27","a28","a29","a30","a31","a32","a33","a34","a35","a36","a37","a38","a39","a40","a41","a42","a43","a44","a45","a46","a47","a48","a49","a50","a51","a52","a53","a54","a55","a56","a57","a58","a59","a60","a61","a62","a63","a64","a65","a66","a67","a68","a69","a70","a71","a72","a73","a74","a75","a76","a77","a78","a79","a80","a81","a82","a83","a84","a85","a86","a87","a88","a89","a90","a91","a92","a93","a94","a95","a96","a97","a98","a99","a100","a101","a102","a103","a104","a105","a106","a107","a108","a109","a110","a111","a112","a113","a114","a115","a116","a117","a118","a119","a120","a121","a122","a123","a124","a125","a126","a127","a128","a129","a130","a131","a132","a133","a134","a135","a136","a137","a138","a139","a140","a141","a142","a143","a144","a145","a146","a147","a148","a149","a150","a151","a152","a153","a154","a155","a156","a157","a158","a159","a160","a161","a162","a163","a164","a165","a166","a167","a168","a169","a170","a171","a172","a173","a174","a175","a176","a177","a178","a179","a180","a181","a182","a183","a184","a185","a186","a187","a188","a189","a190","a191","a192","a193","a194","a195","a196","a197","a198","a199","a200","a201","a202","a203","a204","a205","a206","a207","a208","a209","a210","a211","a212","a213","a214","a215","a216","a217","a218","a219","a220","a221","a222","a223","a224","a225","a226","a227","a228","a229","a230","a231","a232","a233","a234","a235","a236","a237","a238","a239","a240","a241","a242","a243","a244","a245","a246","a247","a248","a249","a250","a251","a252","a253","a254","a255","s40","s41","s42","s43","s44","s45","s46","s47");
  if (flags) {
    const int lane = __builtin_amdgcn_mbcnt_hi(~0u, __builtin_amdgcn_mbcnt_lo(~0u, 0u));
    for (int e = 0; e < 6; ++e) {
      if ((flags >> e) & 1) {
        const size_t r0 = qbase + (size_t)((e >> 1) * 256 + wid * 64 + (e & 1) * 32) * 256;
        safe_rows((const unsigned short*)(q + r0), (const unsigned short*)(k + kbase), (const unsigned short*)(v + kbase), (unsigned short*)(o + r0), lane);
      }
    }
  }
}

extern "C" void kernel_launch(void* const* d_in, const int* in_sizes, int n_in, void* d_out, int out_size, void* d_ws, size_t ws_size, hipStream_t stream) {
  (void)hipFuncSetAttribute((const void*)gqa_attn, hipFuncAttributeMaxDynamicSharedMemorySize, 163840);
  hipLaunchKernelGGL(gqa_attn, dim3(256), dim3(256), 163840, stream, (const char*)d_in[0], (const char*)d_in[1], (const char*)d_in[2], (char*)d_out);
}
```
